```python
import torch

def _softmax(x, axis=-1):
    return torch.softmax(x, dim=axis)
def _gelu(x):
    return torch.nn.functional.gelu(x, approximate="tanh")
def _silu(x):
    return torch.nn.functional.silu(x)
def _sigmoid(x):
    return torch.sigmoid(x)
def _layernorm(x, axis=-1, eps=1e-5):
    m = x.mean(dim=axis, keepdim=True)
    v = x.var(dim=axis, keepdim=True, unbiased=False)
    return (x - m) / torch.sqrt(v + eps)
def _scatter_add(tgt, idx, src, axis=0):
    return tgt.clone().index_add_(axis, idx, src)
def _segment_sum(src, seg, n):
    out = torch.zeros((n,) + src.shape[1:], dtype=src.dtype)
    return out.index_add_(0, seg, src)
def _pad(x, pw):
    flat = tuple(v for pair in reversed(pw) for v in pair)
    return torch.nn.functional.pad(x, flat)

kernel_name = 'standalone__normalfloat_lut_af4__prompt'

def setup_inputs():

    _w = (lambda a: (lambda t: t.to(torch.bfloat16) if t.is_floating_point() else t)(torch.as_tensor(a)))
    B, S, D, F = 1, 2048, 1024, 3584
    G = -1 if -1 > 0 else D
    D = (D // G) * G
    ng = D // G
    nf4 = torch.tensor([
        -1.0, -0.6961928009986877, -0.5250730514526367, -0.39491748809814453,
        -0.28444138169288635, -0.18477343022823334, -0.09105003625154495, 0.0,
        0.07958029955625534, 0.16093020141124725, 0.24611230194568634,
        0.33791524171829224, 0.44070982933044434, 0.5626170039176941,
        0.7229568362236023, 1.0,
    ], dtype=float)
    qw = torch.randint(0, 16, (D, F)).to(torch.int64)
    return {
        "x": _w((lambda *s: torch.randn(*s, dtype=torch.bfloat16) * 0.05)(B, S, D)),
        "qw": _w(qw),
        "lut": _w(nf4),
        "scales": _w(torch.abs((lambda *s: torch.randn(*s, dtype=torch.bfloat16) * 0.05)(ng, F)).to(float) + 0.01),
    }

def reference(x, qw, lut, scales):
    B, S, D = x.shape
    ng, F = scales.shape
    G = D // ng
    deq = lut[qw]
    s = (lambda x, n, axis: torch.repeat_interleave(x, n, dim=axis))(scales, G, axis=0)
    w = (lambda x, dt: x.to(dt))(deq, x.dtype) * (lambda x, dt: x.to(dt))(s, x.dtype)
    return torch.einsum("bsd,df->bsf", x, w)
```

```cpp
#include <hip/hip_runtime.h>
#include <stdint.h>

__global__ __launch_bounds__(512, 2)
void k_nf4(const unsigned short* __restrict__ x, const int* __restrict__ qw, const unsigned short* __restrict__ lut,
           const unsigned short* __restrict__ scales, unsigned short* __restrict__ out, char* __restrict__ ws) {
    __shared__ __attribute__((aligned(1024))) char smem[160 * 1024];
    const unsigned ldsb = (unsigned)(uintptr_t)(__attribute__((address_space(3))) char*)smem;
    const auto ka = __builtin_amdgcn_kernarg_segment_ptr();
    unsigned tid = threadIdx.x;
    asm volatile(
        "Lstart_%=:\n"
        "s_load_dwordx8 s[8:15], %[ka], 0x0\n"
        "s_load_dwordx4 s[16:19], %[ka], 0x20\n"
        "s_getpc_b64 s[80:81]\n"
        "s_lshr_b32 s84, %[bid], 3\n"
        "s_lshl_b32 s85, s84, 13\n"
        "v_lshl_add_u32 v34, %[tid], 4, s85\n"
        "v_cmp_gt_u32 vcc, Lend_%=-Lstart_%=-64, v34\n"
        "s_and_saveexec_b64 s[78:79], vcc\n"
        "s_cbranch_execz Lncp_%=\n"
        "global_load_dwordx4 v[56:59], v34, s[80:81]\n"
        "Lncp_%=:\n"
        "s_mov_b64 exec, -1\n"
        "v_mov_b32 v33, %[tid]\n"
        "v_readfirstlane_b32 s44, %[tid]\n"
        "s_bfe_u32 s74, %[bid], 0x20001\n"
        "s_and_b32 s75, s84, 3\n"
        "s_lshl2_add_u32 s45, s74, s75\n"
        "s_lshr_b32 s74, s84, 2\n"
        "s_and_b32 s75, %[bid], 1\n"
        "s_lshl3_add_u32 s46, s75, s74\n"
        "s_lshl1_add_u32 s47, s74, s75\n"
        "s_lshr_b32 s44, s44, 6\n"
        "s_mul_i32 s62, s45, 896\n"
        "s_lshl_b32 s56, s45, 18\n"
        "s_mov_b32 s87, 0\n"
        "v_and_b32 v32, 63, v33\n"
        "v_lshrrev_b32 v34, 5, v32\n"
        "v_bfe_u32 v35, v32, 4, 1\n"
        "v_bfe_u32 v36, v32, 2, 2\n"
        "v_lshlrev_b32 v36, 2, v36\n"
        "v_and_b32 v37, 3, v32\n"
        "v_mul_u32_u24 v38, 112, v34\n"
        "v_mad_u32_u24 v39, v35, 48, v38\n"
        "v_lshl_add_u32 v40, v36, 1, v39\n"
        "v_lshl_add_u32 v40, v37, 2, v40\n"
        "v_add3_u32 v39, v39, v36, 32\n"
        "v_add_u32 v38, 96, v38\n"
        "v_add_u32 v38, v38, v36\n"
        "v_cmp_eq_u32 vcc, 2, v37\n"
        "s_nop 1\n"
        "v_cndmask_b32 v38, v38, v39, vcc\n"
        "v_cmp_gt_u32 vcc, 2, v37\n"
        "s_nop 1\n"
        "v_cndmask_b32 v38, v38, v40, vcc\n"
        "v_lshlrev_b32 v100, 2, v38\n"
        "v_lshlrev_b32 v103, 4, v32\n"
        "s_and_b32 s74, s44, 1\n"
        "s_lshl1_add_u32 s54, s47, s74\n"
        "s_mul_i32 s50, s54, 114688\n"
        "s_add_u32 s50, s50, s62\n"
        "s_and_b32 s75, s44, 6\n"
        "s_waitcnt lgkmcnt(0)\n"
        "s_mov_b32 s20, s10\n"
        "s_and_b32 s21, s11, 0xffff\n"
        "s_mov_b32 s22, 14680064\n"
        "s_mov_b32 s23, 0x20000\n"
        "s_cmp_lg_u32 s75, 4\n"
        "s_cbranch_scc1 Lner_%=\n"
        "buffer_load_dwordx4 v[0:3], v100, s[20:23], s50 offen nt\n"
        "s_add_u32 s75, s50, 14336\n"
        "buffer_load_dwordx4 v[4:7], v100, s[20:23], s75 offen nt\n"
        "s_add_u32 s76, s50, 28672\n"
        "buffer_load_dwordx4 v[8:11], v100, s[20:23], s76 offen nt\n"
        "s_add_u32 s77, s50, 43008\n"
        "buffer_load_dwordx4 v[12:15], v100, s[20:23], s77 offen nt\n"
        "s_add_u32 s74, s50, 57344\n"
        "buffer_load_dwordx4 v[16:19], v100, s[20:23], s74 offen nt\n"
        "s_add_u32 s75, s50, 71680\n"
        "buffer_load_dwordx4 v[20:23], v100, s[20:23], s75 offen nt\n"
        "s_add_u32 s76, s50, 86016\n"
        "buffer_load_dwordx4 v[24:27], v100, s[20:23], s76 offen nt\n"
        "s_add_u32 s77, s50, 100352\n"
        "buffer_load_dwordx4 v[28:31], v100, s[20:23], s77 offen nt\n"
        "Lner_%=:\n"
        "s_lshl_b32 s74, s45, 8\n"
        "s_add_u32 s68, s18, s74\n"
        "s_addc_u32 s69, s19, 0\n"
        "v_mov_b32 v42, 0\n"
        "global_load_dwordx2 v[42:43], v42, s[68:69] sc1\n"
        "v_bfe_u32 v34, v33, 3, 4\n"
        "v_lshlrev_b32 v34, 1, v34\n"
        "global_load_ushort v44, v34, s[12:13]\n"
        "s_load_dwordx8 s[92:99], s[12:13], 0x0\n"
        "s_cmp_lt_u32 s44, 4\n"
        "s_cbranch_scc1 Lcomp_%=\n"
        "Lhsel_%=:\n"
        "s_cmp_eq_u32 s44, 5\n"
        "s_cbranch_scc1 Lhelper1_%=\n"
        "s_cmp_eq_u32 s44, 6\n"
        "s_cbranch_scc1 Lhelper2_%=\n"
        "s_cmp_eq_u32 s44, 7\n"
        "s_cbranch_scc1 Lhelper3_%=\n"
        "Lhelper0_%=:\n"
        "s_lshl_b32 s74, s54, 11\n"
        "s_add_u32 s51, s74, s56\n"
        "s_add_u32 s55, s47, 64\n"
        "s_mul_i32 s52, s55, 114688\n"
        "s_add_u32 s52, s52, s62\n"
        "s_lshl_b32 s75, s55, 11\n"
        "s_add_u32 s53, s75, s56\n"
        "v_lshrrev_b32 v34, 4, v32\n"
        "v_and_b32 v35, 7, v32\n"
        "v_xor_b32 v35, v35, v34\n"
        "v_lshlrev_b32 v35, 4, v35\n"
        "v_lshrrev_b32 v36, 3, v32\n"
        "v_lshl_or_b32 v101, v36, 11, v35\n"
        "v_xor_b32 v102, 0x4040, v101\n"
        "v_and_b32 v104, 1, v32\n"
        "v_lshlrev_b32 v104, 3, v104\n"
        "s_lshl_b32 s57, s46, 18\n"
        "s_add_u32 s57, s57, 0\n"
        "s_mov_b32 s32, s8\n"
        "s_and_b32 s33, s9, 0xffff\n"
        "s_mov_b32 s34, 4194304\n"
        "s_mov_b32 s35, 0x20000\n"
        "s_mov_b32 m0, 65536\n"
        "s_add_u32 s86, s57, 0\n"
        "buffer_load_dwordx4 v101, s[32:35], s86 offen  lds\n"
        "s_mov_b32 m0, 66560\n"
        "s_nop 0\n"
        "buffer_load_dwordx4 v102, s[32:35], s86 offen  lds\n"
        "s_mov_b32 m0, 67584\n"
        "s_add_u32 s86, s57, 32768\n"
        "buffer_load_dwordx4 v101, s[32:35], s86 offen  lds\n"
        "s_mov_b32 m0, 68608\n"
        "s_nop 0\n"
        "buffer_load_dwordx4 v102, s[32:35], s86 offen  lds\n"
        "s_add_u32 s24, s18, 65536\n"
        "s_addc_u32 s25, s19, 0\n"
        "s_and_b32 s25, s25, 0xffff\n"
        "s_mov_b32 s26, 4194304\n"
        "s_mov_b32 s27, 0x20000\n"
        "s_lshl_b32 s74, s45, 11\n"
        "s_add_u32 s74, s74, 8192\n"
        "s_add_u32 s28, s18, s74\n"
        "s_addc_u32 s29, s19, 0\n"
        "s_and_b32 s29, s29, 0xffff\n"
        "s_mov_b32 s30, 1024\n"
        "s_mov_b32 s31, 0x20000\n"
        "s_waitcnt vmcnt(5)\n"
        "s_mov_b32 s4, 0x55555555\n"
        "s_mov_b32 s5, 0x55555555\n"
        "v_readfirstlane_b32 s66, v42\n"
        "v_readfirstlane_b32 s67, v43\n"
        "s_mov_b32 s6, 0x33333333\n"
        "s_mov_b32 s7, 0x33333333\n"
        "s_nop 1\n"
        "s_add_u32 s64, s66, 1\n"
        "s_add_u32 s65, s67, 1\n"
        "v_lshl_or_b32 v0, v4, 4, v0\n"
        "v_lshl_or_b32 v1, v5, 4, v1\n"
        "v_lshl_or_b32 v2, v6, 4, v2\n"
        "v_lshl_or_b32 v3, v7, 4, v3\n"
        "v_lshl_or_b32 v8, v12, 4, v8\n"
        "v_lshl_or_b32 v9, v13, 4, v9\n"
        "v_lshl_or_b32 v10, v14, 4, v10\n"
        "v_lshl_or_b32 v11, v15, 4, v11\n"
        "v_lshl_or_b32 v0, v8, 8, v0\n"
        "v_lshl_or_b32 v1, v9, 8, v1\n"
        "v_lshl_or_b32 v2, v10, 8, v2\n"
        "v_lshl_or_b32 v3, v11, 8, v3\n"
        "v_lshl_or_b32 v16, v20, 4, v16\n"
        "v_lshl_or_b32 v17, v21, 4, v17\n"
        "v_lshl_or_b32 v18, v22, 4, v18\n"
        "v_lshl_or_b32 v19, v23, 4, v19\n"
        "v_lshl_or_b32 v24, v28, 4, v24\n"
        "v_lshl_or_b32 v25, v29, 4, v25\n"
        "v_lshl_or_b32 v26, v30, 4, v26\n"
        "v_lshl_or_b32 v27, v31, 4, v27\n"
        "v_lshl_or_b32 v16, v24, 8, v16\n"
        "v_lshl_or_b32 v17, v25, 8, v17\n"
        "v_lshl_or_b32 v18, v26, 8, v18\n"
        "v_lshl_or_b32 v19, v27, 8, v19\n"
        "v_lshl_or_b32 v4, v16, 16, v0\n"
        "v_lshl_or_b32 v5, v17, 16, v1\n"
        "v_lshl_or_b32 v6, v18, 16, v2\n"
        "v_lshl_or_b32 v7, v19, 16, v3\n"
        "s_mov_b64 vcc, s[4:5]\n"
        "s_nop 0\n"
        "v_cndmask_b32_dpp v8, v5, v4, vcc quad_perm:[1,0,3,2] row_mask:0xf bank_mask:0xf\n"
        "v_cndmask_b32_dpp v10, v7, v6, vcc quad_perm:[1,0,3,2] row_mask:0xf bank_mask:0xf\n"
        "s_not_b64 vcc, vcc\n"
        "s_nop 0\n"
        "v_cndmask_b32_dpp v9, v4, v5, vcc quad_perm:[1,0,3,2] row_mask:0xf bank_mask:0xf\n"
        "v_cndmask_b32_dpp v11, v6, v7, vcc quad_perm:[1,0,3,2] row_mask:0xf bank_mask:0xf\n"
        "s_mov_b64 vcc, s[6:7]\n"
        "s_nop 0\n"
        "v_cndmask_b32_dpp v28, v10, v8, vcc quad_perm:[2,3,0,1] row_mask:0xf bank_mask:0xf\n"
        "v_cndmask_b32_dpp v29, v11, v9, vcc quad_perm:[2,3,0,1] row_mask:0xf bank_mask:0xf\n"
        "s_not_b64 vcc, vcc\n"
        "s_nop 0\n"
        "v_cndmask_b32_dpp v30, v8, v10, vcc quad_perm:[2,3,0,1] row_mask:0xf bank_mask:0xf\n"
        "v_cndmask_b32_dpp v31, v9, v11, vcc quad_perm:[2,3,0,1] row_mask:0xf bank_mask:0xf\n"
        "v_mov_b32 v12, v28\n"
        "v_mov_b32 v13, s64\n"
        "v_mov_b32 v14, v29\n"
        "v_mov_b32 v15, s65\n"
        "buffer_store_dwordx4 v[12:15], v103, s[24:27], s51 offen offset:0 sc1\n"
        "v_mov_b32 v16, v30\n"
        "v_mov_b32 v17, s64\n"
        "v_mov_b32 v18, v31\n"
        "v_mov_b32 v19, s65\n"
        "buffer_store_dwordx4 v[16:19], v103, s[24:27], s51 offen offset:1024 sc1\n"
        "s_nop 1\n"
        "s_mov_b32 m0, 81920\n"
        "s_add_u32 s86, s57, 128\n"
        "buffer_load_dwordx4 v101, s[32:35], s86 offen  lds\n"
        "s_mov_b32 m0, 82944\n"
        "s_nop 0\n"
        "buffer_load_dwordx4 v102, s[32:35], s86 offen  lds\n"
        "s_mov_b32 m0, 83968\n"
        "s_add_u32 s86, s57, 32896\n"
        "buffer_load_dwordx4 v101, s[32:35], s86 offen  lds\n"
        "s_mov_b32 m0, 84992\n"
        "s_nop 0\n"
        "buffer_load_dwordx4 v102, s[32:35], s86 offen  lds\n"
        "v_lshrrev_b32 v62, 4, v32\n"
        "v_and_b32 v61, 15, v32\n"
        "v_lshrrev_b32 v38, 1, v62\n"
        "v_and_b32 v39, 1, v62\n"
        "v_mul_u32_u24 v38, 112, v38\n"
        "v_mad_u32_u24 v40, v39, 48, v38\n"
        "v_lshrrev_b32 v39, 2, v61\n"
        "v_lshl_add_u32 v246, v39, 3, v40\n"
        "v_and_b32 v39, 3, v61\n"
        "v_add_u32 v246, v246, v39\n"
        "v_add3_u32 v247, v40, v61, 32\n"
        "v_add_u32 v38, 96, v38\n"
        "v_add_u32 v248, v38, v61\n"
        "v_lshlrev_b32 v246, 2, v246\n"
        "v_lshlrev_b32 v247, 2, v247\n"
        "v_lshlrev_b32 v248, 2, v248\n"
        "v_lshrrev_b32 v34, 3, v33\n"
        "v_lshlrev_b32 v34, 8, v34\n"
        "v_and_b32 v35, 7, v33\n"
        "v_lshl_or_b32 v34, v35, 4, v34\n"
        "s_waitcnt lgkmcnt(0)\n"
        "s_bitcmp1_b32 s44, 2\n"
        "s_cselect_b32 s74, s93, s92\n"
        "s_bitcmp1_b32 s44, 2\n"
        "s_cselect_b32 s75, s95, s94\n"
        "s_bitcmp1_b32 s44, 2\n"
        "s_cselect_b32 s76, s97, s96\n"
        "s_bitcmp1_b32 s44, 2\n"
        "s_cselect_b32 s77, s99, s98\n"
        "s_lshl_b32 s92, s74, 16\n"
        "s_and_b32 s74, s74, 0xffff0000\n"
        "s_lshl_b32 s93, s75, 16\n"
        "s_and_b32 s75, s75, 0xffff0000\n"
        "s_lshl_b32 s94, s76, 16\n"
        "s_and_b32 s76, s76, 0xffff0000\n"
        "s_lshl_b32 s95, s77, 16\n"
        "s_and_b32 s77, s77, 0xffff0000\n"
        "s_bitcmp1_b32 s44, 1\n"
        "s_cselect_b32 s74, s74, s92\n"
        "s_cselect_b32 s75, s75, s93\n"
        "s_cselect_b32 s76, s76, s94\n"
        "s_cselect_b32 s77, s77, s95\n"
        "s_waitcnt vmcnt(10)\n"
        "v_or_b32 v48, s74, v44\n"
        "v_or_b32 v49, s74, v44\n"
        "v_or_b32 v50, s74, v44\n"
        "v_or_b32 v51, s74, v44\n"
        "ds_write_b128 v34, v[48:51] offset:0\n"
        "s_nop 1\n"
        "v_or_b32 v48, s75, v44\n"
        "v_or_b32 v49, s75, v44\n"
        "v_or_b32 v50, s75, v44\n"
        "v_or_b32 v51, s75, v44\n"
        "ds_write_b128 v34, v[48:51] offset:16384\n"
        "s_nop 1\n"
        "v_or_b32 v48, s76, v44\n"
        "v_or_b32 v49, s76, v44\n"
        "v_or_b32 v50, s76, v44\n"
        "v_or_b32 v51, s76, v44\n"
        "ds_write_b128 v34, v[48:51] offset:32768\n"
        "s_nop 1\n"
        "v_or_b32 v48, s77, v44\n"
        "v_or_b32 v49, s77, v44\n"
        "v_or_b32 v50, s77, v44\n"
        "v_or_b32 v51, s77, v44\n"
        "ds_write_b128 v34, v[48:51] offset:49152\n"
        "s_waitcnt vmcnt(4)\n"
        "s_waitcnt lgkmcnt(0)\n"
        "s_barrier\n"
        "s_branch Lhm0_%=\n"
        "Lhelper1_%=:\n"
        "s_lshl_b32 s74, s54, 11\n"
        "s_add_u32 s51, s74, s56\n"
        "s_add_u32 s55, s47, 80\n"
        "s_mul_i32 s52, s55, 114688\n"
        "s_add_u32 s52, s52, s62\n"
        "s_lshl_b32 s75, s55, 11\n"
        "s_add_u32 s53, s75, s56\n"
        "v_lshrrev_b32 v34, 4, v32\n"
        "v_and_b32 v35, 7, v32\n"
        "v_xor_b32 v35, v35, v34\n"
        "v_lshlrev_b32 v35, 4, v35\n"
        "v_lshrrev_b32 v36, 3, v32\n"
        "v_lshl_or_b32 v101, v36, 11, v35\n"
        "v_xor_b32 v102, 0x4040, v101\n"
        "v_and_b32 v104, 1, v32\n"
        "v_lshlrev_b32 v104, 3, v104\n"
        "s_lshl_b32 s57, s46, 18\n"
        "s_add_u32 s57, s57, 65536\n"
        "s_mov_b32 s32, s8\n"
        "s_and_b32 s33, s9, 0xffff\n"
        "s_mov_b32 s34, 4194304\n"
        "s_mov_b32 s35, 0x20000\n"
        "s_mov_b32 m0, 69632\n"
        "s_add_u32 s86, s57, 0\n"
        "buffer_load_dwordx4 v101, s[32:35], s86 offen  lds\n"
        "s_mov_b32 m0, 70656\n"
        "s_nop 0\n"
        "buffer_load_dwordx4 v102, s[32:35], s86 offen  lds\n"
        "s_mov_b32 m0, 71680\n"
        "s_add_u32 s86, s57, 32768\n"
        "buffer_load_dwordx4 v101, s[32:35], s86 offen  lds\n"
        "s_mov_b32 m0, 72704\n"
        "s_nop 0\n"
        "buffer_load_dwordx4 v102, s[32:35], s86 offen  lds\n"
        "s_add_u32 s24, s18, 65536\n"
        "s_addc_u32 s25, s19, 0\n"
        "s_and_b32 s25, s25, 0xffff\n"
        "s_mov_b32 s26, 4194304\n"
        "s_mov_b32 s27, 0x20000\n"
        "s_lshl_b32 s74, s45, 11\n"
        "s_add_u32 s74, s74, 8192\n"
        "s_add_u32 s28, s18, s74\n"
        "s_addc_u32 s29, s19, 0\n"
        "s_and_b32 s29, s29, 0xffff\n"
        "s_mov_b32 s30, 1024\n"
        "s_mov_b32 s31, 0x20000\n"
        "s_waitcnt vmcnt(5)\n"
        "s_mov_b32 s4, 0x55555555\n"
        "s_mov_b32 s5, 0x55555555\n"
        "v_readfirstlane_b32 s66, v42\n"
        "v_readfirstlane_b32 s67, v43\n"
        "s_mov_b32 s6, 0x33333333\n"
        "s_mov_b32 s7, 0x33333333\n"
        "s_nop 1\n"
        "s_add_u32 s64, s66, 1\n"
        "s_add_u32 s65, s67, 1\n"
        "v_lshl_or_b32 v0, v4, 4, v0\n"
        "v_lshl_or_b32 v1, v5, 4, v1\n"
        "v_lshl_or_b32 v2, v6, 4, v2\n"
        "v_lshl_or_b32 v3, v7, 4, v3\n"
        "v_lshl_or_b32 v8, v12, 4, v8\n"
        "v_lshl_or_b32 v9, v13, 4, v9\n"
        "v_lshl_or_b32 v10, v14, 4, v10\n"
        "v_lshl_or_b32 v11, v15, 4, v11\n"
        "v_lshl_or_b32 v0, v8, 8, v0\n"
        "v_lshl_or_b32 v1, v9, 8, v1\n"
        "v_lshl_or_b32 v2, v10, 8, v2\n"
        "v_lshl_or_b32 v3, v11, 8, v3\n"
        "v_lshl_or_b32 v16, v20, 4, v16\n"
        "v_lshl_or_b32 v17, v21, 4, v17\n"
        "v_lshl_or_b32 v18, v22, 4, v18\n"
        "v_lshl_or_b32 v19, v23, 4, v19\n"
        "v_lshl_or_b32 v24, v28, 4, v24\n"
        "v_lshl_or_b32 v25, v29, 4, v25\n"
        "v_lshl_or_b32 v26, v30, 4, v26\n"
        "v_lshl_or_b32 v27, v31, 4, v27\n"
        "v_lshl_or_b32 v16, v24, 8, v16\n"
        "v_lshl_or_b32 v17, v25, 8, v17\n"
        "v_lshl_or_b32 v18, v26, 8, v18\n"
        "v_lshl_or_b32 v19, v27, 8, v19\n"
        "v_lshl_or_b32 v4, v16, 16, v0\n"
        "v_lshl_or_b32 v5, v17, 16, v1\n"
        "v_lshl_or_b32 v6, v18, 16, v2\n"
        "v_lshl_or_b32 v7, v19, 16, v3\n"
        "s_mov_b64 vcc, s[4:5]\n"
        "s_nop 0\n"
        "v_cndmask_b32_dpp v8, v5, v4, vcc quad_perm:[1,0,3,2] row_mask:0xf bank_mask:0xf\n"
        "v_cndmask_b32_dpp v10, v7, v6, vcc quad_perm:[1,0,3,2] row_mask:0xf bank_mask:0xf\n"
        "s_not_b64 vcc, vcc\n"
        "s_nop 0\n"
        "v_cndmask_b32_dpp v9, v4, v5, vcc quad_perm:[1,0,3,2] row_mask:0xf bank_mask:0xf\n"
        "v_cndmask_b32_dpp v11, v6, v7, vcc quad_perm:[1,0,3,2] row_mask:0xf bank_mask:0xf\n"
        "s_mov_b64 vcc, s[6:7]\n"
        "s_nop 0\n"
        "v_cndmask_b32_dpp v28, v10, v8, vcc quad_perm:[2,3,0,1] row_mask:0xf bank_mask:0xf\n"
        "v_cndmask_b32_dpp v29, v11, v9, vcc quad_perm:[2,3,0,1] row_mask:0xf bank_mask:0xf\n"
        "s_not_b64 vcc, vcc\n"
        "s_nop 0\n"
        "v_cndmask_b32_dpp v30, v8, v10, vcc quad_perm:[2,3,0,1] row_mask:0xf bank_mask:0xf\n"
        "v_cndmask_b32_dpp v31, v9, v11, vcc quad_perm:[2,3,0,1] row_mask:0xf bank_mask:0xf\n"
        "v_mov_b32 v12, v28\n"
        "v_mov_b32 v13, s64\n"
        "v_mov_b32 v14, v29\n"
        "v_mov_b32 v15, s65\n"
        "buffer_store_dwordx4 v[12:15], v103, s[24:27], s51 offen offset:0 sc1\n"
        "v_mov_b32 v16, v30\n"
        "v_mov_b32 v17, s64\n"
        "v_mov_b32 v18, v31\n"
        "v_mov_b32 v19, s65\n"
        "buffer_store_dwordx4 v[16:19], v103, s[24:27], s51 offen offset:1024 sc1\n"
        "s_nop 1\n"
        "s_mov_b32 m0, 86016\n"
        "s_add_u32 s86, s57, 128\n"
        "buffer_load_dwordx4 v101, s[32:35], s86 offen  lds\n"
        "s_mov_b32 m0, 87040\n"
        "s_nop 0\n"
        "buffer_load_dwordx4 v102, s[32:35], s86 offen  lds\n"
        "s_mov_b32 m0, 88064\n"
        "s_add_u32 s86, s57, 32896\n"
        "buffer_load_dwordx4 v101, s[32:35], s86 offen  lds\n"
        "s_mov_b32 m0, 89088\n"
        "s_nop 0\n"
        "buffer_load_dwordx4 v102, s[32:35], s86 offen  lds\n"
        "v_lshrrev_b32 v62, 4, v32\n"
        "v_and_b32 v61, 15, v32\n"
        "v_lshrrev_b32 v38, 1, v62\n"
        "v_and_b32 v39, 1, v62\n"
        "v_mul_u32_u24 v38, 112, v38\n"
        "v_mad_u32_u24 v40, v39, 48, v38\n"
        "v_lshrrev_b32 v39, 2, v61\n"
        "v_lshl_add_u32 v246, v39, 3, v40\n"
        "v_and_b32 v39, 3, v61\n"
        "v_add_u32 v246, v246, v39\n"
        "v_add3_u32 v247, v40, v61, 32\n"
        "v_add_u32 v38, 96, v38\n"
        "v_add_u32 v248, v38, v61\n"
        "v_lshlrev_b32 v246, 2, v246\n"
        "v_lshlrev_b32 v247, 2, v247\n"
        "v_lshlrev_b32 v248, 2, v248\n"
        "v_lshrrev_b32 v34, 3, v33\n"
        "v_lshlrev_b32 v34, 8, v34\n"
        "v_and_b32 v35, 7, v33\n"
        "v_lshl_or_b32 v34, v35, 4, v34\n"
        "s_waitcnt lgkmcnt(0)\n"
        "s_bitcmp1_b32 s44, 2\n"
        "s_cselect_b32 s74, s93, s92\n"
        "s_bitcmp1_b32 s44, 2\n"
        "s_cselect_b32 s75, s95, s94\n"
        "s_bitcmp1_b32 s44, 2\n"
        "s_cselect_b32 s76, s97, s96\n"
        "s_bitcmp1_b32 s44, 2\n"
        "s_cselect_b32 s77, s99, s98\n"
        "s_lshl_b32 s92, s74, 16\n"
        "s_and_b32 s74, s74, 0xffff0000\n"
        "s_lshl_b32 s93, s75, 16\n"
        "s_and_b32 s75, s75, 0xffff0000\n"
        "s_lshl_b32 s94, s76, 16\n"
        "s_and_b32 s76, s76, 0xffff0000\n"
        "s_lshl_b32 s95, s77, 16\n"
        "s_and_b32 s77, s77, 0xffff0000\n"
        "s_bitcmp1_b32 s44, 1\n"
        "s_cselect_b32 s74, s74, s92\n"
        "s_cselect_b32 s75, s75, s93\n"
        "s_cselect_b32 s76, s76, s94\n"
        "s_cselect_b32 s77, s77, s95\n"
        "s_waitcnt vmcnt(10)\n"
        "v_or_b32 v48, s74, v44\n"
        "v_or_b32 v49, s74, v44\n"
        "v_or_b32 v50, s74, v44\n"
        "v_or_b32 v51, s74, v44\n"
        "ds_write_b128 v34, v[48:51] offset:0\n"
        "s_nop 1\n"
        "v_or_b32 v48, s75, v44\n"
        "v_or_b32 v49, s75, v44\n"
        "v_or_b32 v50, s75, v44\n"
        "v_or_b32 v51, s75, v44\n"
        "ds_write_b128 v34, v[48:51] offset:16384\n"
        "s_nop 1\n"
        "v_or_b32 v48, s76, v44\n"
        "v_or_b32 v49, s76, v44\n"
        "v_or_b32 v50, s76, v44\n"
        "v_or_b32 v51, s76, v44\n"
        "ds_write_b128 v34, v[48:51] offset:32768\n"
        "s_nop 1\n"
        "v_or_b32 v48, s77, v44\n"
        "v_or_b32 v49, s77, v44\n"
        "v_or_b32 v50, s77, v44\n"
        "v_or_b32 v51, s77, v44\n"
        "ds_write_b128 v34, v[48:51] offset:49152\n"
        "s_waitcnt vmcnt(4)\n"
        "s_waitcnt lgkmcnt(0)\n"
        "s_barrier\n"
        "s_branch Lhm1_%=\n"
        "Lhelper2_%=:\n"
        "s_lshl1_add_u32 s54, s47, 32\n"
        "s_mul_i32 s50, s54, 114688\n"
        "s_add_u32 s50, s50, s62\n"
        "s_lshl_b32 s75, s54, 11\n"
        "s_add_u32 s51, s75, s56\n"
        "s_add_u32 s55, s47, 96\n"
        "s_mul_i32 s52, s55, 114688\n"
        "s_add_u32 s52, s52, s62\n"
        "s_lshl_b32 s76, s55, 11\n"
        "s_add_u32 s53, s76, s56\n"
        "v_lshrrev_b32 v34, 4, v32\n"
        "v_and_b32 v35, 7, v32\n"
        "v_xor_b32 v35, v35, v34\n"
        "v_lshlrev_b32 v35, 4, v35\n"
        "v_lshrrev_b32 v36, 3, v32\n"
        "v_lshl_or_b32 v101, v36, 11, v35\n"
        "v_xor_b32 v102, 0x4040, v101\n"
        "v_and_b32 v104, 1, v32\n"
        "v_lshlrev_b32 v104, 3, v104\n"
        "s_lshl_b32 s57, s46, 18\n"
        "s_add_u32 s57, s57, 131072\n"
        "s_mov_b32 s32, s8\n"
        "s_and_b32 s33, s9, 0xffff\n"
        "s_mov_b32 s34, 4194304\n"
        "s_mov_b32 s35, 0x20000\n"
        "s_mov_b32 m0, 73728\n"
        "s_add_u32 s86, s57, 0\n"
        "buffer_load_dwordx4 v101, s[32:35], s86 offen  lds\n"
        "s_mov_b32 m0, 74752\n"
        "s_nop 0\n"
        "buffer_load_dwordx4 v102, s[32:35], s86 offen  lds\n"
        "s_mov_b32 m0, 75776\n"
        "s_add_u32 s86, s57, 32768\n"
        "buffer_load_dwordx4 v101, s[32:35], s86 offen  lds\n"
        "s_mov_b32 m0, 76800\n"
        "s_nop 0\n"
        "buffer_load_dwordx4 v102, s[32:35], s86 offen  lds\n"
        "s_add_u32 s24, s18, 65536\n"
        "s_addc_u32 s25, s19, 0\n"
        "s_and_b32 s25, s25, 0xffff\n"
        "s_mov_b32 s26, 4194304\n"
        "s_mov_b32 s27, 0x20000\n"
        "s_lshl_b32 s74, s45, 11\n"
        "s_add_u32 s74, s74, 8192\n"
        "s_add_u32 s28, s18, s74\n"
        "s_addc_u32 s29, s19, 0\n"
        "s_and_b32 s29, s29, 0xffff\n"
        "s_mov_b32 s30, 1024\n"
        "s_mov_b32 s31, 0x20000\n"
        "s_waitcnt vmcnt(5)\n"
        "s_mov_b32 s4, 0x55555555\n"
        "s_mov_b32 s5, 0x55555555\n"
        "v_readfirstlane_b32 s66, v42\n"
        "v_readfirstlane_b32 s67, v43\n"
        "s_mov_b32 s6, 0x33333333\n"
        "s_mov_b32 s7, 0x33333333\n"
        "s_nop 1\n"
        "s_add_u32 s64, s66, 1\n"
        "s_add_u32 s65, s67, 1\n"
        "s_sleep 4\n"
        "buffer_load_dwordx4 v[0:3], v100, s[20:23], s50 offen nt\n"
        "s_add_u32 s77, s50, 14336\n"
        "buffer_load_dwordx4 v[4:7], v100, s[20:23], s77 offen nt\n"
        "s_add_u32 s74, s50, 28672\n"
        "buffer_load_dwordx4 v[8:11], v100, s[20:23], s74 offen nt\n"
        "s_add_u32 s75, s50, 43008\n"
        "buffer_load_dwordx4 v[12:15], v100, s[20:23], s75 offen nt\n"
        "s_add_u32 s76, s50, 57344\n"
        "buffer_load_dwordx4 v[16:19], v100, s[20:23], s76 offen nt\n"
        "s_add_u32 s77, s50, 71680\n"
        "buffer_load_dwordx4 v[20:23], v100, s[20:23], s77 offen nt\n"
        "s_add_u32 s74, s50, 86016\n"
        "buffer_load_dwordx4 v[24:27], v100, s[20:23], s74 offen nt\n"
        "s_add_u32 s75, s50, 100352\n"
        "buffer_load_dwordx4 v[28:31], v100, s[20:23], s75 offen nt\n"
        "s_mov_b32 m0, 90112\n"
        "s_add_u32 s86, s57, 128\n"
        "buffer_load_dwordx4 v101, s[32:35], s86 offen  lds\n"
        "s_mov_b32 m0, 91136\n"
        "s_nop 0\n"
        "buffer_load_dwordx4 v102, s[32:35], s86 offen  lds\n"
        "s_mov_b32 m0, 92160\n"
        "s_add_u32 s86, s57, 32896\n"
        "buffer_load_dwordx4 v101, s[32:35], s86 offen  lds\n"
        "s_mov_b32 m0, 93184\n"
        "s_nop 0\n"
        "buffer_load_dwordx4 v102, s[32:35], s86 offen  lds\n"
        "v_lshrrev_b32 v62, 4, v32\n"
        "v_and_b32 v61, 15, v32\n"
        "v_lshrrev_b32 v38, 1, v62\n"
        "v_and_b32 v39, 1, v62\n"
        "v_mul_u32_u24 v38, 112, v38\n"
        "v_mad_u32_u24 v40, v39, 48, v38\n"
        "v_lshrrev_b32 v39, 2, v61\n"
        "v_lshl_add_u32 v246, v39, 3, v40\n"
        "v_and_b32 v39, 3, v61\n"
        "v_add_u32 v246, v246, v39\n"
        "v_add3_u32 v247, v40, v61, 32\n"
        "v_add_u32 v38, 96, v38\n"
        "v_add_u32 v248, v38, v61\n"
        "v_lshlrev_b32 v246, 2, v246\n"
        "v_lshlrev_b32 v247, 2, v247\n"
        "v_lshlrev_b32 v248, 2, v248\n"
        "v_lshrrev_b32 v34, 3, v33\n"
        "v_lshlrev_b32 v34, 8, v34\n"
        "v_and_b32 v35, 7, v33\n"
        "v_lshl_or_b32 v34, v35, 4, v34\n"
        "s_waitcnt lgkmcnt(0)\n"
        "s_bitcmp1_b32 s44, 2\n"
        "s_cselect_b32 s74, s93, s92\n"
        "s_bitcmp1_b32 s44, 2\n"
        "s_cselect_b32 s75, s95, s94\n"
        "s_bitcmp1_b32 s44, 2\n"
        "s_cselect_b32 s76, s97, s96\n"
        "s_bitcmp1_b32 s44, 2\n"
        "s_cselect_b32 s77, s99, s98\n"
        "s_lshl_b32 s92, s74, 16\n"
        "s_and_b32 s74, s74, 0xffff0000\n"
        "s_lshl_b32 s93, s75, 16\n"
        "s_and_b32 s75, s75, 0xffff0000\n"
        "s_lshl_b32 s94, s76, 16\n"
        "s_and_b32 s76, s76, 0xffff0000\n"
        "s_lshl_b32 s95, s77, 16\n"
        "s_and_b32 s77, s77, 0xffff0000\n"
        "s_bitcmp1_b32 s44, 1\n"
        "s_cselect_b32 s74, s74, s92\n"
        "s_cselect_b32 s75, s75, s93\n"
        "s_cselect_b32 s76, s76, s94\n"
        "s_cselect_b32 s77, s77, s95\n"
        "s_waitcnt vmcnt(16)\n"
        "v_or_b32 v48, s74, v44\n"
        "v_or_b32 v49, s74, v44\n"
        "v_or_b32 v50, s74, v44\n"
        "v_or_b32 v51, s74, v44\n"
        "ds_write_b128 v34, v[48:51] offset:0\n"
        "s_nop 1\n"
        "v_or_b32 v48, s75, v44\n"
        "v_or_b32 v49, s75, v44\n"
        "v_or_b32 v50, s75, v44\n"
        "v_or_b32 v51, s75, v44\n"
        "ds_write_b128 v34, v[48:51] offset:16384\n"
        "s_nop 1\n"
        "v_or_b32 v48, s76, v44\n"
        "v_or_b32 v49, s76, v44\n"
        "v_or_b32 v50, s76, v44\n"
        "v_or_b32 v51, s76, v44\n"
        "ds_write_b128 v34, v[48:51] offset:32768\n"
        "s_nop 1\n"
        "v_or_b32 v48, s77, v44\n"
        "v_or_b32 v49, s77, v44\n"
        "v_or_b32 v50, s77, v44\n"
        "v_or_b32 v51, s77, v44\n"
        "ds_write_b128 v34, v[48:51] offset:49152\n"
        "s_waitcnt vmcnt(12)\n"
        "s_waitcnt lgkmcnt(0)\n"
        "s_barrier\n"
        "s_branch Lhm2_%=\n"
        "Lhelper3_%=:\n"
        "s_lshl1_add_u32 s54, s47, 33\n"
        "s_mul_i32 s50, s54, 114688\n"
        "s_add_u32 s50, s50, s62\n"
        "s_lshl_b32 s75, s54, 11\n"
        "s_add_u32 s51, s75, s56\n"
        "s_add_u32 s55, s47, 112\n"
        "s_mul_i32 s52, s55, 114688\n"
        "s_add_u32 s52, s52, s62\n"
        "s_lshl_b32 s76, s55, 11\n"
        "s_add_u32 s53, s76, s56\n"
        "v_lshrrev_b32 v34, 4, v32\n"
        "v_and_b32 v35, 7, v32\n"
        "v_xor_b32 v35, v35, v34\n"
        "v_lshlrev_b32 v35, 4, v35\n"
        "v_lshrrev_b32 v36, 3, v32\n"
        "v_lshl_or_b32 v101, v36, 11, v35\n"
        "v_xor_b32 v102, 0x4040, v101\n"
        "v_and_b32 v104, 1, v32\n"
        "v_lshlrev_b32 v104, 3, v104\n"
        "s_lshl_b32 s57, s46, 18\n"
        "s_add_u32 s57, s57, 196608\n"
        "s_mov_b32 s32, s8\n"
        "s_and_b32 s33, s9, 0xffff\n"
        "s_mov_b32 s34, 4194304\n"
        "s_mov_b32 s35, 0x20000\n"
        "s_mov_b32 m0, 77824\n"
        "s_add_u32 s86, s57, 0\n"
        "buffer_load_dwordx4 v101, s[32:35], s86 offen  lds\n"
        "s_mov_b32 m0, 78848\n"
        "s_nop 0\n"
        "buffer_load_dwordx4 v102, s[32:35], s86 offen  lds\n"
        "s_mov_b32 m0, 79872\n"
        "s_add_u32 s86, s57, 32768\n"
        "buffer_load_dwordx4 v101, s[32:35], s86 offen  lds\n"
        "s_mov_b32 m0, 80896\n"
        "s_nop 0\n"
        "buffer_load_dwordx4 v102, s[32:35], s86 offen  lds\n"
        "s_add_u32 s24, s18, 65536\n"
        "s_addc_u32 s25, s19, 0\n"
        "s_and_b32 s25, s25, 0xffff\n"
        "s_mov_b32 s26, 4194304\n"
        "s_mov_b32 s27, 0x20000\n"
        "s_lshl_b32 s74, s45, 11\n"
        "s_add_u32 s74, s74, 8192\n"
        "s_add_u32 s28, s18, s74\n"
        "s_addc_u32 s29, s19, 0\n"
        "s_and_b32 s29, s29, 0xffff\n"
        "s_mov_b32 s30, 1024\n"
        "s_mov_b32 s31, 0x20000\n"
        "s_waitcnt vmcnt(5)\n"
        "s_mov_b32 s4, 0x55555555\n"
        "s_mov_b32 s5, 0x55555555\n"
        "v_readfirstlane_b32 s66, v42\n"
        "v_readfirstlane_b32 s67, v43\n"
        "s_mov_b32 s6, 0x33333333\n"
        "s_mov_b32 s7, 0x33333333\n"
        "s_nop 1\n"
        "s_add_u32 s64, s66, 1\n"
        "s_add_u32 s65, s67, 1\n"
        "s_sleep 8\n"
        "buffer_load_dwordx4 v[0:3], v100, s[20:23], s50 offen nt\n"
        "s_add_u32 s77, s50, 14336\n"
        "buffer_load_dwordx4 v[4:7], v100, s[20:23], s77 offen nt\n"
        "s_add_u32 s74, s50, 28672\n"
        "buffer_load_dwordx4 v[8:11], v100, s[20:23], s74 offen nt\n"
        "s_add_u32 s75, s50, 43008\n"
        "buffer_load_dwordx4 v[12:15], v100, s[20:23], s75 offen nt\n"
        "s_add_u32 s76, s50, 57344\n"
        "buffer_load_dwordx4 v[16:19], v100, s[20:23], s76 offen nt\n"
        "s_add_u32 s77, s50, 71680\n"
        "buffer_load_dwordx4 v[20:23], v100, s[20:23], s77 offen nt\n"
        "s_add_u32 s74, s50, 86016\n"
        "buffer_load_dwordx4 v[24:27], v100, s[20:23], s74 offen nt\n"
        "s_add_u32 s75, s50, 100352\n"
        "buffer_load_dwordx4 v[28:31], v100, s[20:23], s75 offen nt\n"
        "s_mov_b32 m0, 94208\n"
        "s_add_u32 s86, s57, 128\n"
        "buffer_load_dwordx4 v101, s[32:35], s86 offen  lds\n"
        "s_mov_b32 m0, 95232\n"
        "s_nop 0\n"
        "buffer_load_dwordx4 v102, s[32:35], s86 offen  lds\n"
        "s_mov_b32 m0, 96256\n"
        "s_add_u32 s86, s57, 32896\n"
        "buffer_load_dwordx4 v101, s[32:35], s86 offen  lds\n"
        "s_mov_b32 m0, 97280\n"
        "s_nop 0\n"
        "buffer_load_dwordx4 v102, s[32:35], s86 offen  lds\n"
        "v_lshrrev_b32 v62, 4, v32\n"
        "v_and_b32 v61, 15, v32\n"
        "v_lshrrev_b32 v38, 1, v62\n"
        "v_and_b32 v39, 1, v62\n"
        "v_mul_u32_u24 v38, 112, v38\n"
        "v_mad_u32_u24 v40, v39, 48, v38\n"
        "v_lshrrev_b32 v39, 2, v61\n"
        "v_lshl_add_u32 v246, v39, 3, v40\n"
        "v_and_b32 v39, 3, v61\n"
        "v_add_u32 v246, v246, v39\n"
        "v_add3_u32 v247, v40, v61, 32\n"
        "v_add_u32 v38, 96, v38\n"
        "v_add_u32 v248, v38, v61\n"
        "v_lshlrev_b32 v246, 2, v246\n"
        "v_lshlrev_b32 v247, 2, v247\n"
        "v_lshlrev_b32 v248, 2, v248\n"
        "v_lshrrev_b32 v34, 3, v33\n"
        "v_lshlrev_b32 v34, 8, v34\n"
        "v_and_b32 v35, 7, v33\n"
        "v_lshl_or_b32 v34, v35, 4, v34\n"
        "s_waitcnt lgkmcnt(0)\n"
        "s_bitcmp1_b32 s44, 2\n"
        "s_cselect_b32 s74, s93, s92\n"
        "s_bitcmp1_b32 s44, 2\n"
        "s_cselect_b32 s75, s95, s94\n"
        "s_bitcmp1_b32 s44, 2\n"
        "s_cselect_b32 s76, s97, s96\n"
        "s_bitcmp1_b32 s44, 2\n"
        "s_cselect_b32 s77, s99, s98\n"
        "s_lshl_b32 s92, s74, 16\n"
        "s_and_b32 s74, s74, 0xffff0000\n"
        "s_lshl_b32 s93, s75, 16\n"
        "s_and_b32 s75, s75, 0xffff0000\n"
        "s_lshl_b32 s94, s76, 16\n"
        "s_and_b32 s76, s76, 0xffff0000\n"
        "s_lshl_b32 s95, s77, 16\n"
        "s_and_b32 s77, s77, 0xffff0000\n"
        "s_bitcmp1_b32 s44, 1\n"
        "s_cselect_b32 s74, s74, s92\n"
        "s_cselect_b32 s75, s75, s93\n"
        "s_cselect_b32 s76, s76, s94\n"
        "s_cselect_b32 s77, s77, s95\n"
        "s_waitcnt vmcnt(16)\n"
        "v_or_b32 v48, s74, v44\n"
        "v_or_b32 v49, s74, v44\n"
        "v_or_b32 v50, s74, v44\n"
        "v_or_b32 v51, s74, v44\n"
        "ds_write_b128 v34, v[48:51] offset:0\n"
        "s_nop 1\n"
        "v_or_b32 v48, s75, v44\n"
        "v_or_b32 v49, s75, v44\n"
        "v_or_b32 v50, s75, v44\n"
        "v_or_b32 v51, s75, v44\n"
        "ds_write_b128 v34, v[48:51] offset:16384\n"
        "s_nop 1\n"
        "v_or_b32 v48, s76, v44\n"
        "v_or_b32 v49, s76, v44\n"
        "v_or_b32 v50, s76, v44\n"
        "v_or_b32 v51, s76, v44\n"
        "ds_write_b128 v34, v[48:51] offset:32768\n"
        "s_nop 1\n"
        "v_or_b32 v48, s77, v44\n"
        "v_or_b32 v49, s77, v44\n"
        "v_or_b32 v50, s77, v44\n"
        "v_or_b32 v51, s77, v44\n"
        "ds_write_b128 v34, v[48:51] offset:49152\n"
        "s_waitcnt vmcnt(12)\n"
        "s_waitcnt lgkmcnt(0)\n"
        "s_barrier\n"
        "s_branch Lhm3_%=\n"
        "Lcomp_%=:\n"
        "s_add_u32 s24, s18, 65536\n"
        "s_addc_u32 s25, s19, 0\n"
        "s_and_b32 s25, s25, 0xffff\n"
        "s_mov_b32 s26, 4194304\n"
        "s_mov_b32 s27, 0x20000\n"
        "s_lshl_b32 s74, s45, 11\n"
        "s_add_u32 s74, s74, 8192\n"
        "s_add_u32 s28, s18, s74\n"
        "s_addc_u32 s29, s19, 0\n"
        "s_and_b32 s29, s29, 0xffff\n"
        "s_mov_b32 s30, 1024\n"
        "s_mov_b32 s31, 0x20000\n"
        "s_and_b32 s48, s44, 1\n"
        "s_lshr_b32 s49, s44, 1\n"
        "s_mul_i32 s74, s49, 112\n"
        "s_mul_i32 s75, s45, 224\n"
        "s_add_u32 s74, s74, s75\n"
        "s_mul_i32 s75, s48, 48\n"
        "v_mov_b32 v34, 48\n"
        "v_mov_b32 v35, s75\n"
        "v_cmp_gt_u32 vcc, 48, v32\n"
        "s_nop 1\n"
        "v_cndmask_b32 v34, v34, v35, vcc\n"
        "v_add3_u32 v34, v34, v32, s74\n"
        "v_lshlrev_b32 v34, 1, v34\n"
        "global_load_ushort v60, v34, s[14:15]\n"
        "v_and_b32 v61, 15, v32\n"
        "v_lshrrev_b32 v62, 4, v32\n"
        "v_lshrrev_b32 v34, 1, v61\n"
        "v_xor_b32 v34, v34, v62\n"
        "v_lshlrev_b32 v34, 4, v34\n"
        "v_lshl_or_b32 v34, v61, 7, v34\n"
        "s_lshl_b32 s74, s48, 13\n"
        "s_add_u32 s75, s74, 65536\n"
        "v_add_u32 v244, s75, v34\n"
        "s_sub_u32 s75, 73728, s74\n"
        "v_add_u32 v246, s75, v34\n"
        "v_xor_b32 v245, 64, v244\n"
        "v_xor_b32 v247, 64, v246\n"
        "v_and_b32 v248, 31, v32\n"
        "v_lshlrev_b32 v248, 2, v248\n"
        "s_lshl_b32 s74, s44, 8\n"
        "v_lshlrev_b32 v34, 4, v61\n"
        "v_add_u32 v34, s74, v34\n"
        "v_lshl_add_u32 v249, v62, 10, v34\n"
        "v_add_u32 v249, 131072, v249\n"
        "v_lshl_add_u32 v250, v62, 11, v34\n"
        "s_mov_b32 s70, 0x0c0c0400\n"
        "s_mov_b32 s71, 0x0c0c0500\n"
        "s_mov_b32 s72, 0x0c0c0600\n"
        "s_mov_b32 s73, 0x0c0c0700\n"
        "s_mul_i32 s74, s44, 3584\n"
        "s_add_u32 s74, s74, 4096\n"
        "v_mul_u32_u24 v34, 112, v61\n"
        "v_add_u32 v34, s74, v34\n"
        "v_lshl_add_u32 v251, v62, 4, v34\n"
        "v_lshl_add_u32 v252, v62, 3, v34\n"
        "s_lshl_b32 s74, s44, 9\n"
        "s_add_u32 s74, s74, 18432\n"
        "v_lshrrev_b32 v34, 3, v61\n"
        "v_lshlrev_b32 v34, 1, v34\n"
        "v_xor_b32 v34, v34, v62\n"
        "v_lshlrev_b32 v34, 3, v34\n"
        "v_lshl_add_u32 v34, v61, 5, v34\n"
        "v_add_u32 v254, s74, v34\n"
        "v_lshrrev_b32 v34, 3, v33\n"
        "v_lshlrev_b32 v34, 8, v34\n"
        "v_and_b32 v35, 7, v33\n"
        "v_lshl_or_b32 v34, v35, 4, v34\n"
        "s_waitcnt lgkmcnt(0)\n"
        "s_bitcmp1_b32 s44, 2\n"
        "s_cselect_b32 s74, s93, s92\n"
        "s_bitcmp1_b32 s44, 2\n"
        "s_cselect_b32 s75, s95, s94\n"
        "s_bitcmp1_b32 s44, 2\n"
        "s_cselect_b32 s76, s97, s96\n"
        "s_bitcmp1_b32 s44, 2\n"
        "s_cselect_b32 s77, s99, s98\n"
        "s_lshl_b32 s92, s74, 16\n"
        "s_and_b32 s74, s74, 0xffff0000\n"
        "s_lshl_b32 s93, s75, 16\n"
        "s_and_b32 s75, s75, 0xffff0000\n"
        "s_lshl_b32 s94, s76, 16\n"
        "s_and_b32 s76, s76, 0xffff0000\n"
        "s_lshl_b32 s95, s77, 16\n"
        "s_and_b32 s77, s77, 0xffff0000\n"
        "s_bitcmp1_b32 s44, 1\n"
        "s_cselect_b32 s74, s74, s92\n"
        "s_cselect_b32 s75, s75, s93\n"
        "s_cselect_b32 s76, s76, s94\n"
        "s_cselect_b32 s77, s77, s95\n"
        "s_waitcnt vmcnt(1)\n"
        "v_or_b32 v48, s74, v44\n"
        "v_or_b32 v49, s74, v44\n"
        "v_or_b32 v50, s74, v44\n"
        "v_or_b32 v51, s74, v44\n"
        "ds_write_b128 v34, v[48:51] offset:0\n"
        "s_nop 1\n"
        "v_or_b32 v48, s75, v44\n"
        "v_or_b32 v49, s75, v44\n"
        "v_or_b32 v50, s75, v44\n"
        "v_or_b32 v51, s75, v44\n"
        "ds_write_b128 v34, v[48:51] offset:16384\n"
        "s_nop 1\n"
        "v_or_b32 v48, s76, v44\n"
        "v_or_b32 v49, s76, v44\n"
        "v_or_b32 v50, s76, v44\n"
        "v_or_b32 v51, s76, v44\n"
        "ds_write_b128 v34, v[48:51] offset:32768\n"
        "s_nop 1\n"
        "v_or_b32 v48, s77, v44\n"
        "v_or_b32 v49, s77, v44\n"
        "v_or_b32 v50, s77, v44\n"
        "v_or_b32 v51, s77, v44\n"
        "ds_write_b128 v34, v[48:51] offset:49152\n"
        "s_mov_b32 s4, 0x55555555\n"
        "s_mov_b32 s5, 0x55555555\n"
        "v_readfirstlane_b32 s66, v42\n"
        "v_readfirstlane_b32 s67, v43\n"
        "s_mov_b32 s6, 0x33333333\n"
        "s_mov_b32 s7, 0x33333333\n"
        "s_nop 1\n"
        "s_add_u32 s64, s66, 1\n"
        "s_add_u32 s65, s67, 1\n"
        "s_waitcnt vmcnt(0)\n"
        "v_lshlrev_b32 v60, 16, v60\n"
        "v_bfe_u32 v34, v32, 2, 1\n"
        "v_lshlrev_b32 v34, 6, v34\n"
        "v_lshrrev_b32 v35, 3, v32\n"
        "v_lshl_add_u32 v34, v35, 4, v34\n"
        "v_and_b32 v35, 3, v32\n"
        "v_lshl_add_u32 v34, v35, 2, v34\n"
        "v_lshlrev_b32 v35, 2, v32\n"
        "v_add_u32 v35, 128, v35\n"
        "v_cmp_gt_u32 vcc, 32, v32\n"
        "s_nop 1\n"
        "v_cndmask_b32 v34, v35, v34, vcc\n"
        "s_lshl_b32 s74, s44, 9\n"
        "s_add_u32 s74, s74, 128\n"
        "v_add_u32 v34, s74, v34\n"
        "ds_write_b32 v34, v60\n"
        "s_waitcnt lgkmcnt(0)\n"
        "s_barrier\n"
        "s_sleep 4\n"
        "s_add_u32 s75, s56, 0\n"
        "buffer_load_dwordx4 v[120:123], v250, s[24:27], s75 offen sc1\n"
        "buffer_load_dwordx4 v[124:127], v250, s[24:27], s75 offen offset:1024 sc1\n"
        "s_add_u32 s76, s56, 8192\n"
        "buffer_load_dwordx4 v[136:139], v250, s[24:27], s76 offen sc1\n"
        "buffer_load_dwordx4 v[140:143], v250, s[24:27], s76 offen offset:1024 sc1\n"
        "s_setprio 2\n"
        "ds_read_b128 v[208:211], v244 offset:0\n"
        "ds_read_b128 v[212:215], v246 offset:0\n"
        "ds_read_b128 v[216:219], v244 offset:2048\n"
        "ds_read_b128 v[220:223], v246 offset:2048\n"
        "ds_read_b128 v[224:227], v244 offset:4096\n"
        "ds_read_b128 v[228:231], v246 offset:4096\n"
        "ds_read_b128 v[232:235], v244 offset:6144\n"
        "s_sleep 4\n"
        "s_add_u32 s77, s56, 16384\n"
        "buffer_load_dwordx4 v[152:155], v250, s[24:27], s77 offen sc1\n"
        "buffer_load_dwordx4 v[156:159], v250, s[24:27], s77 offen offset:1024 sc1\n"
        "s_add_u32 s74, s56, 24576\n"
        "buffer_load_dwordx4 v[168:171], v250, s[24:27], s74 offen sc1\n"
        "buffer_load_dwordx4 v[172:175], v250, s[24:27], s74 offen offset:1024 sc1\n"
        "s_waitcnt vmcnt(6)\n"
        "v_cmp_ne_u32 vcc, s64, v121\n"
        "v_cmp_ne_u32 s[78:79], s65, v123\n"
        "s_or_b64 vcc, vcc, s[78:79]\n"
        "v_cmp_ne_u32 s[78:79], s64, v125\n"
        "s_or_b64 vcc, vcc, s[78:79]\n"
        "v_cmp_ne_u32 s[78:79], s65, v127\n"
        "s_or_b64 vcc, vcc, s[78:79]\n"
        "s_cbranch_scc1 Ls15_%=\n"
        "Lk16_%=:\n"
        "s_barrier\n"
        "v_perm_b32 v240, v120, v248, s70\n"
        "v_perm_b32 v241, v120, v248, s71\n"
        "ds_read_b32 v112, v240\n"
        "v_perm_b32 v242, v120, v248, s72\n"
        "ds_read_b32 v113, v241\n"
        "v_perm_b32 v243, v120, v248, s73\n"
        "ds_read_b32 v114, v242\n"
        "v_perm_b32 v240, v122, v248, s70\n"
        "ds_read_b32 v115, v243\n"
        "v_perm_b32 v241, v122, v248, s71\n"
        "ds_read_b32 v116, v240\n"
        "v_perm_b32 v242, v122, v248, s72\n"
        "ds_read_b32 v117, v241\n"
        "v_perm_b32 v243, v122, v248, s73\n"
        "ds_read_b32 v118, v242\n"
        "v_perm_b32 v240, v124, v248, s70\n"
        "ds_read_b32 v119, v243\n"
        "v_perm_b32 v241, v124, v248, s71\n"
        "ds_read_b32 v120, v240\n"
        "v_perm_b32 v242, v124, v248, s72\n"
        "ds_read_b32 v121, v241\n"
        "v_perm_b32 v243, v124, v248, s73\n"
        "ds_read_b32 v122, v242\n"
        "v_perm_b32 v240, v126, v248, s70\n"
        "ds_read_b32 v123, v243\n"
        "v_perm_b32 v241, v126, v248, s71\n"
        "ds_read_b32 v124, v240\n"
        "v_perm_b32 v242, v126, v248, s73\n"
        "ds_read_b32 v125, v241\n"
        "v_perm_b32 v243, v126, v248, s72\n"
        "ds_read_b32 v127, v242\n"
        "ds_read_b32 v126, v243\n"
        "s_waitcnt lgkmcnt(15)\n"
        "s_waitcnt lgkmcnt(0)\n"
        "v_mfma_f32_16x16x32_bf16 v[0:3], v[112:115], v[208:211], 0\n"
        "s_waitcnt vmcnt(4)\n"
        "ds_read_b128 v[236:239], v246 offset:6144\n"
        "v_bitop3_b32 v128, v137, v141, s64 bitop3:0x7e\n"
        "v_mfma_f32_16x16x32_bf16 v[4:7], v[116:119], v[208:211], 0\n"
        "v_bitop3_b32 v129, v139, v143, s65 bitop3:0x7e\n"
        "v_mfma_f32_16x16x32_bf16 v[8:11], v[120:123], v[208:211], 0\n"
        "v_cmp_ne_u64 vcc, 0, v[128:129]\n"
        "v_mfma_f32_16x16x32_bf16 v[96:99], v[124:127], v[208:211], 0\n"
        "s_cbranch_vccnz Ls1_%=\n"
        "Lk2_%=:\n"
        "v_perm_b32 v240, v136, v248, s70\n"
        "v_mfma_f32_16x16x32_bf16 v[48:51], v[112:115], v[212:215], 0\n"
        "ds_read_b128 v[208:211], v245 offset:0\n"
        "v_perm_b32 v241, v136, v248, s71\n"
        "v_mfma_f32_16x16x32_bf16 v[52:55], v[116:119], v[212:215], 0\n"
        "ds_read_b32 v128, v240\n"
        "v_perm_b32 v242, v136, v248, s72\n"
        "v_mfma_f32_16x16x32_bf16 v[56:59], v[120:123], v[212:215], 0\n"
        "ds_read_b32 v129, v241\n"
        "v_perm_b32 v243, v136, v248, s73\n"
        "v_mfma_f32_16x16x32_bf16 v[12:15], v[112:115], v[216:219], 0\n"
        "ds_read_b128 v[212:215], v247 offset:0\n"
        "v_perm_b32 v240, v138, v248, s70\n"
        "v_mfma_f32_16x16x32_bf16 v[16:19], v[116:119], v[216:219], 0\n"
        "ds_read_b32 v130, v242\n"
        "v_perm_b32 v241, v138, v248, s71\n"
        "v_mfma_f32_16x16x32_bf16 v[20:23], v[120:123], v[216:219], 0\n"
        "ds_read_b32 v131, v243\n"
        "v_perm_b32 v242, v138, v248, s72\n"
        "v_mfma_f32_16x16x32_bf16 v[100:103], v[124:127], v[216:219], 0\n"
        "ds_read_b32 v132, v240\n"
        "v_perm_b32 v243, v138, v248, s73\n"
        "v_mfma_f32_16x16x32_bf16 v[60:63], v[112:115], v[220:223], 0\n"
        "ds_read_b128 v[216:219], v245 offset:2048\n"
        "v_perm_b32 v240, v140, v248, s70\n"
        "v_mfma_f32_16x16x32_bf16 v[64:67], v[116:119], v[220:223], 0\n"
        "ds_read_b32 v133, v241\n"
        "ds_read_b32 v134, v242\n"
        "v_mfma_f32_16x16x32_bf16 v[68:71], v[120:123], v[220:223], 0\n"
        "s_waitcnt lgkmcnt(10)\n"
        "ds_read_b32 v135, v243\n"
        "v_perm_b32 v241, v140, v248, s71\n"
        "v_mfma_f32_16x16x32_bf16 v[24:27], v[112:115], v[224:227], 0\n"
        "ds_read_b128 v[220:223], v247 offset:2048\n"
        "v_perm_b32 v242, v140, v248, s72\n"
        "v_mfma_f32_16x16x32_bf16 v[28:31], v[116:119], v[224:227], 0\n"
        "ds_read_b32 v136, v240\n"
        "v_perm_b32 v243, v140, v248, s73\n"
        "v_mfma_f32_16x16x32_bf16 v[32:35], v[120:123], v[224:227], 0\n"
        "ds_read_b32 v137, v241\n"
        "v_perm_b32 v240, v142, v248, s70\n"
        "v_mfma_f32_16x16x32_bf16 v[104:107], v[124:127], v[224:227], 0\n"
        "ds_read_b32 v138, v242\n"
        "v_perm_b32 v241, v142, v248, s71\n"
        "v_mfma_f32_16x16x32_bf16 v[72:75], v[112:115], v[228:231], 0\n"
        "ds_read_b128 v[224:227], v245 offset:4096\n"
        "v_perm_b32 v242, v142, v248, s73\n"
        "v_mfma_f32_16x16x32_bf16 v[76:79], v[116:119], v[228:231], 0\n"
        "ds_read_b32 v139, v243\n"
        "v_mfma_f32_16x16x32_bf16 v[80:83], v[120:123], v[228:231], 0\n"
        "ds_read_b32 v140, v240\n"
        "v_perm_b32 v243, v142, v248, s72\n"
        "v_mfma_f32_16x16x32_bf16 v[36:39], v[112:115], v[232:235], 0\n"
        "ds_read_b128 v[228:231], v247 offset:4096\n"
        "v_mfma_f32_16x16x32_bf16 v[40:43], v[116:119], v[232:235], 0\n"
        "ds_read_b32 v141, v241\n"
        "v_mfma_f32_16x16x32_bf16 v[44:47], v[120:123], v[232:235], 0\n"
        "ds_read_b32 v143, v242\n"
        "v_mfma_f32_16x16x32_bf16 v[108:111], v[124:127], v[232:235], 0\n"
        "ds_read_b32 v142, v243\n"
        "v_mfma_f32_16x16x32_bf16 v[84:87], v[112:115], v[236:239], 0\n"
        "s_waitcnt vmcnt(2)\n"
        "ds_read_b128 v[232:235], v245 offset:6144\n"
        "v_bitop3_b32 v144, v153, v157, s64 bitop3:0x7e\n"
        "v_mfma_f32_16x16x32_bf16 v[88:91], v[116:119], v[236:239], 0\n"
        "s_add_u32 s75, s56, 32768\n"
        "buffer_load_dwordx4 v[184:187], v250, s[24:27], s75 offen sc1\n"
        "v_bitop3_b32 v145, v155, v159, s65 bitop3:0x7e\n"
        "v_mfma_f32_16x16x32_bf16 v[92:95], v[120:123], v[236:239], 0\n"
        "s_waitcnt lgkmcnt(11)\n"
        "buffer_load_dwordx4 v[188:191], v250, s[24:27], s75 offen offset:1024 sc1\n"
        "v_cmp_ne_u64 vcc, 0, v[144:145]\n"
        "v_mfma_f32_16x16x32_bf16 v[0:3], v[128:131], v[208:211], v[0:3]\n"
        "s_cbranch_vccnz Ls3_%=\n"
        "Lk4_%=:\n"
        "ds_read_b128 v[236:239], v247 offset:6144\n"
        "v_perm_b32 v240, v152, v248, s70\n"
        "v_mfma_f32_16x16x32_bf16 v[4:7], v[132:135], v[208:211], v[4:7]\n"
        "s_waitcnt lgkmcnt(7)\n"
        "s_add_u32 s76, s56, 40960\n"
        "buffer_load_dwordx4 v[200:203], v250, s[24:27], s76 offen sc1\n"
        "v_perm_b32 v241, v152, v248, s71\n"
        "v_mfma_f32_16x16x32_bf16 v[8:11], v[136:139], v[208:211], v[8:11]\n"
        "s_waitcnt lgkmcnt(2)\n"
        "buffer_load_dwordx4 v[204:207], v250, s[24:27], s76 offen offset:1024 sc1\n"
        "v_perm_b32 v242, v152, v248, s72\n"
        "v_mfma_f32_16x16x32_bf16 v[96:99], v[140:143], v[208:211], v[96:99]\n"
        "s_barrier\n"
        "v_mfma_f32_16x16x32_bf16 v[48:51], v[128:131], v[212:215], v[48:51]\n"
        "ds_read_b128 v[208:211], v244 offset:16384\n"
        "v_perm_b32 v243, v152, v248, s73\n"
        "v_mfma_f32_16x16x32_bf16 v[52:55], v[132:135], v[212:215], v[52:55]\n"
        "ds_read_b32 v144, v240\n"
        "v_mfma_f32_16x16x32_bf16 v[56:59], v[136:139], v[212:215], v[56:59]\n"
        "ds_read_b32 v145, v241\n"
        "v_perm_b32 v240, v154, v248, s70\n"
        "v_mfma_f32_16x16x32_bf16 v[12:15], v[128:131], v[216:219], v[12:15]\n"
        "ds_read_b128 v[212:215], v246 offset:16384\n"
        "v_perm_b32 v241, v154, v248, s71\n"
        "v_mfma_f32_16x16x32_bf16 v[16:19], v[132:135], v[216:219], v[16:19]\n"
        "ds_read_b32 v146, v242\n"
        "v_mfma_f32_16x16x32_bf16 v[20:23], v[136:139], v[216:219], v[20:23]\n"
        "ds_read_b32 v147, v243\n"
        "v_perm_b32 v242, v154, v248, s72\n"
        "v_mfma_f32_16x16x32_bf16 v[100:103], v[140:143], v[216:219], v[100:103]\n"
        "ds_read_b32 v148, v240\n"
        "v_perm_b32 v243, v154, v248, s73\n"
        "v_mfma_f32_16x16x32_bf16 v[60:63], v[128:131], v[220:223], v[60:63]\n"
        "ds_read_b128 v[216:219], v244 offset:18432\n"
        "v_perm_b32 v240, v156, v248, s70\n"
        "v_mfma_f32_16x16x32_bf16 v[64:67], v[132:135], v[220:223], v[64:67]\n"
        "ds_read_b32 v149, v241\n"
        "v_mfma_f32_16x16x32_bf16 v[68:71], v[136:139], v[220:223], v[68:71]\n"
        "s_waitcnt lgkmcnt(9)\n"
        "ds_read_b32 v150, v242\n"
        "v_perm_b32 v241, v156, v248, s71\n"
        "v_mfma_f32_16x16x32_bf16 v[24:27], v[128:131], v[224:227], v[24:27]\n"
        "ds_read_b128 v[220:223], v246 offset:18432\n"
        "v_perm_b32 v242, v156, v248, s72\n"
        "v_mfma_f32_16x16x32_bf16 v[28:31], v[132:135], v[224:227], v[28:31]\n"
        "ds_read_b32 v151, v243\n"
        "v_mfma_f32_16x16x32_bf16 v[32:35], v[136:139], v[224:227], v[32:35]\n"
        "ds_read_b32 v152, v240\n"
        "v_perm_b32 v243, v156, v248, s73\n"
        "v_mfma_f32_16x16x32_bf16 v[104:107], v[140:143], v[224:227], v[104:107]\n"
        "ds_read_b32 v153, v241\n"
        "v_perm_b32 v240, v158, v248, s70\n"
        "v_mfma_f32_16x16x32_bf16 v[72:75], v[128:131], v[228:231], v[72:75]\n"
        "ds_read_b128 v[224:227], v244 offset:20480\n"
        "v_perm_b32 v241, v158, v248, s71\n"
        "v_mfma_f32_16x16x32_bf16 v[76:79], v[132:135], v[228:231], v[76:79]\n"
        "ds_read_b32 v154, v242\n"
        "v_mfma_f32_16x16x32_bf16 v[80:83], v[136:139], v[228:231], v[80:83]\n"
        "ds_read_b32 v155, v243\n"
        "v_perm_b32 v242, v158, v248, s73\n"
        "v_mfma_f32_16x16x32_bf16 v[36:39], v[128:131], v[232:235], v[36:39]\n"
        "ds_read_b128 v[228:231], v246 offset:20480\n"
        "v_perm_b32 v243, v158, v248, s72\n"
        "v_mfma_f32_16x16x32_bf16 v[40:43], v[132:135], v[232:235], v[40:43]\n"
        "ds_read_b32 v156, v240\n"
        "v_mfma_f32_16x16x32_bf16 v[44:47], v[136:139], v[232:235], v[44:47]\n"
        "ds_read_b32 v157, v241\n"
        "v_mfma_f32_16x16x32_bf16 v[108:111], v[140:143], v[232:235], v[108:111]\n"
        "ds_read_b32 v159, v242\n"
        "v_mfma_f32_16x16x32_bf16 v[84:87], v[128:131], v[236:239], v[84:87]\n"
        "s_waitcnt vmcnt(4)\n"
        "ds_read_b128 v[232:235], v244 offset:22528\n"
        "v_bitop3_b32 v160, v169, v173, s64 bitop3:0x7e\n"
        "v_mfma_f32_16x16x32_bf16 v[88:91], v[132:135], v[236:239], v[88:91]\n"
        "ds_read_b32 v158, v243\n"
        "v_bitop3_b32 v161, v171, v175, s65 bitop3:0x7e\n"
        "v_mfma_f32_16x16x32_bf16 v[92:95], v[136:139], v[236:239], v[92:95]\n"
        "s_waitcnt lgkmcnt(12)\n"
        "v_cmp_ne_u64 vcc, 0, v[160:161]\n"
        "v_mfma_f32_16x16x32_bf16 v[0:3], v[144:147], v[208:211], v[0:3]\n"
        "s_waitcnt lgkmcnt(11)\n"
        "s_cbranch_vccnz Ls5_%=\n"
        "Lk6_%=:\n"
        "ds_read_b128 v[236:239], v246 offset:22528\n"
        "v_perm_b32 v240, v168, v248, s70\n"
        "v_mfma_f32_16x16x32_bf16 v[4:7], v[148:151], v[208:211], v[4:7]\n"
        "s_waitcnt lgkmcnt(7)\n"
        "ds_read_b32 v160, v240\n"
        "v_perm_b32 v241, v168, v248, s71\n"
        "v_mfma_f32_16x16x32_bf16 v[8:11], v[152:155], v[208:211], v[8:11]\n"
        "s_waitcnt lgkmcnt(2)\n"
        "ds_read_b32 v161, v241\n"
        "v_perm_b32 v242, v168, v248, s72\n"
        "v_mfma_f32_16x16x32_bf16 v[96:99], v[156:159], v[208:211], v[96:99]\n"
        "ds_read_b32 v162, v242\n"
        "v_perm_b32 v243, v168, v248, s73\n"
        "v_mfma_f32_16x16x32_bf16 v[48:51], v[144:147], v[212:215], v[48:51]\n"
        "ds_read_b128 v[208:211], v245 offset:16384\n"
        "v_perm_b32 v240, v170, v248, s70\n"
        "v_mfma_f32_16x16x32_bf16 v[52:55], v[148:151], v[212:215], v[52:55]\n"
        "ds_read_b32 v163, v243\n"
        "v_perm_b32 v241, v170, v248, s71\n"
        "v_mfma_f32_16x16x32_bf16 v[56:59], v[152:155], v[212:215], v[56:59]\n"
        "ds_read_b32 v164, v240\n"
        "v_perm_b32 v242, v170, v248, s72\n"
        "v_mfma_f32_16x16x32_bf16 v[12:15], v[144:147], v[216:219], v[12:15]\n"
        "ds_read_b128 v[212:215], v247 offset:16384\n"
        "v_perm_b32 v243, v170, v248, s73\n"
        "v_mfma_f32_16x16x32_bf16 v[16:19], v[148:151], v[216:219], v[16:19]\n"
        "ds_read_b32 v165, v241\n"
        "v_perm_b32 v240, v172, v248, s70\n"
        "v_mfma_f32_16x16x32_bf16 v[20:23], v[152:155], v[216:219], v[20:23]\n"
        "ds_read_b32 v166, v242\n"
        "v_perm_b32 v241, v172, v248, s71\n"
        "v_mfma_f32_16x16x32_bf16 v[100:103], v[156:159], v[216:219], v[100:103]\n"
        "ds_read_b32 v167, v243\n"
        "v_perm_b32 v242, v172, v248, s72\n"
        "v_mfma_f32_16x16x32_bf16 v[60:63], v[144:147], v[220:223], v[60:63]\n"
        "ds_read_b128 v[216:219], v245 offset:18432\n"
        "v_perm_b32 v243, v172, v248, s73\n"
        "v_mfma_f32_16x16x32_bf16 v[64:67], v[148:151], v[220:223], v[64:67]\n"
        "ds_read_b32 v168, v240\n"
        "v_mfma_f32_16x16x32_bf16 v[68:71], v[152:155], v[220:223], v[68:71]\n"
        "s_waitcnt lgkmcnt(12)\n"
        "ds_read_b32 v169, v241\n"
        "v_perm_b32 v240, v174, v248, s70\n"
        "v_mfma_f32_16x16x32_bf16 v[24:27], v[144:147], v[224:227], v[24:27]\n"
        "ds_read_b128 v[220:223], v247 offset:18432\n"
        "v_perm_b32 v241, v174, v248, s71\n"
        "v_mfma_f32_16x16x32_bf16 v[28:31], v[148:151], v[224:227], v[28:31]\n"
        "ds_read_b32 v170, v242\n"
        "v_mfma_f32_16x16x32_bf16 v[32:35], v[152:155], v[224:227], v[32:35]\n"
        "ds_read_b32 v171, v243\n"
        "v_perm_b32 v242, v174, v248, s73\n"
        "v_mfma_f32_16x16x32_bf16 v[104:107], v[156:159], v[224:227], v[104:107]\n"
        "ds_read_b32 v172, v240\n"
        "v_perm_b32 v243, v174, v248, s72\n"
        "v_mfma_f32_16x16x32_bf16 v[72:75], v[144:147], v[228:231], v[72:75]\n"
        "ds_read_b128 v[224:227], v245 offset:20480\n"
        "v_mfma_f32_16x16x32_bf16 v[76:79], v[148:151], v[228:231], v[76:79]\n"
        "ds_read_b32 v173, v241\n"
        "v_mfma_f32_16x16x32_bf16 v[80:83], v[152:155], v[228:231], v[80:83]\n"
        "ds_read_b32 v175, v242\n"
        "v_mfma_f32_16x16x32_bf16 v[36:39], v[144:147], v[232:235], v[36:39]\n"
        "ds_read_b128 v[228:231], v247 offset:20480\n"
        "v_mfma_f32_16x16x32_bf16 v[40:43], v[148:151], v[232:235], v[40:43]\n"
        "ds_read_b32 v174, v243\n"
        "v_mfma_f32_16x16x32_bf16 v[44:47], v[152:155], v[232:235], v[44:47]\n"
        "v_mfma_f32_16x16x32_bf16 v[108:111], v[156:159], v[232:235], v[108:111]\n"
        "v_mfma_f32_16x16x32_bf16 v[84:87], v[144:147], v[236:239], v[84:87]\n"
        "s_waitcnt vmcnt(2)\n"
        "ds_read_b128 v[232:235], v245 offset:22528\n"
        "v_bitop3_b32 v176, v185, v189, s64 bitop3:0x7e\n"
        "v_mfma_f32_16x16x32_bf16 v[88:91], v[148:151], v[236:239], v[88:91]\n"
        "s_add_u32 s77, s56, 49152\n"
        "buffer_load_dwordx4 v[120:123], v250, s[24:27], s77 offen sc1\n"
        "v_bitop3_b32 v177, v187, v191, s65 bitop3:0x7e\n"
        "v_mfma_f32_16x16x32_bf16 v[92:95], v[152:155], v[236:239], v[92:95]\n"
        "s_waitcnt lgkmcnt(9)\n"
        "buffer_load_dwordx4 v[124:127], v250, s[24:27], s77 offen offset:1024 sc1\n"
        "v_cmp_ne_u64 vcc, 0, v[176:177]\n"
        "v_mfma_f32_16x16x32_bf16 v[0:3], v[160:163], v[208:211], v[0:3]\n"
        "s_cbranch_vccnz Ls7_%=\n"
        "Lk8_%=:\n"
        "ds_read_b128 v[236:239], v247 offset:22528\n"
        "v_perm_b32 v240, v184, v248, s70\n"
        "v_mfma_f32_16x16x32_bf16 v[4:7], v[164:167], v[208:211], v[4:7]\n"
        "s_waitcnt lgkmcnt(8)\n"
        "s_add_u32 s74, s56, 57344\n"
        "buffer_load_dwordx4 v[136:139], v250, s[24:27], s74 offen sc1\n"
        "v_perm_b32 v241, v184, v248, s71\n"
        "v_mfma_f32_16x16x32_bf16 v[8:11], v[168:171], v[208:211], v[8:11]\n"
        "s_waitcnt lgkmcnt(2)\n"
        "buffer_load_dwordx4 v[140:143], v250, s[24:27], s74 offen offset:1024 sc1\n"
        "v_perm_b32 v242, v184, v248, s72\n"
        "v_mfma_f32_16x16x32_bf16 v[96:99], v[172:175], v[208:211], v[96:99]\n"
        "s_barrier\n"
        "v_mfma_f32_16x16x32_bf16 v[48:51], v[160:163], v[212:215], v[48:51]\n"
        "ds_read_b128 v[208:211], v244 offset:32768\n"
        "v_perm_b32 v243, v184, v248, s73\n"
        "v_mfma_f32_16x16x32_bf16 v[52:55], v[164:167], v[212:215], v[52:55]\n"
        "ds_read_b32 v176, v240\n"
        "v_mfma_f32_16x16x32_bf16 v[56:59], v[168:171], v[212:215], v[56:59]\n"
        "ds_read_b32 v177, v241\n"
        "v_perm_b32 v240, v186, v248, s70\n"
        "v_mfma_f32_16x16x32_bf16 v[12:15], v[160:163], v[216:219], v[12:15]\n"
        "ds_read_b128 v[212:215], v246 offset:32768\n"
        "v_perm_b32 v241, v186, v248, s71\n"
        "v_mfma_f32_16x16x32_bf16 v[16:19], v[164:167], v[216:219], v[16:19]\n"
        "ds_read_b32 v178, v242\n"
        "v_mfma_f32_16x16x32_bf16 v[20:23], v[168:171], v[216:219], v[20:23]\n"
        "ds_read_b32 v179, v243\n"
        "v_perm_b32 v242, v186, v248, s72\n"
        "v_mfma_f32_16x16x32_bf16 v[100:103], v[172:175], v[216:219], v[100:103]\n"
        "ds_read_b32 v180, v240\n"
        "v_perm_b32 v243, v186, v248, s73\n"
        "v_mfma_f32_16x16x32_bf16 v[60:63], v[160:163], v[220:223], v[60:63]\n"
        "ds_read_b128 v[216:219], v244 offset:34816\n"
        "v_perm_b32 v240, v188, v248, s70\n"
        "v_mfma_f32_16x16x32_bf16 v[64:67], v[164:167], v[220:223], v[64:67]\n"
        "ds_read_b32 v181, v241\n"
        "v_mfma_f32_16x16x32_bf16 v[68:71], v[168:171], v[220:223], v[68:71]\n"
        "s_waitcnt lgkmcnt(9)\n"
        "ds_read_b32 v182, v242\n"
        "v_perm_b32 v241, v188, v248, s71\n"
        "v_mfma_f32_16x16x32_bf16 v[24:27], v[160:163], v[224:227], v[24:27]\n"
        "ds_read_b128 v[220:223], v246 offset:34816\n"
        "v_perm_b32 v242, v188, v248, s72\n"
        "v_mfma_f32_16x16x32_bf16 v[28:31], v[164:167], v[224:227], v[28:31]\n"
        "ds_read_b32 v183, v243\n"
        "v_mfma_f32_16x16x32_bf16 v[32:35], v[168:171], v[224:227], v[32:35]\n"
        "ds_read_b32 v184, v240\n"
        "v_perm_b32 v243, v188, v248, s73\n"
        "v_mfma_f32_16x16x32_bf16 v[104:107], v[172:175], v[224:227], v[104:107]\n"
        "ds_read_b32 v185, v241\n"
        "v_perm_b32 v240, v190, v248, s70\n"
        "v_mfma_f32_16x16x32_bf16 v[72:75], v[160:163], v[228:231], v[72:75]\n"
        "ds_read_b128 v[224:227], v244 offset:36864\n"
        "v_perm_b32 v241, v190, v248, s71\n"
        "v_mfma_f32_16x16x32_bf16 v[76:79], v[164:167], v[228:231], v[76:79]\n"
        "ds_read_b32 v186, v242\n"
        "v_mfma_f32_16x16x32_bf16 v[80:83], v[168:171], v[228:231], v[80:83]\n"
        "ds_read_b32 v187, v243\n"
        "v_perm_b32 v242, v190, v248, s73\n"
        "v_mfma_f32_16x16x32_bf16 v[36:39], v[160:163], v[232:235], v[36:39]\n"
        "ds_read_b128 v[228:231], v246 offset:36864\n"
        "v_perm_b32 v243, v190, v248, s72\n"
        "v_mfma_f32_16x16x32_bf16 v[40:43], v[164:167], v[232:235], v[40:43]\n"
        "ds_read_b32 v188, v240\n"
        "v_mfma_f32_16x16x32_bf16 v[44:47], v[168:171], v[232:235], v[44:47]\n"
        "ds_read_b32 v189, v241\n"
        "v_mfma_f32_16x16x32_bf16 v[108:111], v[172:175], v[232:235], v[108:111]\n"
        "ds_read_b32 v191, v242\n"
        "v_mfma_f32_16x16x32_bf16 v[84:87], v[160:163], v[236:239], v[84:87]\n"
        "s_waitcnt vmcnt(4)\n"
        "ds_read_b128 v[232:235], v244 offset:38912\n"
        "v_bitop3_b32 v192, v201, v205, s64 bitop3:0x7e\n"
        "v_mfma_f32_16x16x32_bf16 v[88:91], v[164:167], v[236:239], v[88:91]\n"
        "ds_read_b32 v190, v243\n"
        "v_bitop3_b32 v193, v203, v207, s65 bitop3:0x7e\n"
        "v_mfma_f32_16x16x32_bf16 v[92:95], v[168:171], v[236:239], v[92:95]\n"
        "s_waitcnt lgkmcnt(12)\n"
        "v_cmp_ne_u64 vcc, 0, v[192:193]\n"
        "v_mfma_f32_16x16x32_bf16 v[0:3], v[176:179], v[208:211], v[0:3]\n"
        "s_waitcnt lgkmcnt(11)\n"
        "s_cbranch_vccnz Ls9_%=\n"
        "Lk10_%=:\n"
        "ds_read_b128 v[236:239], v246 offset:38912\n"
        "v_perm_b32 v240, v200, v248, s70\n"
        "v_mfma_f32_16x16x32_bf16 v[4:7], v[180:183], v[208:211], v[4:7]\n"
        "s_waitcnt lgkmcnt(7)\n"
        "ds_read_b32 v192, v240\n"
        "v_perm_b32 v241, v200, v248, s71\n"
        "v_mfma_f32_16x16x32_bf16 v[8:11], v[184:187], v[208:211], v[8:11]\n"
        "s_waitcnt lgkmcnt(2)\n"
        "ds_read_b32 v193, v241\n"
        "v_perm_b32 v242, v200, v248, s72\n"
        "v_mfma_f32_16x16x32_bf16 v[96:99], v[188:191], v[208:211], v[96:99]\n"
        "ds_read_b32 v194, v242\n"
        "v_perm_b32 v243, v200, v248, s73\n"
        "v_mfma_f32_16x16x32_bf16 v[48:51], v[176:179], v[212:215], v[48:51]\n"
        "ds_read_b128 v[208:211], v245 offset:32768\n"
        "v_perm_b32 v240, v202, v248, s70\n"
        "v_mfma_f32_16x16x32_bf16 v[52:55], v[180:183], v[212:215], v[52:55]\n"
        "ds_read_b32 v195, v243\n"
        "v_perm_b32 v241, v202, v248, s71\n"
        "v_mfma_f32_16x16x32_bf16 v[56:59], v[184:187], v[212:215], v[56:59]\n"
        "ds_read_b32 v196, v240\n"
        "v_perm_b32 v242, v202, v248, s72\n"
        "v_mfma_f32_16x16x32_bf16 v[12:15], v[176:179], v[216:219], v[12:15]\n"
        "ds_read_b128 v[212:215], v247 offset:32768\n"
        "v_perm_b32 v243, v202, v248, s73\n"
        "v_mfma_f32_16x16x32_bf16 v[16:19], v[180:183], v[216:219], v[16:19]\n"
        "ds_read_b32 v197, v241\n"
        "v_perm_b32 v240, v204, v248, s70\n"
        "v_mfma_f32_16x16x32_bf16 v[20:23], v[184:187], v[216:219], v[20:23]\n"
        "ds_read_b32 v198, v242\n"
        "v_perm_b32 v241, v204, v248, s71\n"
        "v_mfma_f32_16x16x32_bf16 v[100:103], v[188:191], v[216:219], v[100:103]\n"
        "ds_read_b32 v199, v243\n"
        "v_perm_b32 v242, v204, v248, s72\n"
        "v_mfma_f32_16x16x32_bf16 v[60:63], v[176:179], v[220:223], v[60:63]\n"
        "ds_read_b128 v[216:219], v245 offset:34816\n"
        "v_perm_b32 v243, v204, v248, s73\n"
        "v_mfma_f32_16x16x32_bf16 v[64:67], v[180:183], v[220:223], v[64:67]\n"
        "ds_read_b32 v200, v240\n"
        "v_mfma_f32_16x16x32_bf16 v[68:71], v[184:187], v[220:223], v[68:71]\n"
        "s_waitcnt lgkmcnt(12)\n"
        "ds_read_b32 v201, v241\n"
        "v_perm_b32 v240, v206, v248, s70\n"
        "v_mfma_f32_16x16x32_bf16 v[24:27], v[176:179], v[224:227], v[24:27]\n"
        "ds_read_b128 v[220:223], v247 offset:34816\n"
        "v_perm_b32 v241, v206, v248, s71\n"
        "v_mfma_f32_16x16x32_bf16 v[28:31], v[180:183], v[224:227], v[28:31]\n"
        "ds_read_b32 v202, v242\n"
        "v_mfma_f32_16x16x32_bf16 v[32:35], v[184:187], v[224:227], v[32:35]\n"
        "ds_read_b32 v203, v243\n"
        "v_perm_b32 v242, v206, v248, s73\n"
        "v_mfma_f32_16x16x32_bf16 v[104:107], v[188:191], v[224:227], v[104:107]\n"
        "ds_read_b32 v204, v240\n"
        "v_perm_b32 v243, v206, v248, s72\n"
        "v_mfma_f32_16x16x32_bf16 v[72:75], v[176:179], v[228:231], v[72:75]\n"
        "ds_read_b128 v[224:227], v245 offset:36864\n"
        "v_mfma_f32_16x16x32_bf16 v[76:79], v[180:183], v[228:231], v[76:79]\n"
        "ds_read_b32 v205, v241\n"
        "v_mfma_f32_16x16x32_bf16 v[80:83], v[184:187], v[228:231], v[80:83]\n"
        "ds_read_b32 v207, v242\n"
        "v_mfma_f32_16x16x32_bf16 v[36:39], v[176:179], v[232:235], v[36:39]\n"
        "ds_read_b128 v[228:231], v247 offset:36864\n"
        "v_mfma_f32_16x16x32_bf16 v[40:43], v[180:183], v[232:235], v[40:43]\n"
        "ds_read_b32 v206, v243\n"
        "v_mfma_f32_16x16x32_bf16 v[44:47], v[184:187], v[232:235], v[44:47]\n"
        "v_mfma_f32_16x16x32_bf16 v[108:111], v[188:191], v[232:235], v[108:111]\n"
        "v_mfma_f32_16x16x32_bf16 v[84:87], v[176:179], v[236:239], v[84:87]\n"
        "s_waitcnt vmcnt(2)\n"
        "ds_read_b128 v[232:235], v245 offset:38912\n"
        "v_bitop3_b32 v112, v121, v125, s64 bitop3:0x7e\n"
        "v_mfma_f32_16x16x32_bf16 v[88:91], v[180:183], v[236:239], v[88:91]\n"
        "v_bitop3_b32 v113, v123, v127, s65 bitop3:0x7e\n"
        "v_mfma_f32_16x16x32_bf16 v[92:95], v[184:187], v[236:239], v[92:95]\n"
        "s_waitcnt lgkmcnt(9)\n"
        "v_cmp_ne_u64 vcc, 0, v[112:113]\n"
        "v_mfma_f32_16x16x32_bf16 v[0:3], v[192:195], v[208:211], v[0:3]\n"
        "s_cbranch_vccnz Ls11_%=\n"
        "Lk12_%=:\n"
        "ds_read_b128 v[236:239], v247 offset:38912\n"
        "v_perm_b32 v240, v120, v248, s70\n"
        "v_mfma_f32_16x16x32_bf16 v[4:7], v[196:199], v[208:211], v[4:7]\n"
        "s_waitcnt lgkmcnt(8)\n"
        "ds_read_b32 v112, v240\n"
        "v_perm_b32 v241, v120, v248, s71\n"
        "v_mfma_f32_16x16x32_bf16 v[8:11], v[200:203], v[208:211], v[8:11]\n"
        "s_waitcnt lgkmcnt(3)\n"
        "ds_read_b32 v113, v241\n"
        "v_perm_b32 v242, v120, v248, s72\n"
        "v_mfma_f32_16x16x32_bf16 v[96:99], v[204:207], v[208:211], v[96:99]\n"
        "s_barrier\n"
        "v_mfma_f32_16x16x32_bf16 v[48:51], v[192:195], v[212:215], v[48:51]\n"
        "ds_read_b128 v[208:211], v244 offset:49152\n"
        "v_perm_b32 v243, v120, v248, s73\n"
        "v_mfma_f32_16x16x32_bf16 v[52:55], v[196:199], v[212:215], v[52:55]\n"
        "ds_read_b32 v114, v242\n"
        "v_perm_b32 v240, v122, v248, s70\n"
        "v_mfma_f32_16x16x32_bf16 v[56:59], v[200:203], v[212:215], v[56:59]\n"
        "ds_read_b32 v115, v243\n"
        "v_perm_b32 v241, v122, v248, s71\n"
        "v_mfma_f32_16x16x32_bf16 v[12:15], v[192:195], v[216:219], v[12:15]\n"
        "ds_read_b128 v[212:215], v246 offset:49152\n"
        "v_perm_b32 v242, v122, v248, s72\n"
        "v_mfma_f32_16x16x32_bf16 v[16:19], v[196:199], v[216:219], v[16:19]\n"
        "ds_read_b32 v116, v240\n"
        "v_perm_b32 v243, v122, v248, s73\n"
        "v_mfma_f32_16x16x32_bf16 v[20:23], v[200:203], v[216:219], v[20:23]\n"
        "ds_read_b32 v117, v241\n"
        "v_perm_b32 v240, v124, v248, s70\n"
        "v_mfma_f32_16x16x32_bf16 v[100:103], v[204:207], v[216:219], v[100:103]\n"
        "ds_read_b32 v118, v242\n"
        "v_perm_b32 v241, v124, v248, s71\n"
        "v_mfma_f32_16x16x32_bf16 v[60:63], v[192:195], v[220:223], v[60:63]\n"
        "ds_read_b128 v[216:219], v244 offset:51200\n"
        "v_perm_b32 v242, v124, v248, s72\n"
        "v_mfma_f32_16x16x32_bf16 v[64:67], v[196:199], v[220:223], v[64:67]\n"
        "ds_read_b32 v119, v243\n"
        "v_mfma_f32_16x16x32_bf16 v[68:71], v[200:203], v[220:223], v[68:71]\n"
        "s_waitcnt lgkmcnt(11)\n"
        "ds_read_b32 v120, v240\n"
        "v_perm_b32 v243, v124, v248, s73\n"
        "v_mfma_f32_16x16x32_bf16 v[24:27], v[192:195], v[224:227], v[24:27]\n"
        "ds_read_b128 v[220:223], v246 offset:51200\n"
        "v_perm_b32 v240, v126, v248, s70\n"
        "v_mfma_f32_16x16x32_bf16 v[28:31], v[196:199], v[224:227], v[28:31]\n"
        "ds_read_b32 v121, v241\n"
        "v_mfma_f32_16x16x32_bf16 v[32:35], v[200:203], v[224:227], v[32:35]\n"
        "ds_read_b32 v122, v242\n"
        "v_perm_b32 v241, v126, v248, s71\n"
        "v_mfma_f32_16x16x32_bf16 v[104:107], v[204:207], v[224:227], v[104:107]\n"
        "ds_read_b32 v123, v243\n"
        "v_perm_b32 v242, v126, v248, s73\n"
        "v_mfma_f32_16x16x32_bf16 v[72:75], v[192:195], v[228:231], v[72:75]\n"
        "ds_read_b128 v[224:227], v244 offset:53248\n"
        "v_perm_b32 v243, v126, v248, s72\n"
        "v_mfma_f32_16x16x32_bf16 v[76:79], v[196:199], v[228:231], v[76:79]\n"
        "ds_read_b32 v124, v240\n"
        "v_mfma_f32_16x16x32_bf16 v[80:83], v[200:203], v[228:231], v[80:83]\n"
        "ds_read_b32 v125, v241\n"
        "v_mfma_f32_16x16x32_bf16 v[36:39], v[192:195], v[232:235], v[36:39]\n"
        "ds_read_b128 v[228:231], v246 offset:53248\n"
        "v_mfma_f32_16x16x32_bf16 v[40:43], v[196:199], v[232:235], v[40:43]\n"
        "ds_read_b32 v127, v242\n"
        "v_mfma_f32_16x16x32_bf16 v[44:47], v[200:203], v[232:235], v[44:47]\n"
        "ds_read_b32 v126, v243\n"
        "v_mfma_f32_16x16x32_bf16 v[108:111], v[204:207], v[232:235], v[108:111]\n"
        "v_mfma_f32_16x16x32_bf16 v[84:87], v[192:195], v[236:239], v[84:87]\n"
        "s_waitcnt vmcnt(0)\n"
        "ds_read_b128 v[232:235], v244 offset:55296\n"
        "v_bitop3_b32 v128, v137, v141, s64 bitop3:0x7e\n"
        "v_mfma_f32_16x16x32_bf16 v[88:91], v[196:199], v[236:239], v[88:91]\n"
        "v_bitop3_b32 v129, v139, v143, s65 bitop3:0x7e\n"
        "v_mfma_f32_16x16x32_bf16 v[92:95], v[200:203], v[236:239], v[92:95]\n"
        "s_waitcnt lgkmcnt(10)\n"
        "v_cmp_ne_u64 vcc, 0, v[128:129]\n"
        "v_mfma_f32_16x16x32_bf16 v[0:3], v[112:115], v[208:211], v[0:3]\n"
        "s_cbranch_vccnz Ls13_%=\n"
        "Lk14_%=:\n"
        "s_lshl_b32 s90, s44, 9\n"
        "ds_read_b128 v[236:239], v246 offset:55296\n"
        "v_bfe_u32 v250, v249, 10, 2\n"
        "v_mfma_f32_16x16x32_bf16 v[4:7], v[116:119], v[208:211], v[4:7]\n"
        "s_waitcnt lgkmcnt(8)\n"
        "v_lshl_add_u32 v250, v250, 4, s90\n"
        "v_mfma_f32_16x16x32_bf16 v[8:11], v[120:123], v[208:211], v[8:11]\n"
        "s_waitcnt lgkmcnt(2)\n"
        "v_perm_b32 v240, v136, v248, s70\n"
        "v_mfma_f32_16x16x32_bf16 v[96:99], v[124:127], v[208:211], v[96:99]\n"
        "ds_read_b32 v128, v240\n"
        "v_perm_b32 v241, v136, v248, s71\n"
        "v_mfma_f32_16x16x32_bf16 v[48:51], v[112:115], v[212:215], v[48:51]\n"
        "ds_read_b128 v[208:211], v245 offset:49152\n"
        "v_perm_b32 v242, v136, v248, s72\n"
        "v_mfma_f32_16x16x32_bf16 v[52:55], v[116:119], v[212:215], v[52:55]\n"
        "ds_read_b32 v129, v241\n"
        "v_perm_b32 v243, v136, v248, s73\n"
        "v_mfma_f32_16x16x32_bf16 v[56:59], v[120:123], v[212:215], v[56:59]\n"
        "ds_read_b32 v130, v242\n"
        "v_perm_b32 v240, v138, v248, s70\n"
        "v_mfma_f32_16x16x32_bf16 v[12:15], v[112:115], v[216:219], v[12:15]\n"
        "ds_read_b128 v[212:215], v247 offset:49152\n"
        "v_perm_b32 v241, v138, v248, s71\n"
        "v_mfma_f32_16x16x32_bf16 v[16:19], v[116:119], v[216:219], v[16:19]\n"
        "ds_read_b32 v131, v243\n"
        "v_perm_b32 v242, v138, v248, s72\n"
        "v_mfma_f32_16x16x32_bf16 v[20:23], v[120:123], v[216:219], v[20:23]\n"
        "ds_read_b32 v132, v240\n"
        "v_perm_b32 v243, v138, v248, s73\n"
        "v_mfma_f32_16x16x32_bf16 v[100:103], v[124:127], v[216:219], v[100:103]\n"
        "ds_read_b32 v133, v241\n"
        "v_perm_b32 v240, v140, v248, s70\n"
        "v_mfma_f32_16x16x32_bf16 v[60:63], v[112:115], v[220:223], v[60:63]\n"
        "ds_read_b128 v[216:219], v245 offset:51200\n"
        "v_perm_b32 v241, v140, v248, s71\n"
        "v_mfma_f32_16x16x32_bf16 v[64:67], v[116:119], v[220:223], v[64:67]\n"
        "ds_read_b32 v134, v242\n"
        "v_mfma_f32_16x16x32_bf16 v[68:71], v[120:123], v[220:223], v[68:71]\n"
        "s_waitcnt lgkmcnt(10)\n"
        "ds_read_b32 v135, v243\n"
        "v_perm_b32 v242, v140, v248, s72\n"
        "v_mfma_f32_16x16x32_bf16 v[24:27], v[112:115], v[224:227], v[24:27]\n"
        "ds_read_b128 v[220:223], v247 offset:51200\n"
        "v_perm_b32 v243, v140, v248, s73\n"
        "v_mfma_f32_16x16x32_bf16 v[28:31], v[116:119], v[224:227], v[28:31]\n"
        "ds_read_b32 v136, v240\n"
        "v_mfma_f32_16x16x32_bf16 v[32:35], v[120:123], v[224:227], v[32:35]\n"
        "ds_read_b32 v137, v241\n"
        "v_perm_b32 v240, v142, v248, s70\n"
        "v_mfma_f32_16x16x32_bf16 v[104:107], v[124:127], v[224:227], v[104:107]\n"
        "ds_read_b32 v138, v242\n"
        "v_perm_b32 v241, v142, v248, s71\n"
        "v_mfma_f32_16x16x32_bf16 v[72:75], v[112:115], v[228:231], v[72:75]\n"
        "ds_read_b128 v[224:227], v245 offset:53248\n"
        "v_perm_b32 v242, v142, v248, s73\n"
        "v_mfma_f32_16x16x32_bf16 v[76:79], v[116:119], v[228:231], v[76:79]\n"
        "ds_read_b32 v139, v243\n"
        "v_mfma_f32_16x16x32_bf16 v[80:83], v[120:123], v[228:231], v[80:83]\n"
        "ds_read_b32 v140, v240\n"
        "v_perm_b32 v243, v142, v248, s72\n"
        "v_mfma_f32_16x16x32_bf16 v[36:39], v[112:115], v[232:235], v[36:39]\n"
        "ds_read_b128 v[228:231], v247 offset:53248\n"
        "v_mfma_f32_16x16x32_bf16 v[40:43], v[116:119], v[232:235], v[40:43]\n"
        "ds_read_b32 v141, v241\n"
        "v_mfma_f32_16x16x32_bf16 v[44:47], v[120:123], v[232:235], v[44:47]\n"
        "ds_read_b32 v143, v242\n"
        "v_mfma_f32_16x16x32_bf16 v[108:111], v[124:127], v[232:235], v[108:111]\n"
        "ds_read_b32 v142, v243\n"
        "v_mfma_f32_16x16x32_bf16 v[84:87], v[112:115], v[236:239], v[84:87]\n"
        "ds_read_b128 v[232:235], v245 offset:55296\n"
        "v_mfma_f32_16x16x32_bf16 v[88:91], v[116:119], v[236:239], v[88:91]\n"
        "v_mfma_f32_16x16x32_bf16 v[92:95], v[120:123], v[236:239], v[92:95]\n"
        "s_waitcnt lgkmcnt(11)\n"
        "v_mfma_f32_16x16x32_bf16 v[0:3], v[128:131], v[208:211], v[0:3]\n"
        "ds_read_b128 v[236:239], v247 offset:55296\n"
        "v_mfma_f32_16x16x32_bf16 v[4:7], v[132:135], v[208:211], v[4:7]\n"
        "s_waitcnt lgkmcnt(7)\n"
        "v_mfma_f32_16x16x32_bf16 v[8:11], v[136:139], v[208:211], v[8:11]\n"
        "s_waitcnt lgkmcnt(2)\n"
        "v_mfma_f32_16x16x32_bf16 v[96:99], v[140:143], v[208:211], v[96:99]\n"
        "s_barrier\n"
        "v_mfma_f32_16x16x32_bf16 v[48:51], v[128:131], v[212:215], v[48:51]\n"
        "ds_read_b128 v[208:211], v244 offset:0\n"
        "v_mfma_f32_16x16x32_bf16 v[52:55], v[132:135], v[212:215], v[52:55]\n"
        "ds_read_b128 v[156:159], v249 offset:0\n"
        "v_mfma_f32_16x16x32_bf16 v[56:59], v[136:139], v[212:215], v[56:59]\n"
        "v_mfma_f32_16x16x32_bf16 v[12:15], v[128:131], v[216:219], v[12:15]\n"
        "ds_read_b128 v[212:215], v246 offset:0\n"
        "v_mfma_f32_16x16x32_bf16 v[16:19], v[132:135], v[216:219], v[16:19]\n"
        "v_mfma_f32_16x16x32_bf16 v[20:23], v[136:139], v[216:219], v[20:23]\n"
        "v_mfma_f32_16x16x32_bf16 v[100:103], v[140:143], v[216:219], v[100:103]\n"
        "v_mfma_f32_16x16x32_bf16 v[60:63], v[128:131], v[220:223], v[60:63]\n"
        "s_waitcnt lgkmcnt(1)\n"
        "ds_read_b128 v[216:219], v244 offset:2048\n"
        "v_perm_b32 v240, v156, v248, s70\n"
        "v_mfma_f32_16x16x32_bf16 v[64:67], v[132:135], v[220:223], v[64:67]\n"
        "ds_read_b32 v144, v240\n"
        "v_perm_b32 v241, v156, v248, s71\n"
        "v_perm_b32 v242, v156, v248, s72\n"
        "v_mfma_f32_16x16x32_bf16 v[68:71], v[136:139], v[220:223], v[68:71]\n"
        "ds_read_b32 v145, v241\n"
        "v_perm_b32 v243, v156, v248, s73\n"
        "v_mfma_f32_16x16x32_bf16 v[24:27], v[128:131], v[224:227], v[24:27]\n"
        "ds_read_b128 v[220:223], v246 offset:2048\n"
        "ds_read_b32 v146, v242\n"
        "v_perm_b32 v240, v157, v248, s70\n"
        "v_mfma_f32_16x16x32_bf16 v[28:31], v[132:135], v[224:227], v[28:31]\n"
        "ds_read_b32 v147, v243\n"
        "v_perm_b32 v241, v157, v248, s71\n"
        "v_mfma_f32_16x16x32_bf16 v[32:35], v[136:139], v[224:227], v[32:35]\n"
        "ds_read_b32 v148, v240\n"
        "ds_read_b32 v149, v241\n"
        "v_perm_b32 v242, v157, v248, s72\n"
        "v_mfma_f32_16x16x32_bf16 v[104:107], v[140:143], v[224:227], v[104:107]\n"
        "ds_read_b32 v150, v242\n"
        "v_perm_b32 v243, v157, v248, s73\n"
        "v_mfma_f32_16x16x32_bf16 v[72:75], v[128:131], v[228:231], v[72:75]\n"
        "ds_read_b128 v[224:227], v244 offset:4096\n"
        "ds_read_b32 v151, v243\n"
        "v_perm_b32 v240, v158, v248, s70\n"
        "v_mfma_f32_16x16x32_bf16 v[76:79], v[132:135], v[228:231], v[76:79]\n"
        "ds_read_b128 v[172:175], v249 offset:4096\n"
        "v_perm_b32 v241, v158, v248, s71\n"
        "v_mfma_f32_16x16x32_bf16 v[80:83], v[136:139], v[228:231], v[80:83]\n"
        "ds_read_b32 v152, v240\n"
        "ds_read_b32 v153, v241\n"
        "v_perm_b32 v242, v158, v248, s72\n"
        "v_mfma_f32_16x16x32_bf16 v[36:39], v[128:131], v[232:235], v[36:39]\n"
        "ds_read_b128 v[228:231], v246 offset:4096\n"
        "v_perm_b32 v243, v158, v248, s73\n"
        "v_mfma_f32_16x16x32_bf16 v[40:43], v[132:135], v[232:235], v[40:43]\n"
        "ds_read_b32 v154, v242\n"
        "ds_read_b32 v155, v243\n"
        "v_perm_b32 v240, v159, v248, s70\n"
        "v_mfma_f32_16x16x32_bf16 v[44:47], v[136:139], v[232:235], v[44:47]\n"
        "ds_read_b32 v156, v240\n"
        "v_perm_b32 v241, v159, v248, s71\n"
        "v_mfma_f32_16x16x32_bf16 v[108:111], v[140:143], v[232:235], v[108:111]\n"
        "ds_read_b32 v157, v241\n"
        "v_perm_b32 v242, v159, v248, s72\n"
        "v_mfma_f32_16x16x32_bf16 v[84:87], v[128:131], v[236:239], v[84:87]\n"
        "ds_read_b128 v[232:235], v244 offset:6144\n"
        "v_perm_b32 v243, v159, v248, s73\n"
        "v_mfma_f32_16x16x32_bf16 v[88:91], v[132:135], v[236:239], v[88:91]\n"
        "s_waitcnt lgkmcnt(8)\n"
        "ds_read_b32 v158, v242\n"
        "ds_read_b32 v159, v243\n"
        "v_perm_b32 v240, v172, v248, s70\n"
        "v_mfma_f32_16x16x32_bf16 v[92:95], v[136:139], v[236:239], v[92:95]\n"
        "ds_read_b32 v160, v240\n"
        "v_perm_b32 v241, v172, v248, s71\n"
        "v_mfma_f32_16x16x32_bf16 v[0:3], v[144:147], v[208:211], v[0:3]\n"
        "ds_read_b128 v[236:239], v246 offset:6144\n"
        "v_perm_b32 v242, v172, v248, s72\n"
        "v_mfma_f32_16x16x32_bf16 v[4:7], v[148:151], v[208:211], v[4:7]\n"
        "s_waitcnt lgkmcnt(7)\n"
        "ds_read_b32 v161, v241\n"
        "v_perm_b32 v243, v172, v248, s73\n"
        "v_mfma_f32_16x16x32_bf16 v[8:11], v[152:155], v[208:211], v[8:11]\n"
        "s_waitcnt lgkmcnt(3)\n"
        "ds_read_b32 v162, v242\n"
        "v_perm_b32 v240, v173, v248, s70\n"
        "v_mfma_f32_16x16x32_bf16 v[96:99], v[156:159], v[208:211], v[96:99]\n"
        "ds_read_b32 v163, v243\n"
        "v_perm_b32 v241, v173, v248, s71\n"
        "v_mfma_f32_16x16x32_bf16 v[48:51], v[144:147], v[212:215], v[48:51]\n"
        "ds_read_b128 v[208:211], v245 offset:0\n"
        "v_perm_b32 v242, v173, v248, s72\n"
        "v_mfma_f32_16x16x32_bf16 v[52:55], v[148:151], v[212:215], v[52:55]\n"
        "ds_read_b32 v164, v240\n"
        "v_perm_b32 v243, v173, v248, s73\n"
        "v_mfma_f32_16x16x32_bf16 v[56:59], v[152:155], v[212:215], v[56:59]\n"
        "ds_read_b32 v165, v241\n"
        "v_perm_b32 v240, v174, v248, s70\n"
        "v_mfma_f32_16x16x32_bf16 v[12:15], v[144:147], v[216:219], v[12:15]\n"
        "ds_read_b128 v[212:215], v247 offset:0\n"
        "v_perm_b32 v241, v174, v248, s71\n"
        "v_mfma_f32_16x16x32_bf16 v[16:19], v[148:151], v[216:219], v[16:19]\n"
        "ds_read_b32 v166, v242\n"
        "v_mfma_f32_16x16x32_bf16 v[20:23], v[152:155], v[216:219], v[20:23]\n"
        "ds_read_b32 v167, v243\n"
        "v_perm_b32 v242, v174, v248, s72\n"
        "v_mfma_f32_16x16x32_bf16 v[100:103], v[156:159], v[216:219], v[100:103]\n"
        "ds_read_b128 v[188:191], v249 offset:8192\n"
        "v_perm_b32 v243, v174, v248, s73\n"
        "v_mfma_f32_16x16x32_bf16 v[60:63], v[144:147], v[220:223], v[60:63]\n"
        "ds_read_b128 v[216:219], v245 offset:2048\n"
        "v_mfma_f32_16x16x32_bf16 v[64:67], v[148:151], v[220:223], v[64:67]\n"
        "ds_read_b32 v168, v240\n"
        "v_mfma_f32_16x16x32_bf16 v[68:71], v[152:155], v[220:223], v[68:71]\n"
        "s_waitcnt lgkmcnt(12)\n"
        "ds_read_b32 v169, v241\n"
        "v_perm_b32 v240, v175, v248, s70\n"
        "v_mfma_f32_16x16x32_bf16 v[24:27], v[144:147], v[224:227], v[24:27]\n"
        "ds_read_b128 v[220:223], v247 offset:2048\n"
        "v_perm_b32 v241, v175, v248, s71\n"
        "v_mfma_f32_16x16x32_bf16 v[28:31], v[148:151], v[224:227], v[28:31]\n"
        "ds_read_b32 v170, v242\n"
        "v_mfma_f32_16x16x32_bf16 v[32:35], v[152:155], v[224:227], v[32:35]\n"
        "ds_read_b32 v171, v243\n"
        "v_perm_b32 v242, v175, v248, s72\n"
        "v_mfma_f32_16x16x32_bf16 v[104:107], v[156:159], v[224:227], v[104:107]\n"
        "ds_read_b32 v172, v240\n"
        "v_perm_b32 v243, v175, v248, s73\n"
        "v_mfma_f32_16x16x32_bf16 v[72:75], v[144:147], v[228:231], v[72:75]\n"
        "s_waitcnt lgkmcnt(7)\n"
        "ds_read_b128 v[224:227], v245 offset:4096\n"
        "v_perm_b32 v240, v188, v248, s70\n"
        "v_mfma_f32_16x16x32_bf16 v[76:79], v[148:151], v[228:231], v[76:79]\n"
        "ds_read_b32 v173, v241\n"
        "ds_read_b32 v174, v242\n"
        "v_mfma_f32_16x16x32_bf16 v[80:83], v[152:155], v[228:231], v[80:83]\n"
        "ds_read_b32 v175, v243\n"
        "v_perm_b32 v241, v188, v248, s71\n"
        "v_mfma_f32_16x16x32_bf16 v[36:39], v[144:147], v[232:235], v[36:39]\n"
        "ds_read_b128 v[228:231], v247 offset:4096\n"
        "v_perm_b32 v242, v188, v248, s72\n"
        "v_mfma_f32_16x16x32_bf16 v[40:43], v[148:151], v[232:235], v[40:43]\n"
        "ds_read_b32 v176, v240\n"
        "v_perm_b32 v243, v188, v248, s73\n"
        "v_mfma_f32_16x16x32_bf16 v[44:47], v[152:155], v[232:235], v[44:47]\n"
        "ds_read_b32 v177, v241\n"
        "v_perm_b32 v240, v189, v248, s70\n"
        "v_mfma_f32_16x16x32_bf16 v[108:111], v[156:159], v[232:235], v[108:111]\n"
        "ds_read_b32 v178, v242\n"
        "v_perm_b32 v241, v189, v248, s71\n"
        "v_mfma_f32_16x16x32_bf16 v[84:87], v[144:147], v[236:239], v[84:87]\n"
        "ds_read_b128 v[232:235], v245 offset:6144\n"
        "v_perm_b32 v242, v189, v248, s72\n"
        "v_mfma_f32_16x16x32_bf16 v[88:91], v[148:151], v[236:239], v[88:91]\n"
        "ds_read_b32 v179, v243\n"
        "v_mfma_f32_16x16x32_bf16 v[92:95], v[152:155], v[236:239], v[92:95]\n"
        "s_waitcnt lgkmcnt(13)\n"
        "ds_read_b32 v180, v240\n"
        "v_perm_b32 v243, v189, v248, s73\n"
        "v_mfma_f32_16x16x32_bf16 v[0:3], v[160:163], v[208:211], v[0:3]\n"
        "ds_read_b128 v[236:239], v247 offset:6144\n"
        "v_perm_b32 v240, v190, v248, s70\n"
        "v_mfma_f32_16x16x32_bf16 v[4:7], v[164:167], v[208:211], v[4:7]\n"
        "s_waitcnt lgkmcnt(13)\n"
        "ds_read_b32 v181, v241\n"
        "v_mfma_f32_16x16x32_bf16 v[8:11], v[168:171], v[208:211], v[8:11]\n"
        "s_waitcnt lgkmcnt(9)\n"
        "ds_read_b32 v182, v242\n"
        "v_perm_b32 v241, v190, v248, s71\n"
        "v_mfma_f32_16x16x32_bf16 v[96:99], v[172:175], v[208:211], v[96:99]\n"
        "s_barrier\n"
        "v_mfma_f32_16x16x32_bf16 v[48:51], v[160:163], v[212:215], v[48:51]\n"
        "ds_read_b128 v[208:211], v244 offset:16384\n"
        "v_perm_b32 v242, v190, v248, s72\n"
        "v_mfma_f32_16x16x32_bf16 v[52:55], v[164:167], v[212:215], v[52:55]\n"
        "ds_read_b32 v183, v243\n"
        "v_mfma_f32_16x16x32_bf16 v[56:59], v[168:171], v[212:215], v[56:59]\n"
        "ds_read_b128 v[204:207], v249 offset:12288\n"
        "v_perm_b32 v243, v190, v248, s73\n"
        "v_mfma_f32_16x16x32_bf16 v[12:15], v[160:163], v[216:219], v[12:15]\n"
        "ds_read_b128 v[212:215], v246 offset:16384\n"
        "v_mfma_f32_16x16x32_bf16 v[16:19], v[164:167], v[216:219], v[16:19]\n"
        "ds_read_b32 v184, v240\n"
        "v_mfma_f32_16x16x32_bf16 v[20:23], v[168:171], v[216:219], v[20:23]\n"
        "ds_read_b32 v185, v241\n"
        "v_perm_b32 v240, v191, v248, s70\n"
        "v_mfma_f32_16x16x32_bf16 v[100:103], v[172:175], v[216:219], v[100:103]\n"
        "ds_read_b32 v186, v242\n"
        "v_perm_b32 v241, v191, v248, s71\n"
        "v_mfma_f32_16x16x32_bf16 v[60:63], v[160:163], v[220:223], v[60:63]\n"
        "ds_read_b128 v[216:219], v244 offset:18432\n"
        "v_perm_b32 v242, v191, v248, s72\n"
        "v_mfma_f32_16x16x32_bf16 v[64:67], v[164:167], v[220:223], v[64:67]\n"
        "ds_read_b32 v187, v243\n"
        "ds_read_b32 v188, v240\n"
        "v_mfma_f32_16x16x32_bf16 v[68:71], v[168:171], v[220:223], v[68:71]\n"
        "s_waitcnt lgkmcnt(12)\n"
        "ds_read_b32 v189, v241\n"
        "v_perm_b32 v243, v191, v248, s73\n"
        "v_mfma_f32_16x16x32_bf16 v[24:27], v[160:163], v[224:227], v[24:27]\n"
        "s_waitcnt lgkmcnt(8)\n"
        "ds_read_b128 v[220:223], v246 offset:18432\n"
        "v_perm_b32 v240, v204, v248, s70\n"
        "v_mfma_f32_16x16x32_bf16 v[28:31], v[164:167], v[224:227], v[28:31]\n"
        "ds_read_b32 v190, v242\n"
        "v_perm_b32 v241, v204, v248, s71\n"
        "v_mfma_f32_16x16x32_bf16 v[32:35], v[168:171], v[224:227], v[32:35]\n"
        "ds_read_b32 v191, v243\n"
        "v_perm_b32 v242, v204, v248, s72\n"
        "v_mfma_f32_16x16x32_bf16 v[104:107], v[172:175], v[224:227], v[104:107]\n"
        "ds_read_b32 v192, v240\n"
        "v_perm_b32 v243, v204, v248, s73\n"
        "v_mfma_f32_16x16x32_bf16 v[72:75], v[160:163], v[228:231], v[72:75]\n"
        "ds_read_b128 v[224:227], v244 offset:20480\n"
        "v_perm_b32 v240, v205, v248, s70\n"
        "v_mfma_f32_16x16x32_bf16 v[76:79], v[164:167], v[228:231], v[76:79]\n"
        "ds_read_b32 v193, v241\n"
        "v_mfma_f32_16x16x32_bf16 v[80:83], v[168:171], v[228:231], v[80:83]\n"
        "ds_read_b32 v194, v242\n"
        "v_perm_b32 v241, v205, v248, s71\n"
        "v_mfma_f32_16x16x32_bf16 v[36:39], v[160:163], v[232:235], v[36:39]\n"
        "ds_read_b128 v[228:231], v246 offset:20480\n"
        "v_perm_b32 v242, v205, v248, s72\n"
        "v_mfma_f32_16x16x32_bf16 v[40:43], v[164:167], v[232:235], v[40:43]\n"
        "ds_read_b32 v195, v243\n"
        "v_mfma_f32_16x16x32_bf16 v[44:47], v[168:171], v[232:235], v[44:47]\n"
        "ds_read_b32 v196, v240\n"
        "v_perm_b32 v243, v205, v248, s73\n"
        "v_mfma_f32_16x16x32_bf16 v[108:111], v[172:175], v[232:235], v[108:111]\n"
        "ds_read_b32 v197, v241\n"
        "v_perm_b32 v240, v206, v248, s70\n"
        "v_mfma_f32_16x16x32_bf16 v[84:87], v[160:163], v[236:239], v[84:87]\n"
        "ds_read_b128 v[232:235], v244 offset:22528\n"
        "v_perm_b32 v241, v206, v248, s71\n"
        "v_mfma_f32_16x16x32_bf16 v[88:91], v[164:167], v[236:239], v[88:91]\n"
        "ds_read_b32 v198, v242\n"
        "v_mfma_f32_16x16x32_bf16 v[92:95], v[168:171], v[236:239], v[92:95]\n"
        "s_waitcnt lgkmcnt(12)\n"
        "ds_read_b32 v199, v243\n"
        "v_perm_b32 v242, v206, v248, s72\n"
        "v_mfma_f32_16x16x32_bf16 v[0:3], v[176:179], v[208:211], v[0:3]\n"
        "ds_read_b128 v[236:239], v246 offset:22528\n"
        "v_perm_b32 v243, v206, v248, s73\n"
        "v_mfma_f32_16x16x32_bf16 v[4:7], v[180:183], v[208:211], v[4:7]\n"
        "ds_read_b128 v[124:127], v249 offset:16384\n"
        "v_mfma_f32_16x16x32_bf16 v[8:11], v[184:187], v[208:211], v[8:11]\n"
        "s_waitcnt lgkmcnt(13)\n"
        "ds_read_b32 v200, v240\n"
        "v_mfma_f32_16x16x32_bf16 v[96:99], v[188:191], v[208:211], v[96:99]\n"
        "ds_read_b32 v201, v241\n"
        "v_perm_b32 v240, v207, v248, s70\n"
        "v_mfma_f32_16x16x32_bf16 v[48:51], v[176:179], v[212:215], v[48:51]\n"
        "ds_read_b128 v[208:211], v245 offset:16384\n"
        "v_perm_b32 v241, v207, v248, s71\n"
        "v_mfma_f32_16x16x32_bf16 v[52:55], v[180:183], v[212:215], v[52:55]\n"
        "ds_read_b32 v202, v242\n"
        "v_mfma_f32_16x16x32_bf16 v[56:59], v[184:187], v[212:215], v[56:59]\n"
        "ds_read_b32 v203, v243\n"
        "v_perm_b32 v242, v207, v248, s72\n"
        "v_mfma_f32_16x16x32_bf16 v[12:15], v[176:179], v[216:219], v[12:15]\n"
        "ds_read_b128 v[212:215], v247 offset:16384\n"
        "v_perm_b32 v243, v207, v248, s73\n"
        "v_mfma_f32_16x16x32_bf16 v[16:19], v[180:183], v[216:219], v[16:19]\n"
        "ds_read_b32 v204, v240\n"
        "v_mfma_f32_16x16x32_bf16 v[20:23], v[184:187], v[216:219], v[20:23]\n"
        "s_waitcnt lgkmcnt(7)\n"
        "ds_read_b32 v205, v241\n"
        "v_perm_b32 v240, v124, v248, s70\n"
        "v_mfma_f32_16x16x32_bf16 v[100:103], v[188:191], v[216:219], v[100:103]\n"
        "ds_read_b32 v206, v242\n"
        "v_perm_b32 v241, v124, v248, s71\n"
        "v_mfma_f32_16x16x32_bf16 v[60:63], v[176:179], v[220:223], v[60:63]\n"
        "ds_read_b128 v[216:219], v245 offset:18432\n"
        "v_perm_b32 v242, v124, v248, s72\n"
        "v_mfma_f32_16x16x32_bf16 v[64:67], v[180:183], v[220:223], v[64:67]\n"
        "ds_read_b32 v207, v243\n"
        "v_mfma_f32_16x16x32_bf16 v[68:71], v[184:187], v[220:223], v[68:71]\n"
        "ds_read_b32 v112, v240\n"
        "v_perm_b32 v243, v124, v248, s73\n"
        "v_mfma_f32_16x16x32_bf16 v[24:27], v[176:179], v[224:227], v[24:27]\n"
        "ds_read_b128 v[220:223], v247 offset:18432\n"
        "v_perm_b32 v240, v125, v248, s70\n"
        "v_mfma_f32_16x16x32_bf16 v[28:31], v[180:183], v[224:227], v[28:31]\n"
        "ds_read_b32 v113, v241\n"
        "v_mfma_f32_16x16x32_bf16 v[32:35], v[184:187], v[224:227], v[32:35]\n"
        "ds_read_b32 v114, v242\n"
        "v_perm_b32 v241, v125, v248, s71\n"
        "v_mfma_f32_16x16x32_bf16 v[104:107], v[188:191], v[224:227], v[104:107]\n"
        "ds_read_b32 v115, v243\n"
        "v_perm_b32 v242, v125, v248, s72\n"
        "v_mfma_f32_16x16x32_bf16 v[72:75], v[176:179], v[228:231], v[72:75]\n"
        "ds_read_b128 v[224:227], v245 offset:20480\n"
        "v_perm_b32 v243, v125, v248, s73\n"
        "v_mfma_f32_16x16x32_bf16 v[76:79], v[180:183], v[228:231], v[76:79]\n"
        "ds_read_b32 v116, v240\n"
        "v_mfma_f32_16x16x32_bf16 v[80:83], v[184:187], v[228:231], v[80:83]\n"
        "ds_read_b32 v117, v241\n"
        "v_perm_b32 v240, v126, v248, s70\n"
        "v_mfma_f32_16x16x32_bf16 v[36:39], v[176:179], v[232:235], v[36:39]\n"
        "ds_read_b128 v[228:231], v247 offset:20480\n"
        "v_perm_b32 v241, v126, v248, s71\n"
        "v_mfma_f32_16x16x32_bf16 v[40:43], v[180:183], v[232:235], v[40:43]\n"
        "ds_read_b32 v118, v242\n"
        "v_mfma_f32_16x16x32_bf16 v[44:47], v[184:187], v[232:235], v[44:47]\n"
        "ds_read_b32 v119, v243\n"
        "v_perm_b32 v242, v126, v248, s72\n"
        "v_mfma_f32_16x16x32_bf16 v[108:111], v[188:191], v[232:235], v[108:111]\n"
        "ds_read_b128 v[140:143], v249 offset:20480\n"
        "v_perm_b32 v243, v126, v248, s73\n"
        "v_mfma_f32_16x16x32_bf16 v[84:87], v[176:179], v[236:239], v[84:87]\n"
        "ds_read_b128 v[232:235], v245 offset:22528\n"
        "v_mfma_f32_16x16x32_bf16 v[88:91], v[180:183], v[236:239], v[88:91]\n"
        "ds_read_b32 v120, v240\n"
        "v_mfma_f32_16x16x32_bf16 v[92:95], v[184:187], v[236:239], v[92:95]\n"
        "s_waitcnt lgkmcnt(12)\n"
        "ds_read_b32 v121, v241\n"
        "v_perm_b32 v240, v127, v248, s70\n"
        "v_mfma_f32_16x16x32_bf16 v[0:3], v[192:195], v[208:211], v[0:3]\n"
        "ds_read_b128 v[236:239], v247 offset:22528\n"
        "v_perm_b32 v241, v127, v248, s71\n"
        "v_mfma_f32_16x16x32_bf16 v[4:7], v[196:199], v[208:211], v[4:7]\n"
        "ds_read_b32 v122, v242\n"
        "v_mfma_f32_16x16x32_bf16 v[8:11], v[200:203], v[208:211], v[8:11]\n"
        "ds_read_b32 v123, v243\n"
        "v_perm_b32 v242, v127, v248, s72\n"
        "v_mfma_f32_16x16x32_bf16 v[96:99], v[204:207], v[208:211], v[96:99]\n"
        "s_barrier\n"
        "v_mfma_f32_16x16x32_bf16 v[48:51], v[192:195], v[212:215], v[48:51]\n"
        "ds_read_b128 v[208:211], v244 offset:32768\n"
        "v_perm_b32 v243, v127, v248, s73\n"
        "v_mfma_f32_16x16x32_bf16 v[52:55], v[196:199], v[212:215], v[52:55]\n"
        "ds_read_b32 v124, v240\n"
        "v_mfma_f32_16x16x32_bf16 v[56:59], v[200:203], v[212:215], v[56:59]\n"
        "s_waitcnt lgkmcnt(8)\n"
        "ds_read_b32 v125, v241\n"
        "v_perm_b32 v240, v140, v248, s70\n"
        "v_mfma_f32_16x16x32_bf16 v[12:15], v[192:195], v[216:219], v[12:15]\n"
        "ds_read_b128 v[212:215], v246 offset:32768\n"
        "v_perm_b32 v241, v140, v248, s71\n"
        "v_mfma_f32_16x16x32_bf16 v[16:19], v[196:199], v[216:219], v[16:19]\n"
        "ds_read_b32 v126, v242\n"
        "v_mfma_f32_16x16x32_bf16 v[20:23], v[200:203], v[216:219], v[20:23]\n"
        "ds_read_b32 v127, v243\n"
        "v_perm_b32 v242, v140, v248, s72\n"
        "v_mfma_f32_16x16x32_bf16 v[100:103], v[204:207], v[216:219], v[100:103]\n"
        "ds_read_b32 v128, v240\n"
        "v_perm_b32 v243, v140, v248, s73\n"
        "v_mfma_f32_16x16x32_bf16 v[60:63], v[192:195], v[220:223], v[60:63]\n"
        "ds_read_b128 v[216:219], v244 offset:34816\n"
        "v_perm_b32 v240, v141, v248, s70\n"
        "v_mfma_f32_16x16x32_bf16 v[64:67], v[196:199], v[220:223], v[64:67]\n"
        "ds_read_b32 v129, v241\n"
        "v_mfma_f32_16x16x32_bf16 v[68:71], v[200:203], v[220:223], v[68:71]\n"
        "s_waitcnt lgkmcnt(11)\n"
        "ds_read_b32 v130, v242\n"
        "v_perm_b32 v241, v141, v248, s71\n"
        "v_mfma_f32_16x16x32_bf16 v[24:27], v[192:195], v[224:227], v[24:27]\n"
        "ds_read_b128 v[220:223], v246 offset:34816\n"
        "v_perm_b32 v242, v141, v248, s72\n"
        "v_mfma_f32_16x16x32_bf16 v[28:31], v[196:199], v[224:227], v[28:31]\n"
        "ds_read_b32 v131, v243\n"
        "v_mfma_f32_16x16x32_bf16 v[32:35], v[200:203], v[224:227], v[32:35]\n"
        "ds_read_b32 v132, v240\n"
        "v_perm_b32 v243, v141, v248, s73\n"
        "v_mfma_f32_16x16x32_bf16 v[104:107], v[204:207], v[224:227], v[104:107]\n"
        "ds_read_b32 v133, v241\n"
        "v_perm_b32 v240, v142, v248, s70\n"
        "v_mfma_f32_16x16x32_bf16 v[72:75], v[192:195], v[228:231], v[72:75]\n"
        "ds_read_b128 v[224:227], v244 offset:36864\n"
        "v_perm_b32 v241, v142, v248, s71\n"
        "v_mfma_f32_16x16x32_bf16 v[76:79], v[196:199], v[228:231], v[76:79]\n"
        "ds_read_b32 v134, v242\n"
        "v_mfma_f32_16x16x32_bf16 v[80:83], v[200:203], v[228:231], v[80:83]\n"
        "ds_read_b32 v135, v243\n"
        "v_perm_b32 v242, v142, v248, s72\n"
        "v_mfma_f32_16x16x32_bf16 v[36:39], v[192:195], v[232:235], v[36:39]\n"
        "ds_read_b128 v[228:231], v246 offset:36864\n"
        "v_perm_b32 v243, v142, v248, s73\n"
        "v_mfma_f32_16x16x32_bf16 v[40:43], v[196:199], v[232:235], v[40:43]\n"
        "ds_read_b128 v[156:159], v249 offset:24576\n"
        "v_mfma_f32_16x16x32_bf16 v[44:47], v[200:203], v[232:235], v[44:47]\n"
        "ds_read_b32 v136, v240\n"
        "v_mfma_f32_16x16x32_bf16 v[108:111], v[204:207], v[232:235], v[108:111]\n"
        "ds_read_b32 v137, v241\n"
        "v_perm_b32 v240, v143, v248, s70\n"
        "v_mfma_f32_16x16x32_bf16 v[84:87], v[192:195], v[236:239], v[84:87]\n"
        "ds_read_b128 v[232:235], v244 offset:38912\n"
        "v_perm_b32 v241, v143, v248, s71\n"
        "v_mfma_f32_16x16x32_bf16 v[88:91], v[196:199], v[236:239], v[88:91]\n"
        "ds_read_b32 v138, v242\n"
        "v_mfma_f32_16x16x32_bf16 v[92:95], v[200:203], v[236:239], v[92:95]\n"
        "s_waitcnt lgkmcnt(12)\n"
        "ds_read_b32 v139, v243\n"
        "v_perm_b32 v242, v143, v248, s72\n"
        "v_mfma_f32_16x16x32_bf16 v[0:3], v[112:115], v[208:211], v[0:3]\n"
        "ds_read_b128 v[236:239], v246 offset:38912\n"
        "v_perm_b32 v243, v143, v248, s73\n"
        "v_mfma_f32_16x16x32_bf16 v[4:7], v[116:119], v[208:211], v[4:7]\n"
        "ds_read_b32 v140, v240\n"
        "v_mfma_f32_16x16x32_bf16 v[8:11], v[120:123], v[208:211], v[8:11]\n"
        "s_waitcnt lgkmcnt(7)\n"
        "ds_read_b32 v141, v241\n"
        "v_perm_b32 v240, v156, v248, s70\n"
        "v_mfma_f32_16x16x32_bf16 v[96:99], v[124:127], v[208:211], v[96:99]\n"
        "ds_read_b32 v142, v242\n"
        "v_perm_b32 v241, v156, v248, s71\n"
        "v_mfma_f32_16x16x32_bf16 v[48:51], v[112:115], v[212:215], v[48:51]\n"
        "ds_read_b128 v[208:211], v245 offset:32768\n"
        "v_perm_b32 v242, v156, v248, s72\n"
        "v_mfma_f32_16x16x32_bf16 v[52:55], v[116:119], v[212:215], v[52:55]\n"
        "ds_read_b32 v143, v243\n"
        "v_mfma_f32_16x16x32_bf16 v[56:59], v[120:123], v[212:215], v[56:59]\n"
        "ds_read_b32 v144, v240\n"
        "v_perm_b32 v243, v156, v248, s73\n"
        "v_mfma_f32_16x16x32_bf16 v[12:15], v[112:115], v[216:219], v[12:15]\n"
        "ds_read_b128 v[212:215], v247 offset:32768\n"
        "v_perm_b32 v240, v157, v248, s70\n"
        "v_mfma_f32_16x16x32_bf16 v[16:19], v[116:119], v[216:219], v[16:19]\n"
        "ds_read_b32 v145, v241\n"
        "v_mfma_f32_16x16x32_bf16 v[20:23], v[120:123], v[216:219], v[20:23]\n"
        "ds_read_b32 v146, v242\n"
        "v_perm_b32 v241, v157, v248, s71\n"
        "v_mfma_f32_16x16x32_bf16 v[100:103], v[124:127], v[216:219], v[100:103]\n"
        "ds_read_b32 v147, v243\n"
        "v_perm_b32 v242, v157, v248, s72\n"
        "v_mfma_f32_16x16x32_bf16 v[60:63], v[112:115], v[220:223], v[60:63]\n"
        "ds_read_b128 v[216:219], v245 offset:34816\n"
        "v_perm_b32 v243, v157, v248, s73\n"
        "v_mfma_f32_16x16x32_bf16 v[64:67], v[116:119], v[220:223], v[64:67]\n"
        "ds_read_b32 v148, v240\n"
        "v_mfma_f32_16x16x32_bf16 v[68:71], v[120:123], v[220:223], v[68:71]\n"
        "s_waitcnt lgkmcnt(12)\n"
        "ds_read_b32 v149, v241\n"
        "v_perm_b32 v240, v158, v248, s70\n"
        "v_mfma_f32_16x16x32_bf16 v[24:27], v[112:115], v[224:227], v[24:27]\n"
        "ds_read_b128 v[220:223], v247 offset:34816\n"
        "v_perm_b32 v241, v158, v248, s71\n"
        "v_mfma_f32_16x16x32_bf16 v[28:31], v[116:119], v[224:227], v[28:31]\n"
        "ds_read_b32 v150, v242\n"
        "v_mfma_f32_16x16x32_bf16 v[32:35], v[120:123], v[224:227], v[32:35]\n"
        "ds_read_b32 v151, v243\n"
        "v_perm_b32 v242, v158, v248, s72\n"
        "v_mfma_f32_16x16x32_bf16 v[104:107], v[124:127], v[224:227], v[104:107]\n"
        "ds_read_b128 v[172:175], v249 offset:28672\n"
        "v_perm_b32 v243, v158, v248, s73\n"
        "v_mfma_f32_16x16x32_bf16 v[72:75], v[112:115], v[228:231], v[72:75]\n"
        "ds_read_b128 v[224:227], v245 offset:36864\n"
        "v_mfma_f32_16x16x32_bf16 v[76:79], v[116:119], v[228:231], v[76:79]\n"
        "ds_read_b32 v152, v240\n"
        "v_mfma_f32_16x16x32_bf16 v[80:83], v[120:123], v[228:231], v[80:83]\n"
        "ds_read_b32 v153, v241\n"
        "v_perm_b32 v240, v159, v248, s70\n"
        "v_mfma_f32_16x16x32_bf16 v[36:39], v[112:115], v[232:235], v[36:39]\n"
        "ds_read_b128 v[228:231], v247 offset:36864\n"
        "v_perm_b32 v241, v159, v248, s71\n"
        "v_mfma_f32_16x16x32_bf16 v[40:43], v[116:119], v[232:235], v[40:43]\n"
        "ds_read_b32 v154, v242\n"
        "v_mfma_f32_16x16x32_bf16 v[44:47], v[120:123], v[232:235], v[44:47]\n"
        "ds_read_b32 v155, v243\n"
        "v_perm_b32 v242, v159, v248, s72\n"
        "v_mfma_f32_16x16x32_bf16 v[108:111], v[124:127], v[232:235], v[108:111]\n"
        "ds_read_b32 v156, v240\n"
        "v_perm_b32 v243, v159, v248, s73\n"
        "v_mfma_f32_16x16x32_bf16 v[84:87], v[112:115], v[236:239], v[84:87]\n"
        "s_waitcnt lgkmcnt(7)\n"
        "ds_read_b128 v[232:235], v245 offset:38912\n"
        "v_perm_b32 v240, v172, v248, s70\n"
        "v_mfma_f32_16x16x32_bf16 v[88:91], v[116:119], v[236:239], v[88:91]\n"
        "ds_read_b32 v157, v241\n"
        "v_mfma_f32_16x16x32_bf16 v[92:95], v[120:123], v[236:239], v[92:95]\n"
        "ds_read_b32 v158, v242\n"
        "v_perm_b32 v241, v172, v248, s71\n"
        "v_mfma_f32_16x16x32_bf16 v[0:3], v[128:131], v[208:211], v[0:3]\n"
        "ds_read_b128 v[236:239], v247 offset:38912\n"
        "v_perm_b32 v242, v172, v248, s72\n"
        "v_mfma_f32_16x16x32_bf16 v[4:7], v[132:135], v[208:211], v[4:7]\n"
        "ds_read_b32 v159, v243\n"
        "v_mfma_f32_16x16x32_bf16 v[8:11], v[136:139], v[208:211], v[8:11]\n"
        "ds_read_b32 v160, v240\n"
        "v_perm_b32 v243, v172, v248, s73\n"
        "v_mfma_f32_16x16x32_bf16 v[96:99], v[140:143], v[208:211], v[96:99]\n"
        "s_barrier\n"
        "v_mfma_f32_16x16x32_bf16 v[48:51], v[128:131], v[212:215], v[48:51]\n"
        "ds_read_b128 v[208:211], v244 offset:49152\n"
        "v_perm_b32 v240, v173, v248, s70\n"
        "v_mfma_f32_16x16x32_bf16 v[52:55], v[132:135], v[212:215], v[52:55]\n"
        "ds_read_b32 v161, v241\n"
        "v_mfma_f32_16x16x32_bf16 v[56:59], v[136:139], v[212:215], v[56:59]\n"
        "ds_read_b32 v162, v242\n"
        "v_perm_b32 v241, v173, v248, s71\n"
        "v_mfma_f32_16x16x32_bf16 v[12:15], v[128:131], v[216:219], v[12:15]\n"
        "ds_read_b128 v[212:215], v246 offset:49152\n"
        "v_perm_b32 v242, v173, v248, s72\n"
        "v_mfma_f32_16x16x32_bf16 v[16:19], v[132:135], v[216:219], v[16:19]\n"
        "ds_read_b32 v163, v243\n"
        "v_mfma_f32_16x16x32_bf16 v[20:23], v[136:139], v[216:219], v[20:23]\n"
        "ds_read_b32 v164, v240\n"
        "v_perm_b32 v243, v173, v248, s73\n"
        "v_mfma_f32_16x16x32_bf16 v[100:103], v[140:143], v[216:219], v[100:103]\n"
        "ds_read_b32 v165, v241\n"
        "v_perm_b32 v240, v174, v248, s70\n"
        "v_mfma_f32_16x16x32_bf16 v[60:63], v[128:131], v[220:223], v[60:63]\n"
        "ds_read_b128 v[216:219], v244 offset:51200\n"
        "v_perm_b32 v241, v174, v248, s71\n"
        "v_mfma_f32_16x16x32_bf16 v[64:67], v[132:135], v[220:223], v[64:67]\n"
        "ds_read_b32 v166, v242\n"
        "v_mfma_f32_16x16x32_bf16 v[68:71], v[136:139], v[220:223], v[68:71]\n"
        "s_waitcnt lgkmcnt(11)\n"
        "ds_read_b32 v167, v243\n"
        "v_perm_b32 v242, v174, v248, s72\n"
        "v_mfma_f32_16x16x32_bf16 v[24:27], v[128:131], v[224:227], v[24:27]\n"
        "ds_read_b128 v[220:223], v246 offset:51200\n"
        "v_perm_b32 v243, v174, v248, s73\n"
        "v_mfma_f32_16x16x32_bf16 v[28:31], v[132:135], v[224:227], v[28:31]\n"
        "ds_read_b128 v[188:191], v249 offset:0\n"
        "v_mfma_f32_16x16x32_bf16 v[32:35], v[136:139], v[224:227], v[32:35]\n"
        "ds_read_b32 v168, v240\n"
        "v_mfma_f32_16x16x32_bf16 v[104:107], v[140:143], v[224:227], v[104:107]\n"
        "ds_read_b32 v169, v241\n"
        "v_perm_b32 v240, v175, v248, s70\n"
        "v_mfma_f32_16x16x32_bf16 v[72:75], v[128:131], v[228:231], v[72:75]\n"
        "ds_read_b128 v[224:227], v244 offset:53248\n"
        "v_perm_b32 v241, v175, v248, s71\n"
        "v_mfma_f32_16x16x32_bf16 v[76:79], v[132:135], v[228:231], v[76:79]\n"
        "ds_read_b32 v170, v242\n"
        "v_mfma_f32_16x16x32_bf16 v[80:83], v[136:139], v[228:231], v[80:83]\n"
        "ds_read_b32 v171, v243\n"
        "v_perm_b32 v242, v175, v248, s72\n"
        "v_mfma_f32_16x16x32_bf16 v[36:39], v[128:131], v[232:235], v[36:39]\n"
        "ds_read_b128 v[228:231], v246 offset:53248\n"
        "v_perm_b32 v243, v175, v248, s73\n"
        "v_mfma_f32_16x16x32_bf16 v[40:43], v[132:135], v[232:235], v[40:43]\n"
        "ds_read_b32 v172, v240\n"
        "v_mfma_f32_16x16x32_bf16 v[44:47], v[136:139], v[232:235], v[44:47]\n"
        "s_waitcnt lgkmcnt(7)\n"
        "ds_read_b32 v173, v241\n"
        "v_perm_b32 v240, v188, v248, s70\n"
        "v_mfma_f32_16x16x32_bf16 v[108:111], v[140:143], v[232:235], v[108:111]\n"
        "ds_read_b32 v174, v242\n"
        "v_perm_b32 v241, v188, v248, s71\n"
        "v_mfma_f32_16x16x32_bf16 v[84:87], v[128:131], v[236:239], v[84:87]\n"
        "ds_read_b128 v[232:235], v244 offset:55296\n"
        "v_perm_b32 v242, v188, v248, s72\n"
        "v_mfma_f32_16x16x32_bf16 v[88:91], v[132:135], v[236:239], v[88:91]\n"
        "ds_read_b32 v175, v243\n"
        "v_mfma_f32_16x16x32_bf16 v[92:95], v[136:139], v[236:239], v[92:95]\n"
        "ds_read_b32 v176, v240\n"
        "v_perm_b32 v243, v188, v248, s73\n"
        "v_mfma_f32_16x16x32_bf16 v[0:3], v[144:147], v[208:211], v[0:3]\n"
        "ds_read_b128 v[236:239], v246 offset:55296\n"
        "v_perm_b32 v240, v189, v248, s70\n"
        "v_mfma_f32_16x16x32_bf16 v[4:7], v[148:151], v[208:211], v[4:7]\n"
        "ds_read_b32 v177, v241\n"
        "v_mfma_f32_16x16x32_bf16 v[8:11], v[152:155], v[208:211], v[8:11]\n"
        "ds_read_b32 v178, v242\n"
        "v_perm_b32 v241, v189, v248, s71\n"
        "v_mfma_f32_16x16x32_bf16 v[96:99], v[156:159], v[208:211], v[96:99]\n"
        "ds_read_b32 v179, v243\n"
        "v_perm_b32 v242, v189, v248, s72\n"
        "v_mfma_f32_16x16x32_bf16 v[48:51], v[144:147], v[212:215], v[48:51]\n"
        "ds_read_b128 v[208:211], v245 offset:49152\n"
        "v_perm_b32 v243, v189, v248, s73\n"
        "v_mfma_f32_16x16x32_bf16 v[52:55], v[148:151], v[212:215], v[52:55]\n"
        "ds_read_b32 v180, v240\n"
        "v_mfma_f32_16x16x32_bf16 v[56:59], v[152:155], v[212:215], v[56:59]\n"
        "ds_read_b32 v181, v241\n"
        "v_perm_b32 v240, v190, v248, s70\n"
        "v_mfma_f32_16x16x32_bf16 v[12:15], v[144:147], v[216:219], v[12:15]\n"
        "ds_read_b128 v[212:215], v247 offset:49152\n"
        "v_perm_b32 v241, v190, v248, s71\n"
        "v_mfma_f32_16x16x32_bf16 v[16:19], v[148:151], v[216:219], v[16:19]\n"
        "ds_read_b32 v182, v242\n"
        "v_mfma_f32_16x16x32_bf16 v[20:23], v[152:155], v[216:219], v[20:23]\n"
        "ds_read_b32 v183, v243\n"
        "v_perm_b32 v242, v190, v248, s72\n"
        "v_mfma_f32_16x16x32_bf16 v[100:103], v[156:159], v[216:219], v[100:103]\n"
        "ds_read_b128 v[204:207], v249 offset:4096\n"
        "v_perm_b32 v243, v190, v248, s73\n"
        "v_mfma_f32_16x16x32_bf16 v[60:63], v[144:147], v[220:223], v[60:63]\n"
        "ds_read_b128 v[216:219], v245 offset:51200\n"
        "v_mfma_f32_16x16x32_bf16 v[64:67], v[148:151], v[220:223], v[64:67]\n"
        "ds_read_b32 v184, v240\n"
        "v_mfma_f32_16x16x32_bf16 v[68:71], v[152:155], v[220:223], v[68:71]\n"
        "s_waitcnt lgkmcnt(12)\n"
        "ds_read_b32 v185, v241\n"
        "v_perm_b32 v240, v191, v248, s70\n"
        "v_mfma_f32_16x16x32_bf16 v[24:27], v[144:147], v[224:227], v[24:27]\n"
        "ds_read_b128 v[220:223], v247 offset:51200\n"
        "v_perm_b32 v241, v191, v248, s71\n"
        "v_mfma_f32_16x16x32_bf16 v[28:31], v[148:151], v[224:227], v[28:31]\n"
        "ds_read_b32 v186, v242\n"
        "v_mfma_f32_16x16x32_bf16 v[32:35], v[152:155], v[224:227], v[32:35]\n"
        "ds_read_b32 v187, v243\n"
        "v_perm_b32 v242, v191, v248, s72\n"
        "v_mfma_f32_16x16x32_bf16 v[104:107], v[156:159], v[224:227], v[104:107]\n"
        "ds_read_b32 v188, v240\n"
        "v_perm_b32 v243, v191, v248, s73\n"
        "v_mfma_f32_16x16x32_bf16 v[72:75], v[144:147], v[228:231], v[72:75]\n"
        "s_waitcnt lgkmcnt(7)\n"
        "ds_read_b128 v[224:227], v245 offset:53248\n"
        "v_perm_b32 v240, v204, v248, s70\n"
        "v_mfma_f32_16x16x32_bf16 v[76:79], v[148:151], v[228:231], v[76:79]\n"
        "ds_read_b32 v189, v241\n"
        "v_mfma_f32_16x16x32_bf16 v[80:83], v[152:155], v[228:231], v[80:83]\n"
        "ds_read_b32 v190, v242\n"
        "v_perm_b32 v241, v204, v248, s71\n"
        "v_mfma_f32_16x16x32_bf16 v[36:39], v[144:147], v[232:235], v[36:39]\n"
        "ds_read_b128 v[228:231], v247 offset:53248\n"
        "v_perm_b32 v242, v204, v248, s72\n"
        "v_mfma_f32_16x16x32_bf16 v[40:43], v[148:151], v[232:235], v[40:43]\n"
        "ds_read_b32 v191, v243\n"
        "v_mfma_f32_16x16x32_bf16 v[44:47], v[152:155], v[232:235], v[44:47]\n"
        "ds_read_b32 v192, v240\n"
        "v_perm_b32 v243, v204, v248, s73\n"
        "v_mfma_f32_16x16x32_bf16 v[108:111], v[156:159], v[232:235], v[108:111]\n"
        "ds_read_b32 v193, v241\n"
        "v_perm_b32 v240, v205, v248, s70\n"
        "v_mfma_f32_16x16x32_bf16 v[84:87], v[144:147], v[236:239], v[84:87]\n"
        "ds_read_b128 v[232:235], v245 offset:55296\n"
        "v_perm_b32 v241, v205, v248, s71\n"
        "v_mfma_f32_16x16x32_bf16 v[88:91], v[148:151], v[236:239], v[88:91]\n"
        "ds_read_b32 v194, v242\n"
        "v_mfma_f32_16x16x32_bf16 v[92:95], v[152:155], v[236:239], v[92:95]\n"
        "s_waitcnt lgkmcnt(12)\n"
        "ds_read_b32 v195, v243\n"
        "v_perm_b32 v242, v205, v248, s72\n"
        "v_mfma_f32_16x16x32_bf16 v[0:3], v[160:163], v[208:211], v[0:3]\n"
        "ds_read_b128 v[236:239], v247 offset:55296\n"
        "v_perm_b32 v243, v205, v248, s73\n"
        "v_mfma_f32_16x16x32_bf16 v[4:7], v[164:167], v[208:211], v[4:7]\n"
        "ds_read_b32 v196, v240\n"
        "v_mfma_f32_16x16x32_bf16 v[8:11], v[168:171], v[208:211], v[8:11]\n"
        "ds_read_b32 v197, v241\n"
        "v_perm_b32 v240, v206, v248, s70\n"
        "v_mfma_f32_16x16x32_bf16 v[96:99], v[172:175], v[208:211], v[96:99]\n"
        "s_barrier\n"
        "v_mfma_f32_16x16x32_bf16 v[48:51], v[160:163], v[212:215], v[48:51]\n"
        "ds_read_b128 v[208:211], v244 offset:0\n"
        "v_perm_b32 v241, v206, v248, s71\n"
        "v_mfma_f32_16x16x32_bf16 v[52:55], v[164:167], v[212:215], v[52:55]\n"
        "ds_read_b32 v198, v242\n"
        "v_mfma_f32_16x16x32_bf16 v[56:59], v[168:171], v[212:215], v[56:59]\n"
        "ds_read_b32 v199, v243\n"
        "v_perm_b32 v242, v206, v248, s72\n"
        "v_mfma_f32_16x16x32_bf16 v[12:15], v[160:163], v[216:219], v[12:15]\n"
        "ds_read_b128 v[212:215], v246 offset:0\n"
        "v_perm_b32 v243, v206, v248, s73\n"
        "v_mfma_f32_16x16x32_bf16 v[16:19], v[164:167], v[216:219], v[16:19]\n"
        "ds_read_b128 v[124:127], v249 offset:8192\n"
        "v_mfma_f32_16x16x32_bf16 v[20:23], v[168:171], v[216:219], v[20:23]\n"
        "ds_read_b32 v200, v240\n"
        "v_mfma_f32_16x16x32_bf16 v[100:103], v[172:175], v[216:219], v[100:103]\n"
        "ds_read_b32 v201, v241\n"
        "v_perm_b32 v240, v207, v248, s70\n"
        "v_mfma_f32_16x16x32_bf16 v[60:63], v[160:163], v[220:223], v[60:63]\n"
        "ds_read_b128 v[216:219], v244 offset:2048\n"
        "v_perm_b32 v241, v207, v248, s71\n"
        "v_mfma_f32_16x16x32_bf16 v[64:67], v[164:167], v[220:223], v[64:67]\n"
        "ds_read_b32 v202, v242\n"
        "v_mfma_f32_16x16x32_bf16 v[68:71], v[168:171], v[220:223], v[68:71]\n"
        "s_waitcnt lgkmcnt(11)\n"
        "ds_read_b32 v203, v243\n"
        "v_perm_b32 v242, v207, v248, s72\n"
        "v_mfma_f32_16x16x32_bf16 v[24:27], v[160:163], v[224:227], v[24:27]\n"
        "ds_read_b128 v[220:223], v246 offset:2048\n"
        "v_perm_b32 v243, v207, v248, s73\n"
        "v_mfma_f32_16x16x32_bf16 v[28:31], v[164:167], v[224:227], v[28:31]\n"
        "ds_read_b32 v204, v240\n"
        "v_mfma_f32_16x16x32_bf16 v[32:35], v[168:171], v[224:227], v[32:35]\n"
        "s_waitcnt lgkmcnt(7)\n"
        "ds_read_b32 v205, v241\n"
        "v_perm_b32 v240, v124, v248, s70\n"
        "v_mfma_f32_16x16x32_bf16 v[104:107], v[172:175], v[224:227], v[104:107]\n"
        "ds_read_b32 v206, v242\n"
        "v_perm_b32 v241, v124, v248, s71\n"
        "v_mfma_f32_16x16x32_bf16 v[72:75], v[160:163], v[228:231], v[72:75]\n"
        "ds_read_b128 v[224:227], v244 offset:4096\n"
        "v_perm_b32 v242, v124, v248, s72\n"
        "v_mfma_f32_16x16x32_bf16 v[76:79], v[164:167], v[228:231], v[76:79]\n"
        "ds_read_b32 v207, v243\n"
        "v_mfma_f32_16x16x32_bf16 v[80:83], v[168:171], v[228:231], v[80:83]\n"
        "ds_read_b32 v112, v240\n"
        "v_perm_b32 v243, v124, v248, s73\n"
        "v_mfma_f32_16x16x32_bf16 v[36:39], v[160:163], v[232:235], v[36:39]\n"
        "ds_read_b128 v[228:231], v246 offset:4096\n"
        "v_perm_b32 v240, v125, v248, s70\n"
        "v_mfma_f32_16x16x32_bf16 v[40:43], v[164:167], v[232:235], v[40:43]\n"
        "ds_read_b32 v113, v241\n"
        "v_mfma_f32_16x16x32_bf16 v[44:47], v[168:171], v[232:235], v[44:47]\n"
        "ds_read_b32 v114, v242\n"
        "v_perm_b32 v241, v125, v248, s71\n"
        "v_mfma_f32_16x16x32_bf16 v[108:111], v[172:175], v[232:235], v[108:111]\n"
        "ds_read_b32 v115, v243\n"
        "v_perm_b32 v242, v125, v248, s72\n"
        "v_mfma_f32_16x16x32_bf16 v[84:87], v[160:163], v[236:239], v[84:87]\n"
        "ds_read_b128 v[232:235], v244 offset:6144\n"
        "v_perm_b32 v243, v125, v248, s73\n"
        "v_mfma_f32_16x16x32_bf16 v[88:91], v[164:167], v[236:239], v[88:91]\n"
        "ds_read_b32 v116, v240\n"
        "v_mfma_f32_16x16x32_bf16 v[92:95], v[168:171], v[236:239], v[92:95]\n"
        "s_waitcnt lgkmcnt(12)\n"
        "ds_read_b32 v117, v241\n"
        "v_perm_b32 v240, v126, v248, s70\n"
        "v_mfma_f32_16x16x32_bf16 v[0:3], v[176:179], v[208:211], v[0:3]\n"
        "ds_read_b128 v[236:239], v246 offset:6144\n"
        "v_perm_b32 v241, v126, v248, s71\n"
        "v_mfma_f32_16x16x32_bf16 v[4:7], v[180:183], v[208:211], v[4:7]\n"
        "ds_read_b32 v118, v242\n"
        "v_mfma_f32_16x16x32_bf16 v[8:11], v[184:187], v[208:211], v[8:11]\n"
        "ds_read_b32 v119, v243\n"
        "v_perm_b32 v242, v126, v248, s72\n"
        "v_mfma_f32_16x16x32_bf16 v[96:99], v[188:191], v[208:211], v[96:99]\n"
        "ds_read_b128 v[140:143], v249 offset:12288\n"
        "v_perm_b32 v243, v126, v248, s73\n"
        "v_mfma_f32_16x16x32_bf16 v[48:51], v[176:179], v[212:215], v[48:51]\n"
        "ds_read_b128 v[208:211], v245 offset:0\n"
        "v_mfma_f32_16x16x32_bf16 v[52:55], v[180:183], v[212:215], v[52:55]\n"
        "ds_read_b32 v120, v240\n"
        "v_mfma_f32_16x16x32_bf16 v[56:59], v[184:187], v[212:215], v[56:59]\n"
        "ds_read_b32 v121, v241\n"
        "v_perm_b32 v240, v127, v248, s70\n"
        "v_mfma_f32_16x16x32_bf16 v[12:15], v[176:179], v[216:219], v[12:15]\n"
        "ds_read_b128 v[212:215], v247 offset:0\n"
        "v_perm_b32 v241, v127, v248, s71\n"
        "v_mfma_f32_16x16x32_bf16 v[16:19], v[180:183], v[216:219], v[16:19]\n"
        "ds_read_b32 v122, v242\n"
        "v_mfma_f32_16x16x32_bf16 v[20:23], v[184:187], v[216:219], v[20:23]\n"
        "ds_read_b32 v123, v243\n"
        "v_perm_b32 v242, v127, v248, s72\n"
        "v_mfma_f32_16x16x32_bf16 v[100:103], v[188:191], v[216:219], v[100:103]\n"
        "ds_read_b32 v124, v240\n"
        "v_perm_b32 v243, v127, v248, s73\n"
        "v_mfma_f32_16x16x32_bf16 v[60:63], v[176:179], v[220:223], v[60:63]\n"
        "s_waitcnt lgkmcnt(7)\n"
        "ds_read_b128 v[216:219], v245 offset:2048\n"
        "v_perm_b32 v240, v140, v248, s70\n"
        "v_mfma_f32_16x16x32_bf16 v[64:67], v[180:183], v[220:223], v[64:67]\n"
        "ds_read_b32 v125, v241\n"
        "v_mfma_f32_16x16x32_bf16 v[68:71], v[184:187], v[220:223], v[68:71]\n"
        "ds_read_b32 v126, v242\n"
        "v_perm_b32 v241, v140, v248, s71\n"
        "v_mfma_f32_16x16x32_bf16 v[24:27], v[176:179], v[224:227], v[24:27]\n"
        "ds_read_b128 v[220:223], v247 offset:2048\n"
        "v_perm_b32 v242, v140, v248, s72\n"
        "v_mfma_f32_16x16x32_bf16 v[28:31], v[180:183], v[224:227], v[28:31]\n"
        "ds_read_b32 v127, v243\n"
        "v_mfma_f32_16x16x32_bf16 v[32:35], v[184:187], v[224:227], v[32:35]\n"
        "ds_read_b32 v128, v240\n"
        "v_perm_b32 v243, v140, v248, s73\n"
        "v_mfma_f32_16x16x32_bf16 v[104:107], v[188:191], v[224:227], v[104:107]\n"
        "ds_read_b32 v129, v241\n"
        "v_perm_b32 v240, v141, v248, s70\n"
        "v_mfma_f32_16x16x32_bf16 v[72:75], v[176:179], v[228:231], v[72:75]\n"
        "ds_read_b128 v[224:227], v245 offset:4096\n"
        "v_perm_b32 v241, v141, v248, s71\n"
        "v_mfma_f32_16x16x32_bf16 v[76:79], v[180:183], v[228:231], v[76:79]\n"
        "ds_read_b32 v130, v242\n"
        "v_mfma_f32_16x16x32_bf16 v[80:83], v[184:187], v[228:231], v[80:83]\n"
        "ds_read_b32 v131, v243\n"
        "v_perm_b32 v242, v141, v248, s72\n"
        "v_mfma_f32_16x16x32_bf16 v[36:39], v[176:179], v[232:235], v[36:39]\n"
        "ds_read_b128 v[228:231], v247 offset:4096\n"
        "v_perm_b32 v243, v141, v248, s73\n"
        "v_mfma_f32_16x16x32_bf16 v[40:43], v[180:183], v[232:235], v[40:43]\n"
        "ds_read_b32 v132, v240\n"
        "v_mfma_f32_16x16x32_bf16 v[44:47], v[184:187], v[232:235], v[44:47]\n"
        "ds_read_b32 v133, v241\n"
        "v_perm_b32 v240, v142, v248, s70\n"
        "v_mfma_f32_16x16x32_bf16 v[108:111], v[188:191], v[232:235], v[108:111]\n"
        "ds_read_b32 v134, v242\n"
        "v_perm_b32 v241, v142, v248, s71\n"
        "v_mfma_f32_16x16x32_bf16 v[84:87], v[176:179], v[236:239], v[84:87]\n"
        "ds_read_b128 v[232:235], v245 offset:6144\n"
        "v_perm_b32 v242, v142, v248, s72\n"
        "v_mfma_f32_16x16x32_bf16 v[88:91], v[180:183], v[236:239], v[88:91]\n"
        "ds_read_b32 v135, v243\n"
        "v_mfma_f32_16x16x32_bf16 v[92:95], v[184:187], v[236:239], v[92:95]\n"
        "s_waitcnt lgkmcnt(12)\n"
        "ds_read_b32 v136, v240\n"
        "v_perm_b32 v243, v142, v248, s73\n"
        "v_mfma_f32_16x16x32_bf16 v[0:3], v[192:195], v[208:211], v[0:3]\n"
        "ds_read_b128 v[236:239], v247 offset:6144\n"
        "v_perm_b32 v240, v143, v248, s70\n"
        "v_mfma_f32_16x16x32_bf16 v[4:7], v[196:199], v[208:211], v[4:7]\n"
        "ds_read_b32 v137, v241\n"
        "v_mfma_f32_16x16x32_bf16 v[8:11], v[200:203], v[208:211], v[8:11]\n"
        "ds_read_b32 v138, v242\n"
        "v_perm_b32 v241, v143, v248, s71\n"
        "v_mfma_f32_16x16x32_bf16 v[96:99], v[204:207], v[208:211], v[96:99]\n"
        "s_barrier\n"
        "v_mfma_f32_16x16x32_bf16 v[48:51], v[192:195], v[212:215], v[48:51]\n"
        "ds_read_b128 v[208:211], v244 offset:16384\n"
        "v_perm_b32 v242, v143, v248, s72\n"
        "v_mfma_f32_16x16x32_bf16 v[52:55], v[196:199], v[212:215], v[52:55]\n"
        "ds_read_b32 v139, v243\n"
        "ds_read_b128 v[156:159], v249 offset:16384\n"
        "v_mfma_f32_16x16x32_bf16 v[56:59], v[200:203], v[212:215], v[56:59]\n"
        "ds_read_b32 v140, v240\n"
        "v_perm_b32 v243, v143, v248, s73\n"
        "v_mfma_f32_16x16x32_bf16 v[12:15], v[192:195], v[216:219], v[12:15]\n"
        "ds_read_b128 v[212:215], v246 offset:16384\n"
        "v_mfma_f32_16x16x32_bf16 v[16:19], v[196:199], v[216:219], v[16:19]\n"
        "ds_read_b32 v141, v241\n"
        "v_mfma_f32_16x16x32_bf16 v[20:23], v[200:203], v[216:219], v[20:23]\n"
        "ds_read_b32 v142, v242\n"
        "v_mfma_f32_16x16x32_bf16 v[100:103], v[204:207], v[216:219], v[100:103]\n"
        "ds_read_b32 v143, v243\n"
        "v_mfma_f32_16x16x32_bf16 v[60:63], v[192:195], v[220:223], v[60:63]\n"
        "s_waitcnt lgkmcnt(5)\n"
        "ds_read_b128 v[216:219], v244 offset:18432\n"
        "v_perm_b32 v240, v156, v248, s70\n"
        "v_mfma_f32_16x16x32_bf16 v[64:67], v[196:199], v[220:223], v[64:67]\n"
        "ds_read_b32 v144, v240\n"
        "v_perm_b32 v241, v156, v248, s71\n"
        "v_mfma_f32_16x16x32_bf16 v[68:71], v[200:203], v[220:223], v[68:71]\n"
        "ds_read_b32 v145, v241\n"
        "v_perm_b32 v242, v156, v248, s72\n"
        "v_mfma_f32_16x16x32_bf16 v[24:27], v[192:195], v[224:227], v[24:27]\n"
        "ds_read_b128 v[220:223], v246 offset:18432\n"
        "v_perm_b32 v243, v156, v248, s73\n"
        "v_mfma_f32_16x16x32_bf16 v[28:31], v[196:199], v[224:227], v[28:31]\n"
        "ds_read_b32 v146, v242\n"
        "v_perm_b32 v240, v157, v248, s70\n"
        "v_mfma_f32_16x16x32_bf16 v[32:35], v[200:203], v[224:227], v[32:35]\n"
        "ds_read_b32 v147, v243\n"
        "v_perm_b32 v241, v157, v248, s71\n"
        "v_mfma_f32_16x16x32_bf16 v[104:107], v[204:207], v[224:227], v[104:107]\n"
        "ds_read_b32 v148, v240\n"
        "v_perm_b32 v242, v157, v248, s72\n"
        "v_mfma_f32_16x16x32_bf16 v[72:75], v[192:195], v[228:231], v[72:75]\n"
        "ds_read_b128 v[224:227], v244 offset:20480\n"
        "v_perm_b32 v243, v157, v248, s73\n"
        "v_mfma_f32_16x16x32_bf16 v[76:79], v[196:199], v[228:231], v[76:79]\n"
        "ds_read_b32 v149, v241\n"
        "v_perm_b32 v240, v158, v248, s70\n"
        "v_mfma_f32_16x16x32_bf16 v[80:83], v[200:203], v[228:231], v[80:83]\n"
        "ds_read_b32 v150, v242\n"
        "v_perm_b32 v241, v158, v248, s71\n"
        "v_mfma_f32_16x16x32_bf16 v[36:39], v[192:195], v[232:235], v[36:39]\n"
        "ds_read_b128 v[228:231], v246 offset:20480\n"
        "v_perm_b32 v242, v158, v248, s72\n"
        "v_mfma_f32_16x16x32_bf16 v[40:43], v[196:199], v[232:235], v[40:43]\n"
        "ds_read_b32 v151, v243\n"
        "v_mfma_f32_16x16x32_bf16 v[44:47], v[200:203], v[232:235], v[44:47]\n"
        "ds_read_b128 v[172:175], v249 offset:20480\n"
        "v_perm_b32 v243, v158, v248, s73\n"
        "v_mfma_f32_16x16x32_bf16 v[108:111], v[204:207], v[232:235], v[108:111]\n"
        "ds_read_b32 v152, v240\n"
        "v_mfma_f32_16x16x32_bf16 v[84:87], v[192:195], v[236:239], v[84:87]\n"
        "ds_read_b128 v[232:235], v244 offset:22528\n"
        "v_perm_b32 v240, v159, v248, s70\n"
        "v_mfma_f32_16x16x32_bf16 v[88:91], v[196:199], v[236:239], v[88:91]\n"
        "ds_read_b32 v153, v241\n"
        "v_mfma_f32_16x16x32_bf16 v[92:95], v[200:203], v[236:239], v[92:95]\n"
        "s_waitcnt lgkmcnt(12)\n"
        "ds_read_b32 v154, v242\n"
        "v_perm_b32 v241, v159, v248, s71\n"
        "v_mfma_f32_16x16x32_bf16 v[0:3], v[112:115], v[208:211], v[0:3]\n"
        "ds_read_b128 v[236:239], v246 offset:22528\n"
        "v_perm_b32 v242, v159, v248, s72\n"
        "v_mfma_f32_16x16x32_bf16 v[4:7], v[116:119], v[208:211], v[4:7]\n"
        "ds_read_b32 v155, v243\n"
        "ds_read_b32 v156, v240\n"
        "v_mfma_f32_16x16x32_bf16 v[8:11], v[120:123], v[208:211], v[8:11]\n"
        "ds_read_b32 v157, v241\n"
        "v_perm_b32 v243, v159, v248, s73\n"
        "v_mfma_f32_16x16x32_bf16 v[96:99], v[124:127], v[208:211], v[96:99]\n"
        "s_waitcnt lgkmcnt(8)\n"
        "ds_read_b32 v158, v242\n"
        "v_perm_b32 v240, v172, v248, s70\n"
        "v_mfma_f32_16x16x32_bf16 v[48:51], v[112:115], v[212:215], v[48:51]\n"
        "ds_read_b128 v[208:211], v245 offset:16384\n"
        "v_perm_b32 v241, v172, v248, s71\n"
        "v_mfma_f32_16x16x32_bf16 v[52:55], v[116:119], v[212:215], v[52:55]\n"
        "ds_read_b32 v159, v243\n"
        "v_perm_b32 v242, v172, v248, s72\n"
        "v_mfma_f32_16x16x32_bf16 v[56:59], v[120:123], v[212:215], v[56:59]\n"
        "ds_read_b32 v160, v240\n"
        "v_perm_b32 v243, v172, v248, s73\n"
        "v_mfma_f32_16x16x32_bf16 v[12:15], v[112:115], v[216:219], v[12:15]\n"
        "ds_read_b128 v[212:215], v247 offset:16384\n"
        "v_perm_b32 v240, v173, v248, s70\n"
        "v_mfma_f32_16x16x32_bf16 v[16:19], v[116:119], v[216:219], v[16:19]\n"
        "ds_read_b32 v161, v241\n"
        "v_mfma_f32_16x16x32_bf16 v[20:23], v[120:123], v[216:219], v[20:23]\n"
        "ds_read_b32 v162, v242\n"
        "v_perm_b32 v241, v173, v248, s71\n"
        "v_mfma_f32_16x16x32_bf16 v[100:103], v[124:127], v[216:219], v[100:103]\n"
        "ds_read_b32 v163, v243\n"
        "v_perm_b32 v242, v173, v248, s72\n"
        "v_mfma_f32_16x16x32_bf16 v[60:63], v[112:115], v[220:223], v[60:63]\n"
        "ds_read_b128 v[216:219], v245 offset:18432\n"
        "v_perm_b32 v243, v173, v248, s73\n"
        "v_mfma_f32_16x16x32_bf16 v[64:67], v[116:119], v[220:223], v[64:67]\n"
        "ds_read_b32 v164, v240\n"
        "v_mfma_f32_16x16x32_bf16 v[68:71], v[120:123], v[220:223], v[68:71]\n"
        "s_waitcnt lgkmcnt(13)\n"
        "ds_read_b32 v165, v241\n"
        "v_perm_b32 v240, v174, v248, s70\n"
        "v_mfma_f32_16x16x32_bf16 v[24:27], v[112:115], v[224:227], v[24:27]\n"
        "ds_read_b128 v[220:223], v247 offset:18432\n"
        "v_perm_b32 v241, v174, v248, s71\n"
        "v_mfma_f32_16x16x32_bf16 v[28:31], v[116:119], v[224:227], v[28:31]\n"
        "ds_read_b32 v166, v242\n"
        "v_mfma_f32_16x16x32_bf16 v[32:35], v[120:123], v[224:227], v[32:35]\n"
        "ds_read_b32 v167, v243\n"
        "v_perm_b32 v242, v174, v248, s72\n"
        "v_mfma_f32_16x16x32_bf16 v[104:107], v[124:127], v[224:227], v[104:107]\n"
        "ds_read_b128 v[188:191], v249 offset:24576\n"
        "v_perm_b32 v243, v174, v248, s73\n"
        "v_mfma_f32_16x16x32_bf16 v[72:75], v[112:115], v[228:231], v[72:75]\n"
        "ds_read_b128 v[224:227], v245 offset:20480\n"
        "v_mfma_f32_16x16x32_bf16 v[76:79], v[116:119], v[228:231], v[76:79]\n"
        "ds_read_b32 v168, v240\n"
        "v_mfma_f32_16x16x32_bf16 v[80:83], v[120:123], v[228:231], v[80:83]\n"
        "ds_read_b32 v169, v241\n"
        "v_perm_b32 v240, v175, v248, s70\n"
        "v_mfma_f32_16x16x32_bf16 v[36:39], v[112:115], v[232:235], v[36:39]\n"
        "ds_read_b128 v[228:231], v247 offset:20480\n"
        "v_perm_b32 v241, v175, v248, s71\n"
        "v_mfma_f32_16x16x32_bf16 v[40:43], v[116:119], v[232:235], v[40:43]\n"
        "ds_read_b32 v170, v242\n"
        "v_mfma_f32_16x16x32_bf16 v[44:47], v[120:123], v[232:235], v[44:47]\n"
        "ds_read_b32 v171, v243\n"
        "v_perm_b32 v242, v175, v248, s72\n"
        "v_mfma_f32_16x16x32_bf16 v[108:111], v[124:127], v[232:235], v[108:111]\n"
        "ds_read_b32 v172, v240\n"
        "v_perm_b32 v243, v175, v248, s73\n"
        "v_mfma_f32_16x16x32_bf16 v[84:87], v[112:115], v[236:239], v[84:87]\n"
        "s_waitcnt lgkmcnt(7)\n"
        "ds_read_b128 v[232:235], v245 offset:22528\n"
        "v_perm_b32 v240, v188, v248, s70\n"
        "v_mfma_f32_16x16x32_bf16 v[88:91], v[116:119], v[236:239], v[88:91]\n"
        "ds_read_b32 v173, v241\n"
        "v_mfma_f32_16x16x32_bf16 v[92:95], v[120:123], v[236:239], v[92:95]\n"
        "ds_read_b32 v174, v242\n"
        "v_perm_b32 v241, v188, v248, s71\n"
        "v_mfma_f32_16x16x32_bf16 v[0:3], v[128:131], v[208:211], v[0:3]\n"
        "ds_read_b128 v[236:239], v247 offset:22528\n"
        "v_perm_b32 v242, v188, v248, s72\n"
        "v_mfma_f32_16x16x32_bf16 v[4:7], v[132:135], v[208:211], v[4:7]\n"
        "ds_read_b32 v175, v243\n"
        "v_mfma_f32_16x16x32_bf16 v[8:11], v[136:139], v[208:211], v[8:11]\n"
        "ds_read_b32 v176, v240\n"
        "v_perm_b32 v243, v188, v248, s73\n"
        "v_mfma_f32_16x16x32_bf16 v[96:99], v[140:143], v[208:211], v[96:99]\n"
        "s_barrier\n"
        "v_mfma_f32_16x16x32_bf16 v[48:51], v[128:131], v[212:215], v[48:51]\n"
        "ds_read_b128 v[208:211], v244 offset:32768\n"
        "v_perm_b32 v240, v189, v248, s70\n"
        "v_mfma_f32_16x16x32_bf16 v[52:55], v[132:135], v[212:215], v[52:55]\n"
        "ds_read_b32 v177, v241\n"
        "ds_read_b32 v178, v242\n"
        "v_mfma_f32_16x16x32_bf16 v[56:59], v[136:139], v[212:215], v[56:59]\n"
        "ds_read_b32 v179, v243\n"
        "v_perm_b32 v241, v189, v248, s71\n"
        "v_mfma_f32_16x16x32_bf16 v[12:15], v[128:131], v[216:219], v[12:15]\n"
        "ds_read_b128 v[212:215], v246 offset:32768\n"
        "v_perm_b32 v242, v189, v248, s72\n"
        "v_mfma_f32_16x16x32_bf16 v[16:19], v[132:135], v[216:219], v[16:19]\n"
        "ds_read_b32 v180, v240\n"
        "v_perm_b32 v243, v189, v248, s73\n"
        "v_mfma_f32_16x16x32_bf16 v[20:23], v[136:139], v[216:219], v[20:23]\n"
        "ds_read_b32 v181, v241\n"
        "v_perm_b32 v240, v190, v248, s70\n"
        "v_mfma_f32_16x16x32_bf16 v[100:103], v[140:143], v[216:219], v[100:103]\n"
        "ds_read_b32 v182, v242\n"
        "v_perm_b32 v241, v190, v248, s71\n"
        "v_mfma_f32_16x16x32_bf16 v[60:63], v[128:131], v[220:223], v[60:63]\n"
        "ds_read_b128 v[216:219], v244 offset:34816\n"
        "v_perm_b32 v242, v190, v248, s72\n"
        "v_mfma_f32_16x16x32_bf16 v[64:67], v[132:135], v[220:223], v[64:67]\n"
        "ds_read_b32 v183, v243\n"
        "v_mfma_f32_16x16x32_bf16 v[68:71], v[136:139], v[220:223], v[68:71]\n"
        "s_waitcnt lgkmcnt(12)\n"
        "ds_read_b128 v[204:207], v249 offset:28672\n"
        "v_perm_b32 v243, v190, v248, s73\n"
        "v_mfma_f32_16x16x32_bf16 v[24:27], v[128:131], v[224:227], v[24:27]\n"
        "ds_read_b128 v[220:223], v246 offset:34816\n"
        "v_mfma_f32_16x16x32_bf16 v[28:31], v[132:135], v[224:227], v[28:31]\n"
        "ds_read_b32 v184, v240\n"
        "v_mfma_f32_16x16x32_bf16 v[32:35], v[136:139], v[224:227], v[32:35]\n"
        "ds_read_b32 v185, v241\n"
        "v_perm_b32 v240, v191, v248, s70\n"
        "v_mfma_f32_16x16x32_bf16 v[104:107], v[140:143], v[224:227], v[104:107]\n"
        "ds_read_b32 v186, v242\n"
        "v_perm_b32 v241, v191, v248, s71\n"
        "v_mfma_f32_16x16x32_bf16 v[72:75], v[128:131], v[228:231], v[72:75]\n"
        "ds_read_b128 v[224:227], v244 offset:36864\n"
        "v_perm_b32 v242, v191, v248, s72\n"
        "v_mfma_f32_16x16x32_bf16 v[76:79], v[132:135], v[228:231], v[76:79]\n"
        "ds_read_b32 v187, v243\n"
        "v_mfma_f32_16x16x32_bf16 v[80:83], v[136:139], v[228:231], v[80:83]\n"
        "ds_read_b32 v188, v240\n"
        "v_perm_b32 v243, v191, v248, s73\n"
        "v_mfma_f32_16x16x32_bf16 v[36:39], v[128:131], v[232:235], v[36:39]\n"
        "s_waitcnt lgkmcnt(7)\n"
        "ds_read_b128 v[228:231], v246 offset:36864\n"
        "v_perm_b32 v240, v204, v248, s70\n"
        "v_mfma_f32_16x16x32_bf16 v[40:43], v[132:135], v[232:235], v[40:43]\n"
        "ds_read_b32 v189, v241\n"
        "v_mfma_f32_16x16x32_bf16 v[44:47], v[136:139], v[232:235], v[44:47]\n"
        "ds_read_b32 v190, v242\n"
        "v_perm_b32 v241, v204, v248, s71\n"
        "v_mfma_f32_16x16x32_bf16 v[108:111], v[140:143], v[232:235], v[108:111]\n"
        "ds_read_b32 v191, v243\n"
        "v_perm_b32 v242, v204, v248, s72\n"
        "v_mfma_f32_16x16x32_bf16 v[84:87], v[128:131], v[236:239], v[84:87]\n"
        "ds_read_b128 v[232:235], v244 offset:38912\n"
        "v_perm_b32 v243, v204, v248, s73\n"
        "v_mfma_f32_16x16x32_bf16 v[88:91], v[132:135], v[236:239], v[88:91]\n"
        "ds_read_b32 v192, v240\n"
        "ds_read_b32 v193, v241\n"
        "v_mfma_f32_16x16x32_bf16 v[92:95], v[136:139], v[236:239], v[92:95]\n"
        "s_waitcnt lgkmcnt(13)\n"
        "ds_read_b32 v194, v242\n"
        "v_perm_b32 v240, v205, v248, s70\n"
        "v_mfma_f32_16x16x32_bf16 v[0:3], v[144:147], v[208:211], v[0:3]\n"
        "ds_read_b128 v[236:239], v246 offset:38912\n"
        "v_perm_b32 v241, v205, v248, s71\n"
        "v_mfma_f32_16x16x32_bf16 v[4:7], v[148:151], v[208:211], v[4:7]\n"
        "ds_read_b32 v195, v243\n"
        "v_perm_b32 v242, v205, v248, s72\n"
        "v_mfma_f32_16x16x32_bf16 v[8:11], v[152:155], v[208:211], v[8:11]\n"
        "ds_read_b32 v196, v240\n"
        "v_perm_b32 v243, v205, v248, s73\n"
        "v_mfma_f32_16x16x32_bf16 v[96:99], v[156:159], v[208:211], v[96:99]\n"
        "ds_read_b32 v197, v241\n"
        "v_perm_b32 v240, v206, v248, s70\n"
        "v_mfma_f32_16x16x32_bf16 v[48:51], v[144:147], v[212:215], v[48:51]\n"
        "ds_read_b128 v[208:211], v245 offset:32768\n"
        "v_perm_b32 v241, v206, v248, s71\n"
        "v_mfma_f32_16x16x32_bf16 v[52:55], v[148:151], v[212:215], v[52:55]\n"
        "ds_read_b32 v198, v242\n"
        "v_mfma_f32_16x16x32_bf16 v[56:59], v[152:155], v[212:215], v[56:59]\n"
        "ds_read_b32 v199, v243\n"
        "v_perm_b32 v242, v206, v248, s72\n"
        "v_mfma_f32_16x16x32_bf16 v[12:15], v[144:147], v[216:219], v[12:15]\n"
        "ds_read_b128 v[212:215], v247 offset:32768\n"
        "v_perm_b32 v243, v206, v248, s73\n"
        "v_mfma_f32_16x16x32_bf16 v[16:19], v[148:151], v[216:219], v[16:19]\n"
        "ds_read_b128 v[124:127], v249 offset:0\n"
        "v_mfma_f32_16x16x32_bf16 v[20:23], v[152:155], v[216:219], v[20:23]\n"
        "ds_read_b32 v200, v240\n"
        "v_mfma_f32_16x16x32_bf16 v[100:103], v[156:159], v[216:219], v[100:103]\n"
        "ds_read_b32 v201, v241\n"
        "v_perm_b32 v240, v207, v248, s70\n"
        "v_mfma_f32_16x16x32_bf16 v[60:63], v[144:147], v[220:223], v[60:63]\n"
        "ds_read_b128 v[216:219], v245 offset:34816\n"
        "v_perm_b32 v241, v207, v248, s71\n"
        "v_mfma_f32_16x16x32_bf16 v[64:67], v[148:151], v[220:223], v[64:67]\n"
        "ds_read_b32 v202, v242\n"
        "v_mfma_f32_16x16x32_bf16 v[68:71], v[152:155], v[220:223], v[68:71]\n"
        "s_waitcnt lgkmcnt(12)\n"
        "ds_read_b32 v203, v243\n"
        "v_perm_b32 v242, v207, v248, s72\n"
        "v_mfma_f32_16x16x32_bf16 v[24:27], v[144:147], v[224:227], v[24:27]\n"
        "ds_read_b128 v[220:223], v247 offset:34816\n"
        "v_perm_b32 v243, v207, v248, s73\n"
        "v_mfma_f32_16x16x32_bf16 v[28:31], v[148:151], v[224:227], v[28:31]\n"
        "ds_read_b32 v204, v240\n"
        "v_mfma_f32_16x16x32_bf16 v[32:35], v[152:155], v[224:227], v[32:35]\n"
        "s_waitcnt lgkmcnt(7)\n"
        "ds_read_b32 v205, v241\n"
        "v_perm_b32 v240, v124, v248, s70\n"
        "v_mfma_f32_16x16x32_bf16 v[104:107], v[156:159], v[224:227], v[104:107]\n"
        "ds_read_b32 v206, v242\n"
        "v_perm_b32 v241, v124, v248, s71\n"
        "v_mfma_f32_16x16x32_bf16 v[72:75], v[144:147], v[228:231], v[72:75]\n"
        "ds_read_b128 v[224:227], v245 offset:36864\n"
        "v_perm_b32 v242, v124, v248, s72\n"
        "v_mfma_f32_16x16x32_bf16 v[76:79], v[148:151], v[228:231], v[76:79]\n"
        "ds_read_b32 v207, v243\n"
        "ds_read_b32 v112, v240\n"
        "v_mfma_f32_16x16x32_bf16 v[80:83], v[152:155], v[228:231], v[80:83]\n"
        "ds_read_b32 v113, v241\n"
        "v_perm_b32 v243, v124, v248, s73\n"
        "v_mfma_f32_16x16x32_bf16 v[36:39], v[144:147], v[232:235], v[36:39]\n"
        "ds_read_b128 v[228:231], v247 offset:36864\n"
        "v_perm_b32 v240, v125, v248, s70\n"
        "v_mfma_f32_16x16x32_bf16 v[40:43], v[148:151], v[232:235], v[40:43]\n"
        "ds_read_b32 v114, v242\n"
        "v_perm_b32 v241, v125, v248, s71\n"
        "v_mfma_f32_16x16x32_bf16 v[44:47], v[152:155], v[232:235], v[44:47]\n"
        "ds_read_b32 v115, v243\n"
        "v_perm_b32 v242, v125, v248, s72\n"
        "v_mfma_f32_16x16x32_bf16 v[108:111], v[156:159], v[232:235], v[108:111]\n"
        "ds_read_b32 v116, v240\n"
        "v_perm_b32 v243, v125, v248, s73\n"
        "v_mfma_f32_16x16x32_bf16 v[84:87], v[144:147], v[236:239], v[84:87]\n"
        "ds_read_b128 v[232:235], v245 offset:38912\n"
        "v_perm_b32 v240, v126, v248, s70\n"
        "v_mfma_f32_16x16x32_bf16 v[88:91], v[148:151], v[236:239], v[88:91]\n"
        "ds_read_b32 v117, v241\n"
        "v_mfma_f32_16x16x32_bf16 v[92:95], v[152:155], v[236:239], v[92:95]\n"
        "s_waitcnt lgkmcnt(13)\n"
        "ds_read_b32 v118, v242\n"
        "v_perm_b32 v241, v126, v248, s71\n"
        "v_mfma_f32_16x16x32_bf16 v[0:3], v[160:163], v[208:211], v[0:3]\n"
        "ds_read_b128 v[236:239], v247 offset:38912\n"
        "v_perm_b32 v242, v126, v248, s72\n"
        "v_mfma_f32_16x16x32_bf16 v[4:7], v[164:167], v[208:211], v[4:7]\n"
        "ds_read_b32 v119, v243\n"
        "v_mfma_f32_16x16x32_bf16 v[8:11], v[168:171], v[208:211], v[8:11]\n"
        "ds_read_b128 v[140:143], v249 offset:4096\n"
        "v_perm_b32 v243, v126, v248, s73\n"
        "v_mfma_f32_16x16x32_bf16 v[96:99], v[172:175], v[208:211], v[96:99]\n"
        "s_barrier\n"
        "v_mfma_f32_16x16x32_bf16 v[48:51], v[160:163], v[212:215], v[48:51]\n"
        "ds_read_b128 v[208:211], v244 offset:49152\n"
        "v_mfma_f32_16x16x32_bf16 v[52:55], v[164:167], v[212:215], v[52:55]\n"
        "ds_read_b32 v120, v240\n"
        "v_mfma_f32_16x16x32_bf16 v[56:59], v[168:171], v[212:215], v[56:59]\n"
        "ds_read_b32 v121, v241\n"
        "v_perm_b32 v240, v127, v248, s70\n"
        "v_mfma_f32_16x16x32_bf16 v[12:15], v[160:163], v[216:219], v[12:15]\n"
        "ds_read_b128 v[212:215], v246 offset:49152\n"
        "v_perm_b32 v241, v127, v248, s71\n"
        "v_mfma_f32_16x16x32_bf16 v[16:19], v[164:167], v[216:219], v[16:19]\n"
        "ds_read_b32 v122, v242\n"
        "v_mfma_f32_16x16x32_bf16 v[20:23], v[168:171], v[216:219], v[20:23]\n"
        "ds_read_b32 v123, v243\n"
        "v_perm_b32 v242, v127, v248, s72\n"
        "v_mfma_f32_16x16x32_bf16 v[100:103], v[172:175], v[216:219], v[100:103]\n"
        "ds_read_b32 v124, v240\n"
        "v_perm_b32 v243, v127, v248, s73\n"
        "v_mfma_f32_16x16x32_bf16 v[60:63], v[160:163], v[220:223], v[60:63]\n"
        "s_waitcnt lgkmcnt(7)\n"
        "ds_read_b128 v[216:219], v244 offset:51200\n"
        "v_perm_b32 v240, v140, v248, s70\n"
        "v_mfma_f32_16x16x32_bf16 v[64:67], v[164:167], v[220:223], v[64:67]\n"
        "ds_read_b32 v125, v241\n"
        "ds_read_b32 v126, v242\n"
        "v_mfma_f32_16x16x32_bf16 v[68:71], v[168:171], v[220:223], v[68:71]\n"
        "ds_read_b32 v127, v243\n"
        "v_perm_b32 v241, v140, v248, s71\n"
        "v_mfma_f32_16x16x32_bf16 v[24:27], v[160:163], v[224:227], v[24:27]\n"
        "ds_read_b128 v[220:223], v246 offset:51200\n"
        "v_perm_b32 v242, v140, v248, s72\n"
        "v_mfma_f32_16x16x32_bf16 v[28:31], v[164:167], v[224:227], v[28:31]\n"
        "ds_read_b32 v128, v240\n"
        "v_perm_b32 v243, v140, v248, s73\n"
        "v_mfma_f32_16x16x32_bf16 v[32:35], v[168:171], v[224:227], v[32:35]\n"
        "ds_read_b32 v129, v241\n"
        "v_perm_b32 v240, v141, v248, s70\n"
        "v_mfma_f32_16x16x32_bf16 v[104:107], v[172:175], v[224:227], v[104:107]\n"
        "ds_read_b32 v130, v242\n"
        "v_perm_b32 v241, v141, v248, s71\n"
        "v_mfma_f32_16x16x32_bf16 v[72:75], v[160:163], v[228:231], v[72:75]\n"
        "ds_read_b128 v[224:227], v244 offset:53248\n"
        "v_perm_b32 v242, v141, v248, s72\n"
        "v_mfma_f32_16x16x32_bf16 v[76:79], v[164:167], v[228:231], v[76:79]\n"
        "ds_read_b32 v131, v243\n"
        "v_mfma_f32_16x16x32_bf16 v[80:83], v[168:171], v[228:231], v[80:83]\n"
        "ds_read_b32 v132, v240\n"
        "v_perm_b32 v243, v141, v248, s73\n"
        "v_mfma_f32_16x16x32_bf16 v[36:39], v[160:163], v[232:235], v[36:39]\n"
        "ds_read_b128 v[228:231], v246 offset:53248\n"
        "v_perm_b32 v240, v142, v248, s70\n"
        "v_mfma_f32_16x16x32_bf16 v[40:43], v[164:167], v[232:235], v[40:43]\n"
        "ds_read_b32 v133, v241\n"
        "v_mfma_f32_16x16x32_bf16 v[44:47], v[168:171], v[232:235], v[44:47]\n"
        "ds_read_b32 v134, v242\n"
        "v_perm_b32 v241, v142, v248, s71\n"
        "v_mfma_f32_16x16x32_bf16 v[108:111], v[172:175], v[232:235], v[108:111]\n"
        "ds_read_b32 v135, v243\n"
        "v_perm_b32 v242, v142, v248, s72\n"
        "v_mfma_f32_16x16x32_bf16 v[84:87], v[160:163], v[236:239], v[84:87]\n"
        "ds_read_b128 v[232:235], v244 offset:55296\n"
        "v_perm_b32 v243, v142, v248, s73\n"
        "v_mfma_f32_16x16x32_bf16 v[88:91], v[164:167], v[236:239], v[88:91]\n"
        "ds_read_b128 v[156:159], v249 offset:8192\n"
        "v_mfma_f32_16x16x32_bf16 v[92:95], v[168:171], v[236:239], v[92:95]\n"
        "s_waitcnt lgkmcnt(12)\n"
        "ds_read_b32 v136, v240\n"
        "v_mfma_f32_16x16x32_bf16 v[0:3], v[176:179], v[208:211], v[0:3]\n"
        "ds_read_b128 v[236:239], v246 offset:55296\n"
        "v_perm_b32 v240, v143, v248, s70\n"
        "v_mfma_f32_16x16x32_bf16 v[4:7], v[180:183], v[208:211], v[4:7]\n"
        "ds_read_b32 v137, v241\n"
        "v_mfma_f32_16x16x32_bf16 v[8:11], v[184:187], v[208:211], v[8:11]\n"
        "ds_read_b32 v138, v242\n"
        "v_perm_b32 v241, v143, v248, s71\n"
        "v_mfma_f32_16x16x32_bf16 v[96:99], v[188:191], v[208:211], v[96:99]\n"
        "ds_read_b32 v139, v243\n"
        "v_perm_b32 v242, v143, v248, s72\n"
        "v_mfma_f32_16x16x32_bf16 v[48:51], v[176:179], v[212:215], v[48:51]\n"
        "ds_read_b128 v[208:211], v245 offset:49152\n"
        "v_perm_b32 v243, v143, v248, s73\n"
        "v_mfma_f32_16x16x32_bf16 v[52:55], v[180:183], v[212:215], v[52:55]\n"
        "ds_read_b32 v140, v240\n"
        "ds_read_b32 v141, v241\n"
        "v_mfma_f32_16x16x32_bf16 v[56:59], v[184:187], v[212:215], v[56:59]\n"
        "s_waitcnt lgkmcnt(8)\n"
        "ds_read_b32 v142, v242\n"
        "v_perm_b32 v240, v156, v248, s70\n"
        "v_mfma_f32_16x16x32_bf16 v[12:15], v[176:179], v[216:219], v[12:15]\n"
        "ds_read_b128 v[212:215], v247 offset:49152\n"
        "v_perm_b32 v241, v156, v248, s71\n"
        "v_mfma_f32_16x16x32_bf16 v[16:19], v[180:183], v[216:219], v[16:19]\n"
        "ds_read_b32 v143, v243\n"
        "v_perm_b32 v242, v156, v248, s72\n"
        "v_mfma_f32_16x16x32_bf16 v[20:23], v[184:187], v[216:219], v[20:23]\n"
        "ds_read_b32 v144, v240\n"
        "v_perm_b32 v243, v156, v248, s73\n"
        "v_mfma_f32_16x16x32_bf16 v[100:103], v[188:191], v[216:219], v[100:103]\n"
        "ds_read_b32 v145, v241\n"
        "v_perm_b32 v240, v157, v248, s70\n"
        "v_mfma_f32_16x16x32_bf16 v[60:63], v[176:179], v[220:223], v[60:63]\n"
        "ds_read_b128 v[216:219], v245 offset:51200\n"
        "v_perm_b32 v241, v157, v248, s71\n"
        "v_mfma_f32_16x16x32_bf16 v[64:67], v[180:183], v[220:223], v[64:67]\n"
        "ds_read_b32 v146, v242\n"
        "v_mfma_f32_16x16x32_bf16 v[68:71], v[184:187], v[220:223], v[68:71]\n"
        "s_waitcnt lgkmcnt(13)\n"
        "ds_read_b32 v147, v243\n"
        "v_perm_b32 v242, v157, v248, s72\n"
        "v_mfma_f32_16x16x32_bf16 v[24:27], v[176:179], v[224:227], v[24:27]\n"
        "ds_read_b128 v[220:223], v247 offset:51200\n"
        "v_perm_b32 v243, v157, v248, s73\n"
        "v_mfma_f32_16x16x32_bf16 v[28:31], v[180:183], v[224:227], v[28:31]\n"
        "ds_read_b32 v148, v240\n"
        "v_mfma_f32_16x16x32_bf16 v[32:35], v[184:187], v[224:227], v[32:35]\n"
        "ds_read_b32 v149, v241\n"
        "v_perm_b32 v240, v158, v248, s70\n"
        "v_mfma_f32_16x16x32_bf16 v[104:107], v[188:191], v[224:227], v[104:107]\n"
        "ds_read_b32 v150, v242\n"
        "v_perm_b32 v241, v158, v248, s71\n"
        "v_mfma_f32_16x16x32_bf16 v[72:75], v[176:179], v[228:231], v[72:75]\n"
        "ds_read_b128 v[224:227], v245 offset:53248\n"
        "v_perm_b32 v242, v158, v248, s72\n"
        "v_mfma_f32_16x16x32_bf16 v[76:79], v[180:183], v[228:231], v[76:79]\n"
        "ds_read_b32 v151, v243\n"
        "v_mfma_f32_16x16x32_bf16 v[80:83], v[184:187], v[228:231], v[80:83]\n"
        "ds_read_b128 v[172:175], v249 offset:12288\n"
        "v_perm_b32 v243, v158, v248, s73\n"
        "v_mfma_f32_16x16x32_bf16 v[36:39], v[176:179], v[232:235], v[36:39]\n"
        "ds_read_b128 v[228:231], v247 offset:53248\n"
        "v_mfma_f32_16x16x32_bf16 v[40:43], v[180:183], v[232:235], v[40:43]\n"
        "ds_read_b32 v152, v240\n"
        "v_mfma_f32_16x16x32_bf16 v[44:47], v[184:187], v[232:235], v[44:47]\n"
        "ds_read_b32 v153, v241\n"
        "v_perm_b32 v240, v159, v248, s70\n"
        "v_mfma_f32_16x16x32_bf16 v[108:111], v[188:191], v[232:235], v[108:111]\n"
        "ds_read_b32 v154, v242\n"
        "v_perm_b32 v241, v159, v248, s71\n"
        "v_mfma_f32_16x16x32_bf16 v[84:87], v[176:179], v[236:239], v[84:87]\n"
        "ds_read_b128 v[232:235], v245 offset:55296\n"
        "v_perm_b32 v242, v159, v248, s72\n"
        "v_mfma_f32_16x16x32_bf16 v[88:91], v[180:183], v[236:239], v[88:91]\n"
        "ds_read_b32 v155, v243\n"
        "ds_read_b32 v156, v240\n"
        "v_mfma_f32_16x16x32_bf16 v[92:95], v[184:187], v[236:239], v[92:95]\n"
        "s_waitcnt lgkmcnt(13)\n"
        "ds_read_b32 v157, v241\n"
        "v_perm_b32 v243, v159, v248, s73\n"
        "v_mfma_f32_16x16x32_bf16 v[0:3], v[192:195], v[208:211], v[0:3]\n"
        "s_waitcnt lgkmcnt(8)\n"
        "ds_read_b128 v[236:239], v247 offset:55296\n"
        "v_perm_b32 v240, v172, v248, s70\n"
        "v_mfma_f32_16x16x32_bf16 v[4:7], v[196:199], v[208:211], v[4:7]\n"
        "ds_read_b32 v158, v242\n"
        "v_perm_b32 v241, v172, v248, s71\n"
        "v_mfma_f32_16x16x32_bf16 v[8:11], v[200:203], v[208:211], v[8:11]\n"
        "ds_read_b32 v159, v243\n"
        "v_perm_b32 v242, v172, v248, s72\n"
        "v_mfma_f32_16x16x32_bf16 v[96:99], v[204:207], v[208:211], v[96:99]\n"
        "s_waitcnt lgkmcnt(2)\n"
        "s_barrier\n"
        "v_mfma_f32_16x16x32_bf16 v[48:51], v[192:195], v[212:215], v[48:51]\n"
        "ds_read_b128 v[208:211], v244 offset:0\n"
        "v_perm_b32 v243, v172, v248, s73\n"
        "v_mfma_f32_16x16x32_bf16 v[52:55], v[196:199], v[212:215], v[52:55]\n"
        "ds_read_b32 v160, v240\n"
        "v_mfma_f32_16x16x32_bf16 v[56:59], v[200:203], v[212:215], v[56:59]\n"
        "ds_read_b32 v161, v241\n"
        "v_perm_b32 v240, v173, v248, s70\n"
        "v_mfma_f32_16x16x32_bf16 v[12:15], v[192:195], v[216:219], v[12:15]\n"
        "ds_read_b128 v[212:215], v246 offset:0\n"
        "v_perm_b32 v241, v173, v248, s71\n"
        "v_mfma_f32_16x16x32_bf16 v[16:19], v[196:199], v[216:219], v[16:19]\n"
        "ds_read_b32 v162, v242\n"
        "v_mfma_f32_16x16x32_bf16 v[20:23], v[200:203], v[216:219], v[20:23]\n"
        "ds_read_b32 v163, v243\n"
        "v_perm_b32 v242, v173, v248, s72\n"
        "v_mfma_f32_16x16x32_bf16 v[100:103], v[204:207], v[216:219], v[100:103]\n"
        "ds_read_b32 v164, v240\n"
        "v_perm_b32 v243, v173, v248, s73\n"
        "v_mfma_f32_16x16x32_bf16 v[60:63], v[192:195], v[220:223], v[60:63]\n"
        "ds_read_b128 v[216:219], v244 offset:2048\n"
        "v_perm_b32 v240, v174, v248, s70\n"
        "v_mfma_f32_16x16x32_bf16 v[64:67], v[196:199], v[220:223], v[64:67]\n"
        "ds_read_b32 v165, v241\n"
        "v_mfma_f32_16x16x32_bf16 v[68:71], v[200:203], v[220:223], v[68:71]\n"
        "ds_read_b32 v166, v242\n"
        "v_perm_b32 v241, v174, v248, s71\n"
        "v_mfma_f32_16x16x32_bf16 v[24:27], v[192:195], v[224:227], v[24:27]\n"
        "ds_read_b128 v[220:223], v246 offset:2048\n"
        "v_perm_b32 v242, v174, v248, s72\n"
        "v_mfma_f32_16x16x32_bf16 v[28:31], v[196:199], v[224:227], v[28:31]\n"
        "ds_read_b32 v167, v243\n"
        "v_mfma_f32_16x16x32_bf16 v[32:35], v[200:203], v[224:227], v[32:35]\n"
        "ds_read_b128 v[188:191], v249 offset:16384\n"
        "v_perm_b32 v243, v174, v248, s73\n"
        "v_mfma_f32_16x16x32_bf16 v[104:107], v[204:207], v[224:227], v[104:107]\n"
        "ds_read_b32 v168, v240\n"
        "v_mfma_f32_16x16x32_bf16 v[72:75], v[192:195], v[228:231], v[72:75]\n"
        "ds_read_b128 v[224:227], v244 offset:4096\n"
        "v_perm_b32 v240, v175, v248, s70\n"
        "v_mfma_f32_16x16x32_bf16 v[76:79], v[196:199], v[228:231], v[76:79]\n"
        "ds_read_b32 v169, v241\n"
        "ds_read_b32 v170, v242\n"
        "v_mfma_f32_16x16x32_bf16 v[80:83], v[200:203], v[228:231], v[80:83]\n"
        "ds_read_b32 v171, v243\n"
        "v_perm_b32 v241, v175, v248, s71\n"
        "v_mfma_f32_16x16x32_bf16 v[36:39], v[192:195], v[232:235], v[36:39]\n"
        "ds_read_b128 v[228:231], v246 offset:4096\n"
        "v_perm_b32 v242, v175, v248, s72\n"
        "v_mfma_f32_16x16x32_bf16 v[40:43], v[196:199], v[232:235], v[40:43]\n"
        "ds_read_b32 v172, v240\n"
        "v_perm_b32 v243, v175, v248, s73\n"
        "v_mfma_f32_16x16x32_bf16 v[44:47], v[200:203], v[232:235], v[44:47]\n"
        "s_waitcnt lgkmcnt(7)\n"
        "ds_read_b32 v173, v241\n"
        "v_perm_b32 v240, v188, v248, s70\n"
        "v_mfma_f32_16x16x32_bf16 v[108:111], v[204:207], v[232:235], v[108:111]\n"
        "ds_read_b32 v174, v242\n"
        "v_perm_b32 v241, v188, v248, s71\n"
        "v_mfma_f32_16x16x32_bf16 v[84:87], v[192:195], v[236:239], v[84:87]\n"
        "ds_read_b128 v[232:235], v244 offset:6144\n"
        "v_perm_b32 v242, v188, v248, s72\n"
        "v_mfma_f32_16x16x32_bf16 v[88:91], v[196:199], v[236:239], v[88:91]\n"
        "ds_read_b32 v175, v243\n"
        "v_mfma_f32_16x16x32_bf16 v[92:95], v[200:203], v[236:239], v[92:95]\n"
        "ds_read_b32 v176, v240\n"
        "v_perm_b32 v243, v188, v248, s73\n"
        "v_mfma_f32_16x16x32_bf16 v[0:3], v[112:115], v[208:211], v[0:3]\n"
        "ds_read_b128 v[236:239], v246 offset:6144\n"
        "v_perm_b32 v240, v189, v248, s70\n"
        "v_mfma_f32_16x16x32_bf16 v[4:7], v[116:119], v[208:211], v[4:7]\n"
        "ds_read_b32 v177, v241\n"
        "v_mfma_f32_16x16x32_bf16 v[8:11], v[120:123], v[208:211], v[8:11]\n"
        "ds_read_b32 v178, v242\n"
        "v_perm_b32 v241, v189, v248, s71\n"
        "v_mfma_f32_16x16x32_bf16 v[96:99], v[124:127], v[208:211], v[96:99]\n"
        "ds_read_b32 v179, v243\n"
        "v_perm_b32 v242, v189, v248, s72\n"
        "v_mfma_f32_16x16x32_bf16 v[48:51], v[112:115], v[212:215], v[48:51]\n"
        "ds_read_b128 v[208:211], v245 offset:0\n"
        "v_perm_b32 v243, v189, v248, s73\n"
        "v_mfma_f32_16x16x32_bf16 v[52:55], v[116:119], v[212:215], v[52:55]\n"
        "ds_read_b32 v180, v240\n"
        "v_mfma_f32_16x16x32_bf16 v[56:59], v[120:123], v[212:215], v[56:59]\n"
        "ds_read_b32 v181, v241\n"
        "v_perm_b32 v240, v190, v248, s70\n"
        "v_mfma_f32_16x16x32_bf16 v[12:15], v[112:115], v[216:219], v[12:15]\n"
        "ds_read_b128 v[212:215], v247 offset:0\n"
        "v_perm_b32 v241, v190, v248, s71\n"
        "v_mfma_f32_16x16x32_bf16 v[16:19], v[116:119], v[216:219], v[16:19]\n"
        "ds_read_b32 v182, v242\n"
        "v_mfma_f32_16x16x32_bf16 v[20:23], v[120:123], v[216:219], v[20:23]\n"
        "ds_read_b32 v183, v243\n"
        "v_perm_b32 v242, v190, v248, s72\n"
        "v_mfma_f32_16x16x32_bf16 v[100:103], v[124:127], v[216:219], v[100:103]\n"
        "ds_read_b128 v[204:207], v249 offset:20480\n"
        "v_perm_b32 v243, v190, v248, s73\n"
        "v_mfma_f32_16x16x32_bf16 v[60:63], v[112:115], v[220:223], v[60:63]\n"
        "ds_read_b128 v[216:219], v245 offset:2048\n"
        "v_mfma_f32_16x16x32_bf16 v[64:67], v[116:119], v[220:223], v[64:67]\n"
        "ds_read_b32 v184, v240\n"
        "v_mfma_f32_16x16x32_bf16 v[68:71], v[120:123], v[220:223], v[68:71]\n"
        "s_waitcnt lgkmcnt(12)\n"
        "ds_read_b32 v185, v241\n"
        "v_perm_b32 v240, v191, v248, s70\n"
        "v_mfma_f32_16x16x32_bf16 v[24:27], v[112:115], v[224:227], v[24:27]\n"
        "ds_read_b128 v[220:223], v247 offset:2048\n"
        "v_perm_b32 v241, v191, v248, s71\n"
        "v_mfma_f32_16x16x32_bf16 v[28:31], v[116:119], v[224:227], v[28:31]\n"
        "ds_read_b32 v186, v242\n"
        "v_mfma_f32_16x16x32_bf16 v[32:35], v[120:123], v[224:227], v[32:35]\n"
        "ds_read_b32 v187, v243\n"
        "v_perm_b32 v242, v191, v248, s72\n"
        "v_mfma_f32_16x16x32_bf16 v[104:107], v[124:127], v[224:227], v[104:107]\n"
        "ds_read_b32 v188, v240\n"
        "v_perm_b32 v243, v191, v248, s73\n"
        "v_mfma_f32_16x16x32_bf16 v[72:75], v[112:115], v[228:231], v[72:75]\n"
        "s_waitcnt lgkmcnt(7)\n"
        "ds_read_b128 v[224:227], v245 offset:4096\n"
        "v_perm_b32 v240, v204, v248, s70\n"
        "v_mfma_f32_16x16x32_bf16 v[76:79], v[116:119], v[228:231], v[76:79]\n"
        "ds_read_b32 v189, v241\n"
        "v_mfma_f32_16x16x32_bf16 v[80:83], v[120:123], v[228:231], v[80:83]\n"
        "ds_read_b32 v190, v242\n"
        "v_perm_b32 v241, v204, v248, s71\n"
        "v_mfma_f32_16x16x32_bf16 v[36:39], v[112:115], v[232:235], v[36:39]\n"
        "ds_read_b128 v[228:231], v247 offset:4096\n"
        "v_perm_b32 v242, v204, v248, s72\n"
        "v_mfma_f32_16x16x32_bf16 v[40:43], v[116:119], v[232:235], v[40:43]\n"
        "ds_read_b32 v191, v243\n"
        "v_mfma_f32_16x16x32_bf16 v[44:47], v[120:123], v[232:235], v[44:47]\n"
        "ds_read_b32 v192, v240\n"
        "v_perm_b32 v243, v204, v248, s73\n"
        "v_mfma_f32_16x16x32_bf16 v[108:111], v[124:127], v[232:235], v[108:111]\n"
        "ds_read_b32 v193, v241\n"
        "v_perm_b32 v240, v205, v248, s70\n"
        "v_mfma_f32_16x16x32_bf16 v[84:87], v[112:115], v[236:239], v[84:87]\n"
        "ds_read_b128 v[232:235], v245 offset:6144\n"
        "v_perm_b32 v241, v205, v248, s71\n"
        "v_mfma_f32_16x16x32_bf16 v[88:91], v[116:119], v[236:239], v[88:91]\n"
        "ds_read_b32 v194, v242\n"
        "v_mfma_f32_16x16x32_bf16 v[92:95], v[120:123], v[236:239], v[92:95]\n"
        "s_waitcnt lgkmcnt(12)\n"
        "ds_read_b32 v195, v243\n"
        "v_perm_b32 v242, v205, v248, s72\n"
        "v_mfma_f32_16x16x32_bf16 v[0:3], v[128:131], v[208:211], v[0:3]\n"
        "ds_read_b128 v[236:239], v247 offset:6144\n"
        "v_perm_b32 v243, v205, v248, s73\n"
        "v_mfma_f32_16x16x32_bf16 v[4:7], v[132:135], v[208:211], v[4:7]\n"
        "ds_read_b32 v196, v240\n"
        "v_mfma_f32_16x16x32_bf16 v[8:11], v[136:139], v[208:211], v[8:11]\n"
        "ds_read_b32 v197, v241\n"
        "v_perm_b32 v240, v206, v248, s70\n"
        "v_mfma_f32_16x16x32_bf16 v[96:99], v[140:143], v[208:211], v[96:99]\n"
        "s_barrier\n"
        "v_mfma_f32_16x16x32_bf16 v[48:51], v[128:131], v[212:215], v[48:51]\n"
        "ds_read_b128 v[208:211], v244 offset:16384\n"
        "v_perm_b32 v241, v206, v248, s71\n"
        "v_mfma_f32_16x16x32_bf16 v[52:55], v[132:135], v[212:215], v[52:55]\n"
        "ds_read_b32 v198, v242\n"
        "v_mfma_f32_16x16x32_bf16 v[56:59], v[136:139], v[212:215], v[56:59]\n"
        "ds_read_b32 v199, v243\n"
        "v_perm_b32 v242, v206, v248, s72\n"
        "v_mfma_f32_16x16x32_bf16 v[12:15], v[128:131], v[216:219], v[12:15]\n"
        "ds_read_b128 v[212:215], v246 offset:16384\n"
        "v_perm_b32 v243, v206, v248, s73\n"
        "v_mfma_f32_16x16x32_bf16 v[16:19], v[132:135], v[216:219], v[16:19]\n"
        "ds_read_b128 v[124:127], v249 offset:24576\n"
        "v_mfma_f32_16x16x32_bf16 v[20:23], v[136:139], v[216:219], v[20:23]\n"
        "ds_read_b32 v200, v240\n"
        "v_mfma_f32_16x16x32_bf16 v[100:103], v[140:143], v[216:219], v[100:103]\n"
        "ds_read_b32 v201, v241\n"
        "v_perm_b32 v240, v207, v248, s70\n"
        "v_mfma_f32_16x16x32_bf16 v[60:63], v[128:131], v[220:223], v[60:63]\n"
        "ds_read_b128 v[216:219], v244 offset:18432\n"
        "v_perm_b32 v241, v207, v248, s71\n"
        "v_mfma_f32_16x16x32_bf16 v[64:67], v[132:135], v[220:223], v[64:67]\n"
        "ds_read_b32 v202, v242\n"
        "v_mfma_f32_16x16x32_bf16 v[68:71], v[136:139], v[220:223], v[68:71]\n"
        "s_waitcnt lgkmcnt(11)\n"
        "ds_read_b32 v203, v243\n"
        "v_perm_b32 v242, v207, v248, s72\n"
        "v_mfma_f32_16x16x32_bf16 v[24:27], v[128:131], v[224:227], v[24:27]\n"
        "ds_read_b128 v[220:223], v246 offset:18432\n"
        "v_perm_b32 v243, v207, v248, s73\n"
        "v_mfma_f32_16x16x32_bf16 v[28:31], v[132:135], v[224:227], v[28:31]\n"
        "ds_read_b32 v204, v240\n"
        "v_mfma_f32_16x16x32_bf16 v[32:35], v[136:139], v[224:227], v[32:35]\n"
        "s_waitcnt lgkmcnt(7)\n"
        "ds_read_b32 v205, v241\n"
        "v_perm_b32 v240, v124, v248, s70\n"
        "v_mfma_f32_16x16x32_bf16 v[104:107], v[140:143], v[224:227], v[104:107]\n"
        "ds_read_b32 v206, v242\n"
        "v_perm_b32 v241, v124, v248, s71\n"
        "v_mfma_f32_16x16x32_bf16 v[72:75], v[128:131], v[228:231], v[72:75]\n"
        "ds_read_b128 v[224:227], v244 offset:20480\n"
        "v_perm_b32 v242, v124, v248, s72\n"
        "v_mfma_f32_16x16x32_bf16 v[76:79], v[132:135], v[228:231], v[76:79]\n"
        "ds_read_b32 v207, v243\n"
        "v_mfma_f32_16x16x32_bf16 v[80:83], v[136:139], v[228:231], v[80:83]\n"
        "ds_read_b32 v112, v240\n"
        "v_perm_b32 v243, v124, v248, s73\n"
        "v_mfma_f32_16x16x32_bf16 v[36:39], v[128:131], v[232:235], v[36:39]\n"
        "ds_read_b128 v[228:231], v246 offset:20480\n"
        "v_perm_b32 v240, v125, v248, s70\n"
        "v_mfma_f32_16x16x32_bf16 v[40:43], v[132:135], v[232:235], v[40:43]\n"
        "ds_read_b32 v113, v241\n"
        "v_mfma_f32_16x16x32_bf16 v[44:47], v[136:139], v[232:235], v[44:47]\n"
        "ds_read_b32 v114, v242\n"
        "v_perm_b32 v241, v125, v248, s71\n"
        "v_mfma_f32_16x16x32_bf16 v[108:111], v[140:143], v[232:235], v[108:111]\n"
        "ds_read_b32 v115, v243\n"
        "v_perm_b32 v242, v125, v248, s72\n"
        "v_mfma_f32_16x16x32_bf16 v[84:87], v[128:131], v[236:239], v[84:87]\n"
        "ds_read_b128 v[232:235], v244 offset:22528\n"
        "v_perm_b32 v243, v125, v248, s73\n"
        "v_mfma_f32_16x16x32_bf16 v[88:91], v[132:135], v[236:239], v[88:91]\n"
        "ds_read_b32 v116, v240\n"
        "v_mfma_f32_16x16x32_bf16 v[92:95], v[136:139], v[236:239], v[92:95]\n"
        "s_waitcnt lgkmcnt(12)\n"
        "ds_read_b32 v117, v241\n"
        "v_perm_b32 v240, v126, v248, s70\n"
        "v_mfma_f32_16x16x32_bf16 v[0:3], v[144:147], v[208:211], v[0:3]\n"
        "ds_read_b128 v[236:239], v246 offset:22528\n"
        "v_perm_b32 v241, v126, v248, s71\n"
        "v_mfma_f32_16x16x32_bf16 v[4:7], v[148:151], v[208:211], v[4:7]\n"
        "ds_read_b32 v118, v242\n"
        "v_mfma_f32_16x16x32_bf16 v[8:11], v[152:155], v[208:211], v[8:11]\n"
        "ds_read_b32 v119, v243\n"
        "v_perm_b32 v242, v126, v248, s72\n"
        "v_mfma_f32_16x16x32_bf16 v[96:99], v[156:159], v[208:211], v[96:99]\n"
        "ds_read_b128 v[140:143], v249 offset:28672\n"
        "v_perm_b32 v243, v126, v248, s73\n"
        "v_mfma_f32_16x16x32_bf16 v[48:51], v[144:147], v[212:215], v[48:51]\n"
        "ds_read_b128 v[208:211], v245 offset:16384\n"
        "v_mfma_f32_16x16x32_bf16 v[52:55], v[148:151], v[212:215], v[52:55]\n"
        "ds_read_b32 v120, v240\n"
        "v_mfma_f32_16x16x32_bf16 v[56:59], v[152:155], v[212:215], v[56:59]\n"
        "s_barrier\n"
        "v_mfma_f32_16x16x32_bf16 v[12:15], v[144:147], v[216:219], v[12:15]\n"
        "ds_read_b128 v[212:215], v244 offset:32768\n"
        "v_perm_b32 v240, v127, v248, s70\n"
        "v_mfma_f32_16x16x32_bf16 v[16:19], v[148:151], v[216:219], v[16:19]\n"
        "ds_read_b32 v121, v241\n"
        "v_mfma_f32_16x16x32_bf16 v[20:23], v[152:155], v[216:219], v[20:23]\n"
        "ds_read_b32 v122, v242\n"
        "v_perm_b32 v241, v127, v248, s71\n"
        "v_mfma_f32_16x16x32_bf16 v[100:103], v[156:159], v[216:219], v[100:103]\n"
        "ds_read_b32 v123, v243\n"
        "v_perm_b32 v242, v127, v248, s72\n"
        "v_mfma_f32_16x16x32_bf16 v[60:63], v[144:147], v[220:223], v[60:63]\n"
        "ds_read_b128 v[216:219], v245 offset:32768\n"
        "v_perm_b32 v243, v127, v248, s73\n"
        "v_mfma_f32_16x16x32_bf16 v[64:67], v[148:151], v[220:223], v[64:67]\n"
        "ds_read_b32 v124, v240\n"
        "v_mfma_f32_16x16x32_bf16 v[68:71], v[152:155], v[220:223], v[68:71]\n"
        "s_waitcnt lgkmcnt(8)\n"
        "ds_read_b32 v125, v241\n"
        "v_perm_b32 v240, v140, v248, s70\n"
        "v_mfma_f32_16x16x32_bf16 v[24:27], v[144:147], v[224:227], v[24:27]\n"
        "ds_read_b128 v[220:223], v244 offset:49152\n"
        "v_perm_b32 v241, v140, v248, s71\n"
        "v_mfma_f32_16x16x32_bf16 v[28:31], v[148:151], v[224:227], v[28:31]\n"
        "ds_read_b32 v126, v242\n"
        "v_mfma_f32_16x16x32_bf16 v[32:35], v[152:155], v[224:227], v[32:35]\n"
        "ds_read_b32 v127, v243\n"
        "v_perm_b32 v242, v140, v248, s72\n"
        "v_mfma_f32_16x16x32_bf16 v[104:107], v[156:159], v[224:227], v[104:107]\n"
        "ds_read_b32 v128, v240\n"
        "v_perm_b32 v243, v140, v248, s73\n"
        "v_mfma_f32_16x16x32_bf16 v[72:75], v[144:147], v[228:231], v[72:75]\n"
        "ds_read_b128 v[224:227], v245 offset:49152\n"
        "v_perm_b32 v240, v141, v248, s70\n"
        "v_mfma_f32_16x16x32_bf16 v[76:79], v[148:151], v[228:231], v[76:79]\n"
        "ds_read_b32 v129, v241\n"
        "v_mfma_f32_16x16x32_bf16 v[80:83], v[152:155], v[228:231], v[80:83]\n"
        "ds_read_b32 v130, v242\n"
        "v_perm_b32 v241, v141, v248, s71\n"
        "v_mfma_f32_16x16x32_bf16 v[36:39], v[144:147], v[232:235], v[36:39]\n"
        "ds_read_b128 v[228:231], v247 offset:16384\n"
        "v_perm_b32 v242, v141, v248, s72\n"
        "v_mfma_f32_16x16x32_bf16 v[40:43], v[148:151], v[232:235], v[40:43]\n"
        "ds_read_b32 v131, v243\n"
        "v_mfma_f32_16x16x32_bf16 v[44:47], v[152:155], v[232:235], v[44:47]\n"
        "ds_read_b32 v132, v240\n"
        "v_perm_b32 v243, v141, v248, s73\n"
        "v_mfma_f32_16x16x32_bf16 v[108:111], v[156:159], v[232:235], v[108:111]\n"
        "ds_read_b128 v[156:159], v250 offset:448\n"
        "v_perm_b32 v240, v142, v248, s70\n"
        "v_mfma_f32_16x16x32_bf16 v[84:87], v[144:147], v[236:239], v[84:87]\n"
        "ds_read_b128 v[232:235], v246 offset:32768\n"
        "v_mfma_f32_16x16x32_bf16 v[88:91], v[148:151], v[236:239], v[88:91]\n"
        "ds_read_b128 v[144:147], v250 offset:128\n"
        "v_mfma_f32_16x16x32_bf16 v[92:95], v[152:155], v[236:239], v[92:95]\n"
        "s_waitcnt lgkmcnt(8)\n"
        "ds_read_b128 v[148:151], v250 offset:192\n"
        "v_mfma_f32_16x16x32_bf16 v[0:3], v[160:163], v[208:211], v[0:3]\n"
        "ds_read_b128 v[236:239], v247 offset:32768\n"
        "v_mfma_f32_16x16x32_bf16 v[0:3], v[176:179], v[212:215], v[0:3]\n"
        "ds_read_b128 v[152:155], v250 offset:384\n"
        "v_mfma_f32_16x16x32_bf16 v[0:3], v[192:195], v[216:219], v[0:3]\n"
        "ds_read_b32 v133, v241\n"
        "v_mfma_f32_16x16x32_bf16 v[0:3], v[112:115], v[220:223], v[0:3]\n"
        "s_waitcnt lgkmcnt(8)\n"
        "ds_read_b32 v134, v242\n"
        "v_perm_b32 v241, v142, v248, s71\n"
        "v_mfma_f32_16x16x32_bf16 v[0:3], v[128:131], v[224:227], v[0:3]\n"
        "ds_read_b32 v135, v243\n"
        "v_perm_b32 v242, v142, v248, s72\n"
        "v_mfma_f32_16x16x32_bf16 v[4:7], v[164:167], v[208:211], v[4:7]\n"
        "ds_read_b32 v136, v240\n"
        "v_perm_b32 v243, v142, v248, s73\n"
        "v_mfma_f32_16x16x32_bf16 v[4:7], v[180:183], v[212:215], v[4:7]\n"
        "ds_read_b32 v137, v241\n"
        "v_perm_b32 v240, v143, v248, s70\n"
        "v_mfma_f32_16x16x32_bf16 v[4:7], v[196:199], v[216:219], v[4:7]\n"
        "ds_read_b32 v138, v242\n"
        "v_perm_b32 v241, v143, v248, s71\n"
        "v_mfma_f32_16x16x32_bf16 v[4:7], v[116:119], v[220:223], v[4:7]\n"
        "s_waitcnt lgkmcnt(3)\n"
        "ds_read_b32 v139, v243\n"
        "v_perm_b32 v242, v143, v248, s72\n"
        "v_mfma_f32_16x16x32_bf16 v[4:7], v[132:135], v[224:227], v[4:7]\n"
        "ds_read_b32 v140, v240\n"
        "v_perm_b32 v243, v143, v248, s73\n"
        "v_mfma_f32_16x16x32_bf16 v[8:11], v[168:171], v[208:211], v[8:11]\n"
        "ds_read_b32 v141, v241\n"
        "v_mul_f32 v0, v0, v144\n"
        "v_mfma_f32_16x16x32_bf16 v[8:11], v[184:187], v[212:215], v[8:11]\n"
        "ds_read_b32 v142, v242\n"
        "v_mul_f32 v1, v1, v145\n"
        "v_mfma_f32_16x16x32_bf16 v[8:11], v[200:203], v[216:219], v[8:11]\n"
        "ds_read_b32 v143, v243\n"
        "v_mul_f32 v2, v2, v146\n"
        "v_mfma_f32_16x16x32_bf16 v[8:11], v[120:123], v[220:223], v[8:11]\n"
        "s_waitcnt lgkmcnt(4)\n"
        "ds_read_b128 v[240:243], v246 offset:49152\n"
        "v_mul_f32 v3, v3, v147\n"
        "v_mfma_f32_16x16x32_bf16 v[8:11], v[136:139], v[224:227], v[8:11]\n"
        "v_cvt_pk_bf16_f32 v0, v0, v1\n"
        "v_cvt_pk_bf16_f32 v1, v2, v3\n"
        "v_mfma_f32_16x16x32_bf16 v[96:99], v[172:175], v[208:211], v[96:99]\n"
        "ds_write_b64 v251, v[0:1] offset:61440\n"
        "v_mfma_f32_16x16x32_bf16 v[96:99], v[188:191], v[212:215], v[96:99]\n"
        "ds_read_b128 v[0:3], v247 offset:49152\n"
        "v_mul_f32 v4, v4, v148\n"
        "v_mfma_f32_16x16x32_bf16 v[96:99], v[204:207], v[216:219], v[96:99]\n"
        "v_mul_f32 v5, v5, v149\n"
        "v_mul_f32 v6, v6, v150\n"
        "v_mfma_f32_16x16x32_bf16 v[96:99], v[124:127], v[220:223], v[96:99]\n"
        "s_waitcnt lgkmcnt(3)\n"
        "v_mul_f32 v7, v7, v151\n"
        "v_cvt_pk_bf16_f32 v4, v4, v5\n"
        "v_mfma_f32_16x16x32_bf16 v[96:99], v[140:143], v[224:227], v[96:99]\n"
        "ds_read_b128 v[208:211], v245 offset:18432\n"
        "v_cvt_pk_bf16_f32 v5, v6, v7\n"
        "v_mfma_f32_16x16x32_bf16 v[48:51], v[160:163], v[228:231], v[48:51]\n"
        "ds_write_b64 v251, v[4:5] offset:61448\n"
        "v_mfma_f32_16x16x32_bf16 v[48:51], v[176:179], v[232:235], v[48:51]\n"
        "s_waitcnt lgkmcnt(4)\n"
        "v_mul_f32 v8, v8, v152\n"
        "v_mul_f32 v9, v9, v153\n"
        "v_mfma_f32_16x16x32_bf16 v[48:51], v[192:195], v[236:239], v[48:51]\n"
        "ds_read_b128 v[212:215], v244 offset:34816\n"
        "v_mul_f32 v10, v10, v154\n"
        "v_mfma_f32_16x16x32_bf16 v[48:51], v[112:115], v[240:243], v[48:51]\n"
        "s_waitcnt lgkmcnt(3)\n"
        "v_mul_f32 v11, v11, v155\n"
        "v_cvt_pk_bf16_f32 v8, v8, v9\n"
        "v_mfma_f32_16x16x32_bf16 v[48:51], v[128:131], v[0:3], v[48:51]\n"
        "v_cvt_pk_bf16_f32 v9, v10, v11\n"
        "v_mul_f32 v96, v96, v156\n"
        "v_mfma_f32_16x16x32_bf16 v[52:55], v[164:167], v[228:231], v[52:55]\n"
        "ds_write_b64 v252, v[8:9] offset:61504\n"
        "v_mfma_f32_16x16x32_bf16 v[52:55], v[180:183], v[232:235], v[52:55]\n"
        "ds_read_b128 v[216:219], v245 offset:34816\n"
        "v_mul_f32 v97, v97, v157\n"
        "v_mfma_f32_16x16x32_bf16 v[52:55], v[196:199], v[236:239], v[52:55]\n"
        "v_mul_f32 v98, v98, v158\n"
        "v_mul_f32 v99, v99, v159\n"
        "v_mfma_f32_16x16x32_bf16 v[52:55], v[116:119], v[240:243], v[52:55]\n"
        "v_cvt_pk_bf16_f32 v96, v96, v97\n"
        "v_cvt_pk_bf16_f32 v97, v98, v99\n"
        "v_mfma_f32_16x16x32_bf16 v[52:55], v[132:135], v[0:3], v[52:55]\n"
        "ds_write_b64 v254, v[96:97] offset:61440\n"
        "v_mfma_f32_16x16x32_bf16 v[56:59], v[168:171], v[228:231], v[56:59]\n"
        "ds_read_b128 v[220:223], v244 offset:51200\n"
        "v_mul_f32 v48, v48, v144\n"
        "v_mfma_f32_16x16x32_bf16 v[56:59], v[184:187], v[232:235], v[56:59]\n"
        "v_mul_f32 v49, v49, v145\n"
        "v_mul_f32 v50, v50, v146\n"
        "v_mfma_f32_16x16x32_bf16 v[56:59], v[200:203], v[236:239], v[56:59]\n"
        "ds_read_b128 v[224:227], v245 offset:51200\n"
        "v_mul_f32 v51, v51, v147\n"
        "v_mfma_f32_16x16x32_bf16 v[56:59], v[120:123], v[240:243], v[56:59]\n"
        "v_cvt_pk_bf16_f32 v48, v48, v49\n"
        "v_cvt_pk_bf16_f32 v49, v50, v51\n"
        "v_mfma_f32_16x16x32_bf16 v[56:59], v[136:139], v[0:3], v[56:59]\n"
        "s_waitcnt lgkmcnt(3)\n"
        "ds_write_b64 v251, v[48:49] offset:63232\n"
        "v_mfma_f32_16x16x32_bf16 v[12:15], v[160:163], v[208:211], v[12:15]\n"
        "ds_read_b128 v[4:7], v247 offset:18432\n"
        "v_mul_f32 v52, v52, v148\n"
        "v_mfma_f32_16x16x32_bf16 v[12:15], v[176:179], v[212:215], v[12:15]\n"
        "v_mul_f32 v53, v53, v149\n"
        "v_mul_f32 v54, v54, v150\n"
        "v_mfma_f32_16x16x32_bf16 v[12:15], v[192:195], v[216:219], v[12:15]\n"
        "s_waitcnt lgkmcnt(3)\n"
        "v_mul_f32 v55, v55, v151\n"
        "v_cvt_pk_bf16_f32 v52, v52, v53\n"
        "v_mfma_f32_16x16x32_bf16 v[12:15], v[112:115], v[220:223], v[12:15]\n"
        "s_waitcnt lgkmcnt(2)\n"
        "ds_read_b128 v[8:11], v246 offset:34816\n"
        "v_cvt_pk_bf16_f32 v53, v54, v55\n"
        "v_mfma_f32_16x16x32_bf16 v[12:15], v[128:131], v[224:227], v[12:15]\n"
        "ds_write_b64 v251, v[52:53] offset:63240\n"
        "v_mfma_f32_16x16x32_bf16 v[16:19], v[164:167], v[208:211], v[16:19]\n"
        "v_mul_f32 v56, v56, v152\n"
        "v_mul_f32 v57, v57, v153\n"
        "v_mfma_f32_16x16x32_bf16 v[16:19], v[180:183], v[212:215], v[16:19]\n"
        "ds_read_b128 v[96:99], v247 offset:34816\n"
        "v_mul_f32 v58, v58, v154\n"
        "v_mfma_f32_16x16x32_bf16 v[16:19], v[196:199], v[216:219], v[16:19]\n"
        "v_mul_f32 v59, v59, v155\n"
        "v_cvt_pk_bf16_f32 v56, v56, v57\n"
        "v_mfma_f32_16x16x32_bf16 v[16:19], v[116:119], v[220:223], v[16:19]\n"
        "v_cvt_pk_bf16_f32 v57, v58, v59\n"
        "v_mul_f32 v12, v12, v144\n"
        "v_mfma_f32_16x16x32_bf16 v[16:19], v[132:135], v[224:227], v[16:19]\n"
        "ds_write_b64 v252, v[56:57] offset:63296\n"
        "v_mfma_f32_16x16x32_bf16 v[20:23], v[168:171], v[208:211], v[20:23]\n"
        "ds_read_b128 v[228:231], v246 offset:51200\n"
        "v_mul_f32 v13, v13, v145\n"
        "v_mfma_f32_16x16x32_bf16 v[20:23], v[184:187], v[212:215], v[20:23]\n"
        "v_mul_f32 v14, v14, v146\n"
        "v_mul_f32 v15, v15, v147\n"
        "v_mfma_f32_16x16x32_bf16 v[20:23], v[200:203], v[216:219], v[20:23]\n"
        "v_cvt_pk_bf16_f32 v12, v12, v13\n"
        "v_cvt_pk_bf16_f32 v13, v14, v15\n"
        "v_mfma_f32_16x16x32_bf16 v[20:23], v[120:123], v[220:223], v[20:23]\n"
        "ds_read_b128 v[232:235], v247 offset:51200\n"
        "v_mul_f32 v16, v16, v148\n"
        "v_mfma_f32_16x16x32_bf16 v[20:23], v[136:139], v[224:227], v[20:23]\n"
        "s_waitcnt lgkmcnt(2)\n"
        "s_barrier\n"
        "v_mfma_f32_16x16x32_bf16 v[100:103], v[172:175], v[208:211], v[100:103]\n"
        "ds_write_b64 v251, v[12:13] offset:0\n"
        "v_mfma_f32_16x16x32_bf16 v[100:103], v[188:191], v[212:215], v[100:103]\n"
        "ds_read_b128 v[236:239], v245 offset:20480\n"
        "v_mul_f32 v17, v17, v149\n"
        "v_mfma_f32_16x16x32_bf16 v[100:103], v[204:207], v[216:219], v[100:103]\n"
        "v_mul_f32 v18, v18, v150\n"
        "v_mul_f32 v19, v19, v151\n"
        "v_mfma_f32_16x16x32_bf16 v[100:103], v[124:127], v[220:223], v[100:103]\n"
        "v_cvt_pk_bf16_f32 v16, v16, v17\n"
        "v_cvt_pk_bf16_f32 v17, v18, v19\n"
        "v_mfma_f32_16x16x32_bf16 v[100:103], v[140:143], v[224:227], v[100:103]\n"
        "s_waitcnt lgkmcnt(3)\n"
        "ds_write_b64 v251, v[16:17] offset:8\n"
        "v_mfma_f32_16x16x32_bf16 v[60:63], v[160:163], v[4:7], v[60:63]\n"
        "ds_read_b128 v[240:243], v244 offset:36864\n"
        "v_mul_f32 v20, v20, v152\n"
        "v_mfma_f32_16x16x32_bf16 v[60:63], v[176:179], v[8:11], v[60:63]\n"
        "s_waitcnt lgkmcnt(4)\n"
        "v_mul_f32 v21, v21, v153\n"
        "v_mul_f32 v22, v22, v154\n"
        "v_mfma_f32_16x16x32_bf16 v[60:63], v[192:195], v[96:99], v[60:63]\n"
        "v_mul_f32 v23, v23, v155\n"
        "v_cvt_pk_bf16_f32 v20, v20, v21\n"
        "v_mfma_f32_16x16x32_bf16 v[60:63], v[112:115], v[228:231], v[60:63]\n"
        "ds_read_b128 v[0:3], v245 offset:36864\n"
        "v_cvt_pk_bf16_f32 v21, v22, v23\n"
        "v_mfma_f32_16x16x32_bf16 v[60:63], v[128:131], v[232:235], v[60:63]\n"
        "ds_write_b64 v252, v[20:21] offset:64\n"
        "v_mfma_f32_16x16x32_bf16 v[64:67], v[164:167], v[4:7], v[64:67]\n"
        "v_mul_f32 v100, v100, v156\n"
        "v_mul_f32 v101, v101, v157\n"
        "v_mfma_f32_16x16x32_bf16 v[64:67], v[180:183], v[8:11], v[64:67]\n"
        "ds_read_b128 v[48:51], v244 offset:53248\n"
        "v_mul_f32 v102, v102, v158\n"
        "v_mfma_f32_16x16x32_bf16 v[64:67], v[196:199], v[96:99], v[64:67]\n"
        "v_mul_f32 v103, v103, v159\n"
        "v_cvt_pk_bf16_f32 v100, v100, v101\n"
        "v_mfma_f32_16x16x32_bf16 v[64:67], v[116:119], v[228:231], v[64:67]\n"
        "v_cvt_pk_bf16_f32 v101, v102, v103\n"
        "v_mul_f32 v60, v60, v144\n"
        "v_mfma_f32_16x16x32_bf16 v[64:67], v[132:135], v[232:235], v[64:67]\n"
        "ds_write_b64 v254, v[100:101] offset:0\n"
        "v_mfma_f32_16x16x32_bf16 v[68:71], v[168:171], v[4:7], v[68:71]\n"
        "ds_read_b128 v[52:55], v245 offset:53248\n"
        "v_mul_f32 v61, v61, v145\n"
        "v_mfma_f32_16x16x32_bf16 v[68:71], v[184:187], v[8:11], v[68:71]\n"
        "v_mul_f32 v62, v62, v146\n"
        "v_mul_f32 v63, v63, v147\n"
        "v_mfma_f32_16x16x32_bf16 v[68:71], v[200:203], v[96:99], v[68:71]\n"
        "v_cvt_pk_bf16_f32 v60, v60, v61\n"
        "v_cvt_pk_bf16_f32 v61, v62, v63\n"
        "v_mfma_f32_16x16x32_bf16 v[68:71], v[120:123], v[228:231], v[68:71]\n"
        "ds_write_b64 v251, v[60:61] offset:1792\n"
        "v_mfma_f32_16x16x32_bf16 v[68:71], v[136:139], v[232:235], v[68:71]\n"
        "s_waitcnt lgkmcnt(3)\n"
        "ds_read_b128 v[56:59], v247 offset:20480\n"
        "v_mul_f32 v64, v64, v148\n"
        "v_mfma_f32_16x16x32_bf16 v[24:27], v[160:163], v[236:239], v[24:27]\n"
        "v_mul_f32 v65, v65, v149\n"
        "v_mul_f32 v66, v66, v150\n"
        "v_mfma_f32_16x16x32_bf16 v[24:27], v[176:179], v[240:243], v[24:27]\n"
        "v_mul_f32 v67, v67, v151\n"
        "v_cvt_pk_bf16_f32 v64, v64, v65\n"
        "v_mfma_f32_16x16x32_bf16 v[24:27], v[192:195], v[0:3], v[24:27]\n"
        "ds_read_b128 v[12:15], v246 offset:36864\n"
        "v_cvt_pk_bf16_f32 v65, v66, v67\n"
        "v_mfma_f32_16x16x32_bf16 v[24:27], v[112:115], v[48:51], v[24:27]\n"
        "s_waitcnt lgkmcnt(3)\n"
        "ds_write_b64 v251, v[64:65] offset:1800\n"
        "v_mfma_f32_16x16x32_bf16 v[24:27], v[128:131], v[52:55], v[24:27]\n"
        "v_mul_f32 v68, v68, v152\n"
        "v_mul_f32 v69, v69, v153\n"
        "v_mfma_f32_16x16x32_bf16 v[28:31], v[164:167], v[236:239], v[28:31]\n"
        "ds_read_b128 v[208:211], v247 offset:36864\n"
        "v_mul_f32 v70, v70, v154\n"
        "v_mfma_f32_16x16x32_bf16 v[28:31], v[180:183], v[240:243], v[28:31]\n"
        "v_mul_f32 v71, v71, v155\n"
        "v_cvt_pk_bf16_f32 v68, v68, v69\n"
        "v_mfma_f32_16x16x32_bf16 v[28:31], v[196:199], v[0:3], v[28:31]\n"
        "v_cvt_pk_bf16_f32 v69, v70, v71\n"
        "v_mul_f32 v24, v24, v144\n"
        "v_mfma_f32_16x16x32_bf16 v[28:31], v[116:119], v[48:51], v[28:31]\n"
        "ds_write_b64 v252, v[68:69] offset:1856\n"
        "v_mfma_f32_16x16x32_bf16 v[28:31], v[132:135], v[52:55], v[28:31]\n"
        "ds_read_b128 v[212:215], v246 offset:53248\n"
        "v_mul_f32 v25, v25, v145\n"
        "v_mfma_f32_16x16x32_bf16 v[32:35], v[168:171], v[236:239], v[32:35]\n"
        "v_mul_f32 v26, v26, v146\n"
        "v_mul_f32 v27, v27, v147\n"
        "v_mfma_f32_16x16x32_bf16 v[32:35], v[184:187], v[240:243], v[32:35]\n"
        "v_cvt_pk_bf16_f32 v24, v24, v25\n"
        "v_cvt_pk_bf16_f32 v25, v26, v27\n"
        "v_mfma_f32_16x16x32_bf16 v[32:35], v[200:203], v[0:3], v[32:35]\n"
        "ds_write_b64 v251, v[24:25] offset:16384\n"
        "v_mfma_f32_16x16x32_bf16 v[32:35], v[120:123], v[48:51], v[32:35]\n"
        "s_waitcnt lgkmcnt(2)\n"
        "s_barrier\n"
        "v_mfma_f32_16x16x32_bf16 v[32:35], v[136:139], v[52:55], v[32:35]\n"
        "ds_read_b128 v[216:219], v247 offset:53248\n"
        "v_mul_f32 v28, v28, v148\n"
        "v_mfma_f32_16x16x32_bf16 v[104:107], v[172:175], v[236:239], v[104:107]\n"
        "v_mul_f32 v29, v29, v149\n"
        "v_mul_f32 v30, v30, v150\n"
        "v_mfma_f32_16x16x32_bf16 v[104:107], v[188:191], v[240:243], v[104:107]\n"
        "v_mul_f32 v31, v31, v151\n"
        "v_cvt_pk_bf16_f32 v28, v28, v29\n"
        "v_mfma_f32_16x16x32_bf16 v[104:107], v[204:207], v[0:3], v[104:107]\n"
        "ds_read_b128 v[220:223], v245 offset:22528\n"
        "v_cvt_pk_bf16_f32 v29, v30, v31\n"
        "v_mfma_f32_16x16x32_bf16 v[104:107], v[124:127], v[48:51], v[104:107]\n"
        "ds_write_b64 v251, v[28:29] offset:16392\n"
        "v_mfma_f32_16x16x32_bf16 v[104:107], v[140:143], v[52:55], v[104:107]\n"
        "s_waitcnt lgkmcnt(4)\n"
        "v_mul_f32 v32, v32, v152\n"
        "v_mul_f32 v33, v33, v153\n"
        "v_mfma_f32_16x16x32_bf16 v[72:75], v[160:163], v[56:59], v[72:75]\n"
        "ds_read_b128 v[16:19], v244 offset:38912\n"
        "v_mul_f32 v34, v34, v154\n"
        "v_mfma_f32_16x16x32_bf16 v[72:75], v[176:179], v[12:15], v[72:75]\n"
        "v_mul_f32 v35, v35, v155\n"
        "v_cvt_pk_bf16_f32 v32, v32, v33\n"
        "v_mfma_f32_16x16x32_bf16 v[72:75], v[192:195], v[208:211], v[72:75]\n"
        "s_waitcnt lgkmcnt(3)\n"
        "v_cvt_pk_bf16_f32 v33, v34, v35\n"
        "v_mul_f32 v104, v104, v156\n"
        "v_mfma_f32_16x16x32_bf16 v[72:75], v[112:115], v[212:215], v[72:75]\n"
        "ds_write_b64 v252, v[32:33] offset:16448\n"
        "v_mfma_f32_16x16x32_bf16 v[72:75], v[128:131], v[216:219], v[72:75]\n"
        "ds_read_b128 v[224:227], v245 offset:38912\n"
        "v_mul_f32 v105, v105, v157\n"
        "v_mfma_f32_16x16x32_bf16 v[76:79], v[164:167], v[56:59], v[76:79]\n"
        "v_mul_f32 v106, v106, v158\n"
        "v_mul_f32 v107, v107, v159\n"
        "v_mfma_f32_16x16x32_bf16 v[76:79], v[180:183], v[12:15], v[76:79]\n"
        "v_cvt_pk_bf16_f32 v104, v104, v105\n"
        "v_cvt_pk_bf16_f32 v105, v106, v107\n"
        "v_mfma_f32_16x16x32_bf16 v[76:79], v[196:199], v[208:211], v[76:79]\n"
        "ds_write_b64 v254, v[104:105] offset:16384\n"
        "v_mfma_f32_16x16x32_bf16 v[76:79], v[116:119], v[212:215], v[76:79]\n"
        "ds_read_b128 v[20:23], v244 offset:55296\n"
        "v_mul_f32 v72, v72, v144\n"
        "v_mfma_f32_16x16x32_bf16 v[76:79], v[132:135], v[216:219], v[76:79]\n"
        "v_mul_f32 v73, v73, v145\n"
        "v_mul_f32 v74, v74, v146\n"
        "v_mfma_f32_16x16x32_bf16 v[80:83], v[168:171], v[56:59], v[80:83]\n"
        "v_mul_f32 v75, v75, v147\n"
        "v_cvt_pk_bf16_f32 v72, v72, v73\n"
        "v_mfma_f32_16x16x32_bf16 v[80:83], v[184:187], v[12:15], v[80:83]\n"
        "ds_read_b128 v[100:103], v245 offset:55296\n"
        "v_cvt_pk_bf16_f32 v73, v74, v75\n"
        "v_mfma_f32_16x16x32_bf16 v[80:83], v[200:203], v[208:211], v[80:83]\n"
        "ds_write_b64 v251, v[72:73] offset:18176\n"
        "v_mfma_f32_16x16x32_bf16 v[80:83], v[120:123], v[212:215], v[80:83]\n"
        "v_mul_f32 v76, v76, v148\n"
        "v_mul_f32 v77, v77, v149\n"
        "v_mfma_f32_16x16x32_bf16 v[80:83], v[136:139], v[216:219], v[80:83]\n"
        "s_waitcnt lgkmcnt(4)\n"
        "ds_read_b128 v[4:7], v247 offset:22528\n"
        "v_mul_f32 v78, v78, v150\n"
        "v_mfma_f32_16x16x32_bf16 v[36:39], v[160:163], v[220:223], v[36:39]\n"
        "v_mul_f32 v79, v79, v151\n"
        "v_cvt_pk_bf16_f32 v76, v76, v77\n"
        "v_mfma_f32_16x16x32_bf16 v[36:39], v[176:179], v[16:19], v[36:39]\n"
        "s_waitcnt lgkmcnt(3)\n"
        "v_cvt_pk_bf16_f32 v77, v78, v79\n"
        "v_mfma_f32_16x16x32_bf16 v[36:39], v[192:195], v[224:227], v[36:39]\n"
        "ds_write_b64 v251, v[76:77] offset:18184\n"
        "v_mfma_f32_16x16x32_bf16 v[36:39], v[112:115], v[20:23], v[36:39]\n"
        "s_waitcnt lgkmcnt(3)\n"
        "ds_read_b128 v[8:11], v246 offset:38912\n"
        "v_mul_f32 v80, v80, v152\n"
        "v_mfma_f32_16x16x32_bf16 v[36:39], v[128:131], v[100:103], v[36:39]\n"
        "v_mul_f32 v81, v81, v153\n"
        "v_mul_f32 v82, v82, v154\n"
        "v_mfma_f32_16x16x32_bf16 v[40:43], v[164:167], v[220:223], v[40:43]\n"
        "v_mul_f32 v83, v83, v155\n"
        "v_cvt_pk_bf16_f32 v80, v80, v81\n"
        "v_mfma_f32_16x16x32_bf16 v[40:43], v[180:183], v[16:19], v[40:43]\n"
        "ds_read_b128 v[96:99], v247 offset:38912\n"
        "v_cvt_pk_bf16_f32 v81, v82, v83\n"
        "v_mfma_f32_16x16x32_bf16 v[40:43], v[196:199], v[224:227], v[40:43]\n"
        "ds_write_b64 v252, v[80:81] offset:18240\n"
        "v_mfma_f32_16x16x32_bf16 v[40:43], v[116:119], v[20:23], v[40:43]\n"
        "v_mul_f32 v36, v36, v144\n"
        "v_mul_f32 v37, v37, v145\n"
        "v_mfma_f32_16x16x32_bf16 v[40:43], v[132:135], v[100:103], v[40:43]\n"
        "ds_read_b128 v[60:63], v246 offset:55296\n"
        "v_mul_f32 v38, v38, v146\n"
        "v_mfma_f32_16x16x32_bf16 v[44:47], v[168:171], v[220:223], v[44:47]\n"
        "v_mul_f32 v39, v39, v147\n"
        "v_cvt_pk_bf16_f32 v36, v36, v37\n"
        "v_mfma_f32_16x16x32_bf16 v[44:47], v[184:187], v[16:19], v[44:47]\n"
        "v_cvt_pk_bf16_f32 v37, v38, v39\n"
        "v_mfma_f32_16x16x32_bf16 v[44:47], v[200:203], v[224:227], v[44:47]\n"
        "s_waitcnt lgkmcnt(1)\n"
        "s_barrier\n"
        "v_mfma_f32_16x16x32_bf16 v[44:47], v[120:123], v[20:23], v[44:47]\n"
        "ds_write_b64 v251, v[36:37] offset:32768\n"
        "v_mfma_f32_16x16x32_bf16 v[44:47], v[136:139], v[100:103], v[44:47]\n"
        "ds_read_b128 v[228:231], v247 offset:55296\n"
        "v_mul_f32 v40, v40, v148\n"
        "v_mfma_f32_16x16x32_bf16 v[108:111], v[172:175], v[220:223], v[108:111]\n"
        "v_mul_f32 v41, v41, v149\n"
        "v_mul_f32 v42, v42, v150\n"
        "v_mfma_f32_16x16x32_bf16 v[108:111], v[188:191], v[16:19], v[108:111]\n"
        "v_mul_f32 v43, v43, v151\n"
        "v_cvt_pk_bf16_f32 v40, v40, v41\n"
        "v_mfma_f32_16x16x32_bf16 v[108:111], v[204:207], v[224:227], v[108:111]\n"
        "v_cvt_pk_bf16_f32 v41, v42, v43\n"
        "v_mul_f32 v44, v44, v152\n"
        "v_mfma_f32_16x16x32_bf16 v[108:111], v[124:127], v[20:23], v[108:111]\n"
        "ds_write_b64 v251, v[40:41] offset:32776\n"
        "v_mfma_f32_16x16x32_bf16 v[108:111], v[140:143], v[100:103], v[108:111]\n"
        "s_waitcnt lgkmcnt(3)\n"
        "v_mul_f32 v45, v45, v153\n"
        "v_mul_f32 v46, v46, v154\n"
        "v_mfma_f32_16x16x32_bf16 v[84:87], v[160:163], v[4:7], v[84:87]\n"
        "v_mul_f32 v47, v47, v155\n"
        "v_cvt_pk_bf16_f32 v44, v44, v45\n"
        "v_mfma_f32_16x16x32_bf16 v[84:87], v[176:179], v[8:11], v[84:87]\n"
        "v_cvt_pk_bf16_f32 v45, v46, v47\n"
        "v_mfma_f32_16x16x32_bf16 v[84:87], v[192:195], v[96:99], v[84:87]\n"
        "s_waitcnt lgkmcnt(1)\n"
        "ds_write_b64 v252, v[44:45] offset:32832\n"
        "v_mfma_f32_16x16x32_bf16 v[84:87], v[112:115], v[60:63], v[84:87]\n"
        "v_mul_f32 v108, v108, v156\n"
        "v_mul_f32 v109, v109, v157\n"
        "v_mfma_f32_16x16x32_bf16 v[84:87], v[128:131], v[228:231], v[84:87]\n"
        "v_mul_f32 v110, v110, v158\n"
        "v_mul_f32 v111, v111, v159\n"
        "v_mfma_f32_16x16x32_bf16 v[88:91], v[164:167], v[4:7], v[88:91]\n"
        "v_cvt_pk_bf16_f32 v108, v108, v109\n"
        "v_cvt_pk_bf16_f32 v109, v110, v111\n"
        "v_mfma_f32_16x16x32_bf16 v[88:91], v[180:183], v[8:11], v[88:91]\n"
        "ds_write_b64 v254, v[108:109] offset:32768\n"
        "v_mfma_f32_16x16x32_bf16 v[88:91], v[196:199], v[96:99], v[88:91]\n"
        "v_mul_f32 v84, v84, v144\n"
        "v_mul_f32 v85, v85, v145\n"
        "v_mfma_f32_16x16x32_bf16 v[88:91], v[116:119], v[60:63], v[88:91]\n"
        "v_mul_f32 v86, v86, v146\n"
        "v_mul_f32 v87, v87, v147\n"
        "v_mfma_f32_16x16x32_bf16 v[88:91], v[132:135], v[228:231], v[88:91]\n"
        "v_cvt_pk_bf16_f32 v84, v84, v85\n"
        "v_cvt_pk_bf16_f32 v85, v86, v87\n"
        "v_mfma_f32_16x16x32_bf16 v[92:95], v[168:171], v[4:7], v[92:95]\n"
        "ds_write_b64 v251, v[84:85] offset:34560\n"
        "v_mfma_f32_16x16x32_bf16 v[92:95], v[184:187], v[8:11], v[92:95]\n"
        "s_nop 1\n"
        "v_mfma_f32_16x16x32_bf16 v[92:95], v[200:203], v[96:99], v[92:95]\n"
        "v_mul_f32 v88, v88, v148\n"
        "v_mul_f32 v89, v89, v149\n"
        "v_mfma_f32_16x16x32_bf16 v[92:95], v[120:123], v[60:63], v[92:95]\n"
        "v_mul_f32 v90, v90, v150\n"
        "v_mul_f32 v91, v91, v151\n"
        "v_mfma_f32_16x16x32_bf16 v[92:95], v[136:139], v[228:231], v[92:95]\n"
        "v_cvt_pk_bf16_f32 v88, v88, v89\n"
        "v_cvt_pk_bf16_f32 v89, v90, v91\n"
        "ds_write_b64 v251, v[88:89] offset:34568\n"
        "s_nop 4\n"
        "v_mul_f32 v92, v92, v152\n"
        "v_mul_f32 v93, v93, v153\n"
        "v_mul_f32 v94, v94, v154\n"
        "v_mul_f32 v95, v95, v155\n"
        "v_cvt_pk_bf16_f32 v92, v92, v93\n"
        "v_cvt_pk_bf16_f32 v93, v94, v95\n"
        "ds_write_b64 v252, v[92:93] offset:34624\n"
        "s_waitcnt lgkmcnt(0)\n"
        "s_barrier\n"
        "s_endpgm\n"
        "Lhm0_%=:\n"
        "s_sleep 10\n"
        "buffer_load_dwordx4 v[0:3], v100, s[20:23], s52 offen nt\n"
        "s_add_u32 s76, s52, 14336\n"
        "buffer_load_dwordx4 v[4:7], v100, s[20:23], s76 offen nt\n"
        "s_add_u32 s77, s52, 28672\n"
        "buffer_load_dwordx4 v[8:11], v100, s[20:23], s77 offen nt\n"
        "s_add_u32 s74, s52, 43008\n"
        "buffer_load_dwordx4 v[12:15], v100, s[20:23], s74 offen nt\n"
        "s_add_u32 s75, s52, 57344\n"
        "buffer_load_dwordx4 v[16:19], v100, s[20:23], s75 offen nt\n"
        "s_add_u32 s76, s52, 71680\n"
        "buffer_load_dwordx4 v[20:23], v100, s[20:23], s76 offen nt\n"
        "s_add_u32 s77, s52, 86016\n"
        "buffer_load_dwordx4 v[24:27], v100, s[20:23], s77 offen nt\n"
        "s_add_u32 s74, s52, 100352\n"
        "buffer_load_dwordx4 v[28:31], v100, s[20:23], s74 offen nt\n"
        "s_barrier\n"
        "s_mov_b32 m0, 98304\n"
        "s_add_u32 s86, s57, 256\n"
        "buffer_load_dwordx4 v101, s[32:35], s86 offen  lds\n"
        "s_mov_b32 m0, 99328\n"
        "s_nop 0\n"
        "buffer_load_dwordx4 v102, s[32:35], s86 offen  lds\n"
        "s_mov_b32 m0, 100352\n"
        "s_add_u32 s86, s57, 33024\n"
        "buffer_load_dwordx4 v101, s[32:35], s86 offen  lds\n"
        "s_mov_b32 m0, 101376\n"
        "s_nop 0\n"
        "buffer_load_dwordx4 v102, s[32:35], s86 offen  lds\n"
        "s_waitcnt vmcnt(12)\n"
        "s_barrier\n"
        "s_mov_b32 m0, 114688\n"
        "s_add_u32 s86, s57, 384\n"
        "buffer_load_dwordx4 v101, s[32:35], s86 offen  lds\n"
        "s_mov_b32 m0, 115712\n"
        "s_nop 0\n"
        "buffer_load_dwordx4 v102, s[32:35], s86 offen  lds\n"
        "s_mov_b32 m0, 116736\n"
        "s_add_u32 s86, s57, 33152\n"
        "buffer_load_dwordx4 v101, s[32:35], s86 offen  lds\n"
        "s_mov_b32 m0, 117760\n"
        "s_nop 0\n"
        "buffer_load_dwordx4 v102, s[32:35], s86 offen  lds\n"
        "s_waitcnt vmcnt(4)\n"
        "s_barrier\n"
        "buffer_load_dwordx2 v[106:107], v104, s[28:31], 0 offen offset:256 sc1\n"
        "buffer_load_dwordx2 v[108:109], v104, s[28:31], 0 offen offset:320 sc1\n"
        "s_mov_b32 m0, 65536\n"
        "s_add_u32 s86, s57, 512\n"
        "buffer_load_dwordx4 v101, s[32:35], s86 offen  lds\n"
        "s_mov_b32 m0, 66560\n"
        "s_nop 0\n"
        "buffer_load_dwordx4 v102, s[32:35], s86 offen  lds\n"
        "s_mov_b32 m0, 67584\n"
        "s_add_u32 s86, s57, 33280\n"
        "buffer_load_dwordx4 v101, s[32:35], s86 offen  lds\n"
        "s_mov_b32 m0, 68608\n"
        "s_nop 0\n"
        "buffer_load_dwordx4 v102, s[32:35], s86 offen  lds\n"
        "v_lshl_or_b32 v0, v4, 4, v0\n"
        "v_lshl_or_b32 v1, v5, 4, v1\n"
        "v_lshl_or_b32 v2, v6, 4, v2\n"
        "v_lshl_or_b32 v3, v7, 4, v3\n"
        "v_lshl_or_b32 v8, v12, 4, v8\n"
        "v_lshl_or_b32 v9, v13, 4, v9\n"
        "v_lshl_or_b32 v10, v14, 4, v10\n"
        "v_lshl_or_b32 v11, v15, 4, v11\n"
        "v_lshl_or_b32 v0, v8, 8, v0\n"
        "v_lshl_or_b32 v1, v9, 8, v1\n"
        "v_lshl_or_b32 v2, v10, 8, v2\n"
        "v_lshl_or_b32 v3, v11, 8, v3\n"
        "v_lshl_or_b32 v16, v20, 4, v16\n"
        "v_lshl_or_b32 v17, v21, 4, v17\n"
        "v_lshl_or_b32 v18, v22, 4, v18\n"
        "v_lshl_or_b32 v19, v23, 4, v19\n"
        "v_lshl_or_b32 v24, v28, 4, v24\n"
        "v_lshl_or_b32 v25, v29, 4, v25\n"
        "v_lshl_or_b32 v26, v30, 4, v26\n"
        "v_lshl_or_b32 v27, v31, 4, v27\n"
        "v_lshl_or_b32 v16, v24, 8, v16\n"
        "v_lshl_or_b32 v17, v25, 8, v17\n"
        "v_lshl_or_b32 v18, v26, 8, v18\n"
        "v_lshl_or_b32 v19, v27, 8, v19\n"
        "v_lshl_or_b32 v4, v16, 16, v0\n"
        "v_lshl_or_b32 v5, v17, 16, v1\n"
        "v_lshl_or_b32 v6, v18, 16, v2\n"
        "v_lshl_or_b32 v7, v19, 16, v3\n"
        "s_mov_b64 vcc, s[4:5]\n"
        "s_nop 0\n"
        "v_cndmask_b32_dpp v8, v5, v4, vcc quad_perm:[1,0,3,2] row_mask:0xf bank_mask:0xf\n"
        "v_cndmask_b32_dpp v10, v7, v6, vcc quad_perm:[1,0,3,2] row_mask:0xf bank_mask:0xf\n"
        "s_not_b64 vcc, vcc\n"
        "s_nop 0\n"
        "v_cndmask_b32_dpp v9, v4, v5, vcc quad_perm:[1,0,3,2] row_mask:0xf bank_mask:0xf\n"
        "v_cndmask_b32_dpp v11, v6, v7, vcc quad_perm:[1,0,3,2] row_mask:0xf bank_mask:0xf\n"
        "s_mov_b64 vcc, s[6:7]\n"
        "s_nop 0\n"
        "v_cndmask_b32_dpp v28, v10, v8, vcc quad_perm:[2,3,0,1] row_mask:0xf bank_mask:0xf\n"
        "v_cndmask_b32_dpp v29, v11, v9, vcc quad_perm:[2,3,0,1] row_mask:0xf bank_mask:0xf\n"
        "s_not_b64 vcc, vcc\n"
        "s_nop 0\n"
        "v_cndmask_b32_dpp v30, v8, v10, vcc quad_perm:[2,3,0,1] row_mask:0xf bank_mask:0xf\n"
        "v_cndmask_b32_dpp v31, v9, v11, vcc quad_perm:[2,3,0,1] row_mask:0xf bank_mask:0xf\n"
        "buffer_store_dwordx4 v[28:31], v103, s[24:27], s53 offen sc1\n"
        "s_nop 1\n"
        "s_waitcnt vmcnt(7)\n"
        "s_barrier\n"
        "buffer_load_dwordx2 v[110:111], v104, s[28:31], 0 offen offset:384 sc1\n"
        "s_waitcnt vmcnt(7)\n"
        "v_cmp_ne_u32 vcc, s64, v106\n"
        "v_cmp_ne_u32 s[78:79], s65, v107\n"
        "s_or_b64 vcc, vcc, s[78:79]\n"
        "s_cbranch_scc1 Ls17_%=\n"
        "Lk18_%=:\n"
        "s_mov_b32 m0, 131072\n"
        "s_add_u32 s75, s56, 65536\n"
        "buffer_load_dwordx4 v103, s[24:27], s75 offen sc1 lds\n"
        "s_mov_b32 m0, 132096\n"
        "s_add_u32 s75, s75, 2048\n"
        "buffer_load_dwordx4 v103, s[24:27], s75 offen sc1 lds\n"
        "Le19_%=:\n"
        "s_waitcnt vmcnt(8)\n"
        "v_cmp_ne_u32 vcc, s64, v108\n"
        "v_cmp_ne_u32 s[78:79], s65, v109\n"
        "s_or_b64 vcc, vcc, s[78:79]\n"
        "s_cbranch_scc1 Ls20_%=\n"
        "Lk21_%=:\n"
        "s_mov_b32 m0, 139264\n"
        "s_add_u32 s76, s56, 81920\n"
        "buffer_load_dwordx4 v103, s[24:27], s76 offen sc1 lds\n"
        "s_mov_b32 m0, 140288\n"
        "s_add_u32 s76, s76, 2048\n"
        "buffer_load_dwordx4 v103, s[24:27], s76 offen sc1 lds\n"
        "Le22_%=:\n"
        "s_mov_b32 m0, 81920\n"
        "s_add_u32 s86, s57, 640\n"
        "buffer_load_dwordx4 v101, s[32:35], s86 offen  lds\n"
        "s_mov_b32 m0, 82944\n"
        "s_nop 0\n"
        "buffer_load_dwordx4 v102, s[32:35], s86 offen  lds\n"
        "s_mov_b32 m0, 83968\n"
        "s_add_u32 s86, s57, 33408\n"
        "buffer_load_dwordx4 v101, s[32:35], s86 offen  lds\n"
        "s_mov_b32 m0, 84992\n"
        "s_nop 0\n"
        "buffer_load_dwordx4 v102, s[32:35], s86 offen  lds\n"
        "s_waitcnt vmcnt(9)\n"
        "v_mov_b32 v36, s64\n"
        "v_mov_b32 v37, s65\n"
        "s_lshl_b32 s74, s55, 3\n"
        "v_mov_b32 v38, s74\n"
        "s_mov_b64 exec, 1\n"
        "buffer_store_dwordx2 v[36:37], v38, s[28:31], 0 offen sc1\n"
        "s_mov_b64 exec, -1\n"
        "s_waitcnt vmcnt(7)\n"
        "s_waitcnt vmcnt(5)\n"
        "s_barrier\n"
        "buffer_load_dwordx2 v[112:113], v104, s[28:31], 0 offen offset:448 sc1\n"
        "v_cmp_ne_u32 vcc, s64, v110\n"
        "v_cmp_ne_u32 s[78:79], s65, v111\n"
        "s_or_b64 vcc, vcc, s[78:79]\n"
        "s_cbranch_scc1 Ls23_%=\n"
        "Lk24_%=:\n"
        "s_mov_b32 m0, 147456\n"
        "s_add_u32 s77, s56, 98304\n"
        "buffer_load_dwordx4 v103, s[24:27], s77 offen sc1 lds\n"
        "s_mov_b32 m0, 148480\n"
        "s_add_u32 s77, s77, 2048\n"
        "buffer_load_dwordx4 v103, s[24:27], s77 offen sc1 lds\n"
        "Le25_%=:\n"
        "s_mov_b32 m0, 98304\n"
        "s_add_u32 s86, s57, 768\n"
        "buffer_load_dwordx4 v101, s[32:35], s86 offen  lds\n"
        "s_mov_b32 m0, 99328\n"
        "s_nop 0\n"
        "buffer_load_dwordx4 v102, s[32:35], s86 offen  lds\n"
        "s_mov_b32 m0, 100352\n"
        "s_add_u32 s86, s57, 33536\n"
        "buffer_load_dwordx4 v101, s[32:35], s86 offen  lds\n"
        "s_mov_b32 m0, 101376\n"
        "s_nop 0\n"
        "buffer_load_dwordx4 v102, s[32:35], s86 offen  lds\n"
        "s_waitcnt vmcnt(8)\n"
        "s_waitcnt vmcnt(4)\n"
        "s_barrier\n"
        "buffer_load_dwordx2 v[106:107], v104, s[28:31], 0 offen offset:512 sc1\n"
        "v_cmp_ne_u32 vcc, s64, v112\n"
        "v_cmp_ne_u32 s[78:79], s65, v113\n"
        "s_or_b64 vcc, vcc, s[78:79]\n"
        "s_cbranch_scc1 Ls26_%=\n"
        "Lk27_%=:\n"
        "s_mov_b32 m0, 155648\n"
        "s_add_u32 s74, s56, 114688\n"
        "buffer_load_dwordx4 v103, s[24:27], s74 offen sc1 lds\n"
        "s_mov_b32 m0, 156672\n"
        "s_add_u32 s74, s74, 2048\n"
        "buffer_load_dwordx4 v103, s[24:27], s74 offen sc1 lds\n"
        "Le28_%=:\n"
        "s_mov_b32 m0, 114688\n"
        "s_add_u32 s86, s57, 896\n"
        "buffer_load_dwordx4 v101, s[32:35], s86 offen  lds\n"
        "s_mov_b32 m0, 115712\n"
        "s_nop 0\n"
        "buffer_load_dwordx4 v102, s[32:35], s86 offen  lds\n"
        "s_mov_b32 m0, 116736\n"
        "s_add_u32 s86, s57, 33664\n"
        "buffer_load_dwordx4 v101, s[32:35], s86 offen  lds\n"
        "s_mov_b32 m0, 117760\n"
        "s_nop 0\n"
        "buffer_load_dwordx4 v102, s[32:35], s86 offen  lds\n"
        "s_waitcnt vmcnt(7)\n"
        "s_waitcnt vmcnt(4)\n"
        "s_barrier\n"
        "buffer_load_dwordx2 v[108:109], v104, s[28:31], 0 offen offset:576 sc1\n"
        "buffer_load_dwordx2 v[110:111], v104, s[28:31], 0 offen offset:640 sc1\n"
        "buffer_load_dwordx2 v[112:113], v104, s[28:31], 0 offen offset:704 sc1\n"
        "v_cmp_ne_u32 vcc, s64, v106\n"
        "v_cmp_ne_u32 s[78:79], s65, v107\n"
        "s_or_b64 vcc, vcc, s[78:79]\n"
        "s_cbranch_scc1 Ls29_%=\n"
        "Lk30_%=:\n"
        "s_mov_b32 m0, 131072\n"
        "s_add_u32 s75, s56, 131072\n"
        "buffer_load_dwordx4 v103, s[24:27], s75 offen sc1 lds\n"
        "s_mov_b32 m0, 132096\n"
        "s_add_u32 s75, s75, 2048\n"
        "buffer_load_dwordx4 v103, s[24:27], s75 offen sc1 lds\n"
        "Le31_%=:\n"
        "s_mov_b32 m0, 65536\n"
        "s_add_u32 s86, s57, 1024\n"
        "buffer_load_dwordx4 v101, s[32:35], s86 offen  lds\n"
        "s_mov_b32 m0, 66560\n"
        "s_nop 0\n"
        "buffer_load_dwordx4 v102, s[32:35], s86 offen  lds\n"
        "s_mov_b32 m0, 67584\n"
        "s_add_u32 s86, s57, 33792\n"
        "buffer_load_dwordx4 v101, s[32:35], s86 offen  lds\n"
        "s_mov_b32 m0, 68608\n"
        "s_nop 0\n"
        "buffer_load_dwordx4 v102, s[32:35], s86 offen  lds\n"
        "s_waitcnt vmcnt(9)\n"
        "s_waitcnt vmcnt(4)\n"
        "s_barrier\n"
        "buffer_load_dwordx2 v[106:107], v104, s[28:31], 0 offen offset:768 sc1\n"
        "v_cmp_ne_u32 vcc, s64, v108\n"
        "v_cmp_ne_u32 s[78:79], s65, v109\n"
        "s_or_b64 vcc, vcc, s[78:79]\n"
        "s_cbranch_scc1 Ls32_%=\n"
        "Lk33_%=:\n"
        "s_mov_b32 m0, 139264\n"
        "s_add_u32 s76, s56, 147456\n"
        "buffer_load_dwordx4 v103, s[24:27], s76 offen sc1 lds\n"
        "s_mov_b32 m0, 140288\n"
        "s_add_u32 s76, s76, 2048\n"
        "buffer_load_dwordx4 v103, s[24:27], s76 offen sc1 lds\n"
        "Le34_%=:\n"
        "v_cmp_ne_u32 vcc, s64, v110\n"
        "v_cmp_ne_u32 s[78:79], s65, v111\n"
        "s_or_b64 vcc, vcc, s[78:79]\n"
        "s_cbranch_scc1 Ls35_%=\n"
        "Lk36_%=:\n"
        "s_mov_b32 m0, 147456\n"
        "s_add_u32 s77, s56, 163840\n"
        "buffer_load_dwordx4 v103, s[24:27], s77 offen sc1 lds\n"
        "s_mov_b32 m0, 148480\n"
        "s_add_u32 s77, s77, 2048\n"
        "buffer_load_dwordx4 v103, s[24:27], s77 offen sc1 lds\n"
        "Le37_%=:\n"
        "v_cmp_ne_u32 vcc, s64, v112\n"
        "v_cmp_ne_u32 s[78:79], s65, v113\n"
        "s_or_b64 vcc, vcc, s[78:79]\n"
        "s_cbranch_scc1 Ls38_%=\n"
        "Lk39_%=:\n"
        "s_mov_b32 m0, 155648\n"
        "s_add_u32 s74, s56, 180224\n"
        "buffer_load_dwordx4 v103, s[24:27], s74 offen sc1 lds\n"
        "s_mov_b32 m0, 156672\n"
        "s_add_u32 s74, s74, 2048\n"
        "buffer_load_dwordx4 v103, s[24:27], s74 offen sc1 lds\n"
        "Le40_%=:\n"
        "s_mov_b32 m0, 81920\n"
        "s_add_u32 s86, s57, 1152\n"
        "buffer_load_dwordx4 v101, s[32:35], s86 offen  lds\n"
        "s_mov_b32 m0, 82944\n"
        "s_nop 0\n"
        "buffer_load_dwordx4 v102, s[32:35], s86 offen  lds\n"
        "s_mov_b32 m0, 83968\n"
        "s_add_u32 s86, s57, 33920\n"
        "buffer_load_dwordx4 v101, s[32:35], s86 offen  lds\n"
        "s_mov_b32 m0, 84992\n"
        "s_nop 0\n"
        "buffer_load_dwordx4 v102, s[32:35], s86 offen  lds\n"
        "s_waitcnt vmcnt(11)\n"
        "s_waitcnt vmcnt(8)\n"
        "s_barrier\n"
        "buffer_load_dwordx2 v[108:109], v104, s[28:31], 0 offen offset:832 sc1\n"
        "v_cmp_ne_u32 vcc, s64, v106\n"
        "v_cmp_ne_u32 s[78:79], s65, v107\n"
        "s_or_b64 vcc, vcc, s[78:79]\n"
        "s_cbranch_scc1 Ls41_%=\n"
        "Lk42_%=:\n"
        "s_mov_b32 m0, 131072\n"
        "s_add_u32 s75, s56, 196608\n"
        "buffer_load_dwordx4 v103, s[24:27], s75 offen sc1 lds\n"
        "s_mov_b32 m0, 132096\n"
        "s_add_u32 s75, s75, 2048\n"
        "buffer_load_dwordx4 v103, s[24:27], s75 offen sc1 lds\n"
        "Le43_%=:\n"
        "s_mov_b32 m0, 98304\n"
        "s_add_u32 s86, s57, 1280\n"
        "buffer_load_dwordx4 v101, s[32:35], s86 offen  lds\n"
        "s_mov_b32 m0, 99328\n"
        "s_nop 0\n"
        "buffer_load_dwordx4 v102, s[32:35], s86 offen  lds\n"
        "s_mov_b32 m0, 100352\n"
        "s_add_u32 s86, s57, 34048\n"
        "buffer_load_dwordx4 v101, s[32:35], s86 offen  lds\n"
        "s_mov_b32 m0, 101376\n"
        "s_nop 0\n"
        "buffer_load_dwordx4 v102, s[32:35], s86 offen  lds\n"
        "s_waitcnt vmcnt(7)\n"
        "s_barrier\n"
        "buffer_load_dwordx2 v[110:111], v104, s[28:31], 0 offen offset:896 sc1\n"
        "s_waitcnt vmcnt(7)\n"
        "v_cmp_ne_u32 vcc, s64, v108\n"
        "v_cmp_ne_u32 s[78:79], s65, v109\n"
        "s_or_b64 vcc, vcc, s[78:79]\n"
        "s_cbranch_scc1 Ls44_%=\n"
        "Lk45_%=:\n"
        "s_mov_b32 m0, 139264\n"
        "s_add_u32 s76, s56, 212992\n"
        "buffer_load_dwordx4 v103, s[24:27], s76 offen sc1 lds\n"
        "s_mov_b32 m0, 140288\n"
        "s_add_u32 s76, s76, 2048\n"
        "buffer_load_dwordx4 v103, s[24:27], s76 offen sc1 lds\n"
        "Le46_%=:\n"
        "s_mov_b32 m0, 114688\n"
        "s_add_u32 s86, s57, 1408\n"
        "buffer_load_dwordx4 v101, s[32:35], s86 offen  lds\n"
        "s_mov_b32 m0, 115712\n"
        "s_nop 0\n"
        "buffer_load_dwordx4 v102, s[32:35], s86 offen  lds\n"
        "s_mov_b32 m0, 116736\n"
        "s_add_u32 s86, s57, 34176\n"
        "buffer_load_dwordx4 v101, s[32:35], s86 offen  lds\n"
        "s_mov_b32 m0, 117760\n"
        "s_nop 0\n"
        "buffer_load_dwordx4 v102, s[32:35], s86 offen  lds\n"
        "s_mov_b32 s36, s16\n"
        "s_and_b32 s37, s17, 0xffff\n"
        "s_mov_b32 s38, 14680064\n"
        "s_mov_b32 s39, 0x20000\n"
        "s_mul_i32 s59, s46, 917504\n"
        "s_mul_i32 s74, s45, 448\n"
        "s_add_u32 s59, s59, s74\n"
        "v_add_u32 v34, 0, v32\n"
        "v_mul_u32_u24 v35, 2341, v34\n"
        "v_lshrrev_b32 v35, 16, v35\n"
        "v_mul_u32_u24 v36, 28, v35\n"
        "v_sub_u32 v36, v34, v36\n"
        "v_lshlrev_b32 v36, 4, v36\n"
        "v_min_u32 v37, 7, v35\n"
        "v_cmp_le_u32 vcc, 224, v36\n"
        "v_subrev_u32 v38, 224, v36\n"
        "v_mov_b32 v39, 0\n"
        "v_mov_b32 v40, 2\n"
        "v_cndmask_b32 v38, v36, v38, vcc\n"
        "v_cndmask_b32 v39, v39, v40, vcc\n"
        "v_mul_u32_u24 v64, 3584, v39\n"
        "v_mul_u32_u24 v40, 112, v37\n"
        "v_add_u32 v64, v40, v64\n"
        "v_add_u32 v64, v38, v64\n"
        "v_add_u32 v64, 4096, v64\n"
        "v_add_u32 v40, 5280, v64\n"
        "v_cmp_le_u32 vcc, 96, v38\n"
        "s_nop 1\n"
        "v_cndmask_b32 v64, v64, v40, vcc\n"
        "v_lshlrev_b32 v39, 9, v39\n"
        "v_lshl_add_u32 v39, v37, 5, v39\n"
        "v_xor_b32 v40, 0, v38\n"
        "v_add_u32 v39, v39, v40\n"
        "v_add_u32 v39, 18240, v39\n"
        "v_cmp_le_u32 vcc, 192, v38\n"
        "s_nop 1\n"
        "v_cndmask_b32 v64, v64, v39, vcc\n"
        "v_mul_u32_u24 v68, 7168, v35\n"
        "v_add_u32 v68, v68, v36\n"
        "v_add_u32 v68, 0, v68\n"
        "s_waitcnt vmcnt(7)\n"
        "s_barrier\n"
        "buffer_load_dwordx2 v[112:113], v104, s[28:31], 0 offen offset:960 sc1\n"
        "s_waitcnt vmcnt(7)\n"
        "v_cmp_ne_u32 vcc, s64, v110\n"
        "v_cmp_ne_u32 s[78:79], s65, v111\n"
        "s_or_b64 vcc, vcc, s[78:79]\n"
        "s_cbranch_scc1 Ls47_%=\n"
        "Lk48_%=:\n"
        "s_mov_b32 m0, 147456\n"
        "s_add_u32 s77, s56, 229376\n"
        "buffer_load_dwordx4 v103, s[24:27], s77 offen sc1 lds\n"
        "s_mov_b32 m0, 148480\n"
        "s_add_u32 s77, s77, 2048\n"
        "buffer_load_dwordx4 v103, s[24:27], s77 offen sc1 lds\n"
        "Le49_%=:\n"
        "s_mov_b32 m0, 65536\n"
        "s_add_u32 s86, s57, 1536\n"
        "buffer_load_dwordx4 v101, s[32:35], s86 offen  lds\n"
        "s_mov_b32 m0, 66560\n"
        "s_nop 0\n"
        "buffer_load_dwordx4 v102, s[32:35], s86 offen  lds\n"
        "s_mov_b32 m0, 67584\n"
        "s_add_u32 s86, s57, 34304\n"
        "buffer_load_dwordx4 v101, s[32:35], s86 offen  lds\n"
        "s_mov_b32 m0, 68608\n"
        "s_nop 0\n"
        "buffer_load_dwordx4 v102, s[32:35], s86 offen  lds\n"
        "v_add_u32 v34, 64, v32\n"
        "v_mul_u32_u24 v35, 2341, v34\n"
        "v_lshrrev_b32 v35, 16, v35\n"
        "v_mul_u32_u24 v36, 28, v35\n"
        "v_sub_u32 v36, v34, v36\n"
        "v_lshlrev_b32 v36, 4, v36\n"
        "v_min_u32 v37, 7, v35\n"
        "v_cmp_le_u32 vcc, 224, v36\n"
        "v_subrev_u32 v38, 224, v36\n"
        "v_mov_b32 v39, 0\n"
        "v_mov_b32 v40, 2\n"
        "v_cndmask_b32 v38, v36, v38, vcc\n"
        "v_cndmask_b32 v39, v39, v40, vcc\n"
        "v_mul_u32_u24 v65, 3584, v39\n"
        "v_mul_u32_u24 v40, 112, v37\n"
        "v_add_u32 v65, v40, v65\n"
        "v_add_u32 v65, v38, v65\n"
        "v_add_u32 v65, 4096, v65\n"
        "v_add_u32 v40, 5280, v65\n"
        "v_cmp_le_u32 vcc, 96, v38\n"
        "s_nop 1\n"
        "v_cndmask_b32 v65, v65, v40, vcc\n"
        "v_lshlrev_b32 v39, 9, v39\n"
        "v_lshl_add_u32 v39, v37, 5, v39\n"
        "v_xor_b32 v40, 0, v38\n"
        "v_add_u32 v39, v39, v40\n"
        "v_add_u32 v39, 18240, v39\n"
        "v_cmp_le_u32 vcc, 192, v38\n"
        "s_nop 1\n"
        "v_cndmask_b32 v65, v65, v39, vcc\n"
        "v_mul_u32_u24 v69, 7168, v35\n"
        "v_add_u32 v69, v69, v36\n"
        "v_add_u32 v69, 0, v69\n"
        "s_waitcnt vmcnt(7)\n"
        "s_barrier\n"
        "s_waitcnt vmcnt(6)\n"
        "v_cmp_ne_u32 vcc, s64, v112\n"
        "v_cmp_ne_u32 s[78:79], s65, v113\n"
        "s_or_b64 vcc, vcc, s[78:79]\n"
        "s_cbranch_scc1 Ls50_%=\n"
        "Lk51_%=:\n"
        "s_mov_b32 m0, 155648\n"
        "s_add_u32 s74, s56, 245760\n"
        "buffer_load_dwordx4 v103, s[24:27], s74 offen sc1 lds\n"
        "s_mov_b32 m0, 156672\n"
        "s_add_u32 s74, s74, 2048\n"
        "buffer_load_dwordx4 v103, s[24:27], s74 offen sc1 lds\n"
        "Le52_%=:\n"
        "s_mov_b32 m0, 81920\n"
        "s_add_u32 s86, s57, 1664\n"
        "buffer_load_dwordx4 v101, s[32:35], s86 offen  lds\n"
        "s_mov_b32 m0, 82944\n"
        "s_nop 0\n"
        "buffer_load_dwordx4 v102, s[32:35], s86 offen  lds\n"
        "s_mov_b32 m0, 83968\n"
        "s_add_u32 s86, s57, 34432\n"
        "buffer_load_dwordx4 v101, s[32:35], s86 offen  lds\n"
        "s_mov_b32 m0, 84992\n"
        "s_nop 0\n"
        "buffer_load_dwordx4 v102, s[32:35], s86 offen  lds\n"
        "v_add_u32 v34, 128, v32\n"
        "v_mul_u32_u24 v35, 2341, v34\n"
        "v_lshrrev_b32 v35, 16, v35\n"
        "v_mul_u32_u24 v36, 28, v35\n"
        "v_sub_u32 v36, v34, v36\n"
        "v_lshlrev_b32 v36, 4, v36\n"
        "v_min_u32 v37, 7, v35\n"
        "v_cmp_le_u32 vcc, 224, v36\n"
        "v_subrev_u32 v38, 224, v36\n"
        "v_mov_b32 v39, 0\n"
        "v_mov_b32 v40, 2\n"
        "v_cndmask_b32 v38, v36, v38, vcc\n"
        "v_cndmask_b32 v39, v39, v40, vcc\n"
        "v_mul_u32_u24 v66, 3584, v39\n"
        "v_mul_u32_u24 v40, 112, v37\n"
        "v_add_u32 v66, v40, v66\n"
        "v_add_u32 v66, v38, v66\n"
        "v_add_u32 v66, 4096, v66\n"
        "v_add_u32 v40, 5280, v66\n"
        "v_cmp_le_u32 vcc, 96, v38\n"
        "s_nop 1\n"
        "v_cndmask_b32 v66, v66, v40, vcc\n"
        "v_lshlrev_b32 v39, 9, v39\n"
        "v_lshl_add_u32 v39, v37, 5, v39\n"
        "v_xor_b32 v40, 0, v38\n"
        "v_add_u32 v39, v39, v40\n"
        "v_add_u32 v39, 18240, v39\n"
        "v_cmp_le_u32 vcc, 192, v38\n"
        "s_nop 1\n"
        "v_cndmask_b32 v66, v66, v39, vcc\n"
        "v_mul_u32_u24 v70, 7168, v35\n"
        "v_add_u32 v70, v70, v36\n"
        "v_add_u32 v70, 0, v70\n"
        "s_waitcnt vmcnt(6)\n"
        "s_barrier\n"
        "s_mov_b32 m0, 98304\n"
        "s_add_u32 s86, s57, 1792\n"
        "buffer_load_dwordx4 v101, s[32:35], s86 offen  lds\n"
        "s_mov_b32 m0, 99328\n"
        "s_nop 0\n"
        "buffer_load_dwordx4 v102, s[32:35], s86 offen  lds\n"
        "s_mov_b32 m0, 100352\n"
        "s_add_u32 s86, s57, 34560\n"
        "buffer_load_dwordx4 v101, s[32:35], s86 offen  lds\n"
        "s_mov_b32 m0, 101376\n"
        "s_nop 0\n"
        "buffer_load_dwordx4 v102, s[32:35], s86 offen  lds\n"
        "s_mov_b32 m0, 114688\n"
        "s_add_u32 s86, s57, 1920\n"
        "buffer_load_dwordx4 v101, s[32:35], s86 offen  lds\n"
        "s_mov_b32 m0, 115712\n"
        "s_nop 0\n"
        "buffer_load_dwordx4 v102, s[32:35], s86 offen  lds\n"
        "s_mov_b32 m0, 116736\n"
        "s_add_u32 s86, s57, 34688\n"
        "buffer_load_dwordx4 v101, s[32:35], s86 offen  lds\n"
        "s_mov_b32 m0, 117760\n"
        "s_nop 0\n"
        "buffer_load_dwordx4 v102, s[32:35], s86 offen  lds\n"
        "v_add_u32 v34, 192, v32\n"
        "v_mul_u32_u24 v35, 2341, v34\n"
        "v_lshrrev_b32 v35, 16, v35\n"
        "v_mul_u32_u24 v36, 28, v35\n"
        "v_sub_u32 v36, v34, v36\n"
        "v_lshlrev_b32 v36, 4, v36\n"
        "v_min_u32 v37, 7, v35\n"
        "v_cmp_le_u32 vcc, 224, v36\n"
        "v_subrev_u32 v38, 224, v36\n"
        "v_mov_b32 v39, 0\n"
        "v_mov_b32 v40, 2\n"
        "v_cndmask_b32 v38, v36, v38, vcc\n"
        "v_cndmask_b32 v39, v39, v40, vcc\n"
        "v_mul_u32_u24 v67, 3584, v39\n"
        "v_mul_u32_u24 v40, 112, v37\n"
        "v_add_u32 v67, v40, v67\n"
        "v_add_u32 v67, v38, v67\n"
        "v_add_u32 v67, 4096, v67\n"
        "v_add_u32 v40, 5280, v67\n"
        "v_cmp_le_u32 vcc, 96, v38\n"
        "s_nop 1\n"
        "v_cndmask_b32 v67, v67, v40, vcc\n"
        "v_lshlrev_b32 v39, 9, v39\n"
        "v_lshl_add_u32 v39, v37, 5, v39\n"
        "v_xor_b32 v40, 0, v38\n"
        "v_add_u32 v39, v39, v40\n"
        "v_add_u32 v39, 18240, v39\n"
        "v_cmp_le_u32 vcc, 192, v38\n"
        "s_nop 1\n"
        "v_cndmask_b32 v67, v67, v39, vcc\n"
        "v_mul_u32_u24 v71, 7168, v35\n"
        "v_add_u32 v71, v71, v36\n"
        "v_add_u32 v71, 0, v71\n"
        "v_mov_b32 v37, 0x7ffffff0\n"
        "v_cmp_gt_u32 vcc, 32, v32\n"
        "s_nop 1\n"
        "v_cndmask_b32 v71, v37, v71, vcc\n"
        "s_waitcnt vmcnt(8)\n"
        "s_barrier\n"
        "s_waitcnt vmcnt(4)\n"
        "s_waitcnt vmcnt(0)\n"
        "s_barrier\n"
        "s_barrier\n"
        "ds_read_b128 v[0:3], v64 offset:61440\n"
        "ds_read_b128 v[4:7], v65 offset:61440\n"
        "ds_read_b128 v[8:11], v66 offset:61440\n"
        "ds_read_b128 v[12:15], v67 offset:61440\n"
        "s_add_u32 s75, s59, 0\n"
        "s_waitcnt lgkmcnt(3)\n"
        "buffer_store_dwordx4 v[0:3], v68, s[36:39], s75 offen sc1 nt\n"
        "s_waitcnt lgkmcnt(2)\n"
        "buffer_store_dwordx4 v[4:7], v69, s[36:39], s75 offen sc1 nt\n"
        "s_waitcnt lgkmcnt(1)\n"
        "buffer_store_dwordx4 v[8:11], v70, s[36:39], s75 offen sc1 nt\n"
        "s_waitcnt lgkmcnt(0)\n"
        "buffer_store_dwordx4 v[12:15], v71, s[36:39], s75 offen sc1 nt\n"
        "s_nop 1\n"
        "s_barrier\n"
        "ds_read_b128 v[16:19], v64 offset:0\n"
        "ds_read_b128 v[20:23], v65 offset:0\n"
        "ds_read_b128 v[24:27], v66 offset:0\n"
        "ds_read_b128 v[28:31], v67 offset:0\n"
        "s_add_u32 s76, s59, 114688\n"
        "s_waitcnt lgkmcnt(3)\n"
        "buffer_store_dwordx4 v[16:19], v68, s[36:39], s76 offen sc1 nt\n"
        "s_waitcnt lgkmcnt(2)\n"
        "buffer_store_dwordx4 v[20:23], v69, s[36:39], s76 offen sc1 nt\n"
        "s_waitcnt lgkmcnt(1)\n"
        "buffer_store_dwordx4 v[24:27], v70, s[36:39], s76 offen sc1 nt\n"
        "s_waitcnt lgkmcnt(0)\n"
        "buffer_store_dwordx4 v[28:31], v71, s[36:39], s76 offen sc1 nt\n"
        "s_nop 1\n"
        "s_barrier\n"
        "ds_read_b128 v[0:3], v64 offset:16384\n"
        "ds_read_b128 v[4:7], v65 offset:16384\n"
        "ds_read_b128 v[8:11], v66 offset:16384\n"
        "ds_read_b128 v[12:15], v67 offset:16384\n"
        "s_add_u32 s77, s59, 229376\n"
        "s_waitcnt lgkmcnt(3)\n"
        "buffer_store_dwordx4 v[0:3], v68, s[36:39], s77 offen sc1 nt\n"
        "s_waitcnt lgkmcnt(2)\n"
        "buffer_store_dwordx4 v[4:7], v69, s[36:39], s77 offen sc1 nt\n"
        "s_waitcnt lgkmcnt(1)\n"
        "buffer_store_dwordx4 v[8:11], v70, s[36:39], s77 offen sc1 nt\n"
        "s_waitcnt lgkmcnt(0)\n"
        "buffer_store_dwordx4 v[12:15], v71, s[36:39], s77 offen sc1 nt\n"
        "s_nop 1\n"
        "s_barrier\n"
        "ds_read_b128 v[16:19], v64 offset:32768\n"
        "ds_read_b128 v[20:23], v65 offset:32768\n"
        "ds_read_b128 v[24:27], v66 offset:32768\n"
        "ds_read_b128 v[28:31], v67 offset:32768\n"
        "s_add_u32 s74, s59, 344064\n"
        "s_waitcnt lgkmcnt(3)\n"
        "buffer_store_dwordx4 v[16:19], v68, s[36:39], s74 offen sc1 nt\n"
        "s_waitcnt lgkmcnt(2)\n"
        "buffer_store_dwordx4 v[20:23], v69, s[36:39], s74 offen sc1 nt\n"
        "s_waitcnt lgkmcnt(1)\n"
        "buffer_store_dwordx4 v[24:27], v70, s[36:39], s74 offen sc1 nt\n"
        "s_waitcnt lgkmcnt(0)\n"
        "buffer_store_dwordx4 v[28:31], v71, s[36:39], s74 offen sc1 nt\n"
        "s_nop 1\n"
        "s_endpgm\n"
        "Lhm1_%=:\n"
        "s_sleep 10\n"
        "buffer_load_dwordx4 v[0:3], v100, s[20:23], s52 offen nt\n"
        "s_add_u32 s76, s52, 14336\n"
        "buffer_load_dwordx4 v[4:7], v100, s[20:23], s76 offen nt\n"
        "s_add_u32 s77, s52, 28672\n"
        "buffer_load_dwordx4 v[8:11], v100, s[20:23], s77 offen nt\n"
        "s_add_u32 s74, s52, 43008\n"
        "buffer_load_dwordx4 v[12:15], v100, s[20:23], s74 offen nt\n"
        "s_add_u32 s75, s52, 57344\n"
        "buffer_load_dwordx4 v[16:19], v100, s[20:23], s75 offen nt\n"
        "s_add_u32 s76, s52, 71680\n"
        "buffer_load_dwordx4 v[20:23], v100, s[20:23], s76 offen nt\n"
        "s_add_u32 s77, s52, 86016\n"
        "buffer_load_dwordx4 v[24:27], v100, s[20:23], s77 offen nt\n"
        "s_add_u32 s74, s52, 100352\n"
        "buffer_load_dwordx4 v[28:31], v100, s[20:23], s74 offen nt\n"
        "s_barrier\n"
        "s_mov_b32 m0, 102400\n"
        "s_add_u32 s86, s57, 256\n"
        "buffer_load_dwordx4 v101, s[32:35], s86 offen  lds\n"
        "s_mov_b32 m0, 103424\n"
        "s_nop 0\n"
        "buffer_load_dwordx4 v102, s[32:35], s86 offen  lds\n"
        "s_mov_b32 m0, 104448\n"
        "s_add_u32 s86, s57, 33024\n"
        "buffer_load_dwordx4 v101, s[32:35], s86 offen  lds\n"
        "s_mov_b32 m0, 105472\n"
        "s_nop 0\n"
        "buffer_load_dwordx4 v102, s[32:35], s86 offen  lds\n"
        "s_waitcnt vmcnt(12)\n"
        "s_barrier\n"
        "s_mov_b32 m0, 118784\n"
        "s_add_u32 s86, s57, 384\n"
        "buffer_load_dwordx4 v101, s[32:35], s86 offen  lds\n"
        "s_mov_b32 m0, 119808\n"
        "s_nop 0\n"
        "buffer_load_dwordx4 v102, s[32:35], s86 offen  lds\n"
        "s_mov_b32 m0, 120832\n"
        "s_add_u32 s86, s57, 33152\n"
        "buffer_load_dwordx4 v101, s[32:35], s86 offen  lds\n"
        "s_mov_b32 m0, 121856\n"
        "s_nop 0\n"
        "buffer_load_dwordx4 v102, s[32:35], s86 offen  lds\n"
        "s_waitcnt vmcnt(4)\n"
        "s_barrier\n"
        "buffer_load_dwordx2 v[106:107], v104, s[28:31], 0 offen offset:272 sc1\n"
        "buffer_load_dwordx2 v[108:109], v104, s[28:31], 0 offen offset:336 sc1\n"
        "s_mov_b32 m0, 69632\n"
        "s_add_u32 s86, s57, 512\n"
        "buffer_load_dwordx4 v101, s[32:35], s86 offen  lds\n"
        "s_mov_b32 m0, 70656\n"
        "s_nop 0\n"
        "buffer_load_dwordx4 v102, s[32:35], s86 offen  lds\n"
        "s_mov_b32 m0, 71680\n"
        "s_add_u32 s86, s57, 33280\n"
        "buffer_load_dwordx4 v101, s[32:35], s86 offen  lds\n"
        "s_mov_b32 m0, 72704\n"
        "s_nop 0\n"
        "buffer_load_dwordx4 v102, s[32:35], s86 offen  lds\n"
        "v_lshl_or_b32 v0, v4, 4, v0\n"
        "v_lshl_or_b32 v1, v5, 4, v1\n"
        "v_lshl_or_b32 v2, v6, 4, v2\n"
        "v_lshl_or_b32 v3, v7, 4, v3\n"
        "v_lshl_or_b32 v8, v12, 4, v8\n"
        "v_lshl_or_b32 v9, v13, 4, v9\n"
        "v_lshl_or_b32 v10, v14, 4, v10\n"
        "v_lshl_or_b32 v11, v15, 4, v11\n"
        "v_lshl_or_b32 v0, v8, 8, v0\n"
        "v_lshl_or_b32 v1, v9, 8, v1\n"
        "v_lshl_or_b32 v2, v10, 8, v2\n"
        "v_lshl_or_b32 v3, v11, 8, v3\n"
        "v_lshl_or_b32 v16, v20, 4, v16\n"
        "v_lshl_or_b32 v17, v21, 4, v17\n"
        "v_lshl_or_b32 v18, v22, 4, v18\n"
        "v_lshl_or_b32 v19, v23, 4, v19\n"
        "v_lshl_or_b32 v24, v28, 4, v24\n"
        "v_lshl_or_b32 v25, v29, 4, v25\n"
        "v_lshl_or_b32 v26, v30, 4, v26\n"
        "v_lshl_or_b32 v27, v31, 4, v27\n"
        "v_lshl_or_b32 v16, v24, 8, v16\n"
        "v_lshl_or_b32 v17, v25, 8, v17\n"
        "v_lshl_or_b32 v18, v26, 8, v18\n"
        "v_lshl_or_b32 v19, v27, 8, v19\n"
        "v_lshl_or_b32 v4, v16, 16, v0\n"
        "v_lshl_or_b32 v5, v17, 16, v1\n"
        "v_lshl_or_b32 v6, v18, 16, v2\n"
        "v_lshl_or_b32 v7, v19, 16, v3\n"
        "s_mov_b64 vcc, s[4:5]\n"
        "s_nop 0\n"
        "v_cndmask_b32_dpp v8, v5, v4, vcc quad_perm:[1,0,3,2] row_mask:0xf bank_mask:0xf\n"
        "v_cndmask_b32_dpp v10, v7, v6, vcc quad_perm:[1,0,3,2] row_mask:0xf bank_mask:0xf\n"
        "s_not_b64 vcc, vcc\n"
        "s_nop 0\n"
        "v_cndmask_b32_dpp v9, v4, v5, vcc quad_perm:[1,0,3,2] row_mask:0xf bank_mask:0xf\n"
        "v_cndmask_b32_dpp v11, v6, v7, vcc quad_perm:[1,0,3,2] row_mask:0xf bank_mask:0xf\n"
        "s_mov_b64 vcc, s[6:7]\n"
        "s_nop 0\n"
        "v_cndmask_b32_dpp v28, v10, v8, vcc quad_perm:[2,3,0,1] row_mask:0xf bank_mask:0xf\n"
        "v_cndmask_b32_dpp v29, v11, v9, vcc quad_perm:[2,3,0,1] row_mask:0xf bank_mask:0xf\n"
        "s_not_b64 vcc, vcc\n"
        "s_nop 0\n"
        "v_cndmask_b32_dpp v30, v8, v10, vcc quad_perm:[2,3,0,1] row_mask:0xf bank_mask:0xf\n"
        "v_cndmask_b32_dpp v31, v9, v11, vcc quad_perm:[2,3,0,1] row_mask:0xf bank_mask:0xf\n"
        "buffer_store_dwordx4 v[28:31], v103, s[24:27], s53 offen sc1\n"
        "s_nop 1\n"
        "s_waitcnt vmcnt(7)\n"
        "s_barrier\n"
        "buffer_load_dwordx2 v[110:111], v104, s[28:31], 0 offen offset:400 sc1\n"
        "s_waitcnt vmcnt(7)\n"
        "v_cmp_ne_u32 vcc, s64, v106\n"
        "v_cmp_ne_u32 s[78:79], s65, v107\n"
        "s_or_b64 vcc, vcc, s[78:79]\n"
        "s_cbranch_scc1 Ls53_%=\n"
        "Lk54_%=:\n"
        "s_mov_b32 m0, 133120\n"
        "s_add_u32 s75, s56, 69632\n"
        "buffer_load_dwordx4 v103, s[24:27], s75 offen sc1 lds\n"
        "s_mov_b32 m0, 134144\n"
        "s_add_u32 s75, s75, 2048\n"
        "buffer_load_dwordx4 v103, s[24:27], s75 offen sc1 lds\n"
        "Le55_%=:\n"
        "s_waitcnt vmcnt(8)\n"
        "v_cmp_ne_u32 vcc, s64, v108\n"
        "v_cmp_ne_u32 s[78:79], s65, v109\n"
        "s_or_b64 vcc, vcc, s[78:79]\n"
        "s_cbranch_scc1 Ls56_%=\n"
        "Lk57_%=:\n"
        "s_mov_b32 m0, 141312\n"
        "s_add_u32 s76, s56, 86016\n"
        "buffer_load_dwordx4 v103, s[24:27], s76 offen sc1 lds\n"
        "s_mov_b32 m0, 142336\n"
        "s_add_u32 s76, s76, 2048\n"
        "buffer_load_dwordx4 v103, s[24:27], s76 offen sc1 lds\n"
        "Le58_%=:\n"
        "s_mov_b32 m0, 86016\n"
        "s_add_u32 s86, s57, 640\n"
        "buffer_load_dwordx4 v101, s[32:35], s86 offen  lds\n"
        "s_mov_b32 m0, 87040\n"
        "s_nop 0\n"
        "buffer_load_dwordx4 v102, s[32:35], s86 offen  lds\n"
        "s_mov_b32 m0, 88064\n"
        "s_add_u32 s86, s57, 33408\n"
        "buffer_load_dwordx4 v101, s[32:35], s86 offen  lds\n"
        "s_mov_b32 m0, 89088\n"
        "s_nop 0\n"
        "buffer_load_dwordx4 v102, s[32:35], s86 offen  lds\n"
        "s_waitcnt vmcnt(9)\n"
        "v_mov_b32 v36, s64\n"
        "v_mov_b32 v37, s65\n"
        "s_lshl_b32 s74, s55, 3\n"
        "v_mov_b32 v38, s74\n"
        "s_mov_b64 exec, 1\n"
        "buffer_store_dwordx2 v[36:37], v38, s[28:31], 0 offen sc1\n"
        "s_mov_b64 exec, -1\n"
        "s_waitcnt vmcnt(7)\n"
        "s_waitcnt vmcnt(5)\n"
        "s_barrier\n"
        "buffer_load_dwordx2 v[112:113], v104, s[28:31], 0 offen offset:464 sc1\n"
        "v_cmp_ne_u32 vcc, s64, v110\n"
        "v_cmp_ne_u32 s[78:79], s65, v111\n"
        "s_or_b64 vcc, vcc, s[78:79]\n"
        "s_cbranch_scc1 Ls59_%=\n"
        "Lk60_%=:\n"
        "s_mov_b32 m0, 149504\n"
        "s_add_u32 s77, s56, 102400\n"
        "buffer_load_dwordx4 v103, s[24:27], s77 offen sc1 lds\n"
        "s_mov_b32 m0, 150528\n"
        "s_add_u32 s77, s77, 2048\n"
        "buffer_load_dwordx4 v103, s[24:27], s77 offen sc1 lds\n"
        "Le61_%=:\n"
        "s_mov_b32 m0, 102400\n"
        "s_add_u32 s86, s57, 768\n"
        "buffer_load_dwordx4 v101, s[32:35], s86 offen  lds\n"
        "s_mov_b32 m0, 103424\n"
        "s_nop 0\n"
        "buffer_load_dwordx4 v102, s[32:35], s86 offen  lds\n"
        "s_mov_b32 m0, 104448\n"
        "s_add_u32 s86, s57, 33536\n"
        "buffer_load_dwordx4 v101, s[32:35], s86 offen  lds\n"
        "s_mov_b32 m0, 105472\n"
        "s_nop 0\n"
        "buffer_load_dwordx4 v102, s[32:35], s86 offen  lds\n"
        "s_waitcnt vmcnt(8)\n"
        "s_waitcnt vmcnt(4)\n"
        "s_barrier\n"
        "buffer_load_dwordx2 v[106:107], v104, s[28:31], 0 offen offset:528 sc1\n"
        "v_cmp_ne_u32 vcc, s64, v112\n"
        "v_cmp_ne_u32 s[78:79], s65, v113\n"
        "s_or_b64 vcc, vcc, s[78:79]\n"
        "s_cbranch_scc1 Ls62_%=\n"
        "Lk63_%=:\n"
        "s_mov_b32 m0, 157696\n"
        "s_add_u32 s74, s56, 118784\n"
        "buffer_load_dwordx4 v103, s[24:27], s74 offen sc1 lds\n"
        "s_mov_b32 m0, 158720\n"
        "s_add_u32 s74, s74, 2048\n"
        "buffer_load_dwordx4 v103, s[24:27], s74 offen sc1 lds\n"
        "Le64_%=:\n"
        "s_mov_b32 m0, 118784\n"
        "s_add_u32 s86, s57, 896\n"
        "buffer_load_dwordx4 v101, s[32:35], s86 offen  lds\n"
        "s_mov_b32 m0, 119808\n"
        "s_nop 0\n"
        "buffer_load_dwordx4 v102, s[32:35], s86 offen  lds\n"
        "s_mov_b32 m0, 120832\n"
        "s_add_u32 s86, s57, 33664\n"
        "buffer_load_dwordx4 v101, s[32:35], s86 offen  lds\n"
        "s_mov_b32 m0, 121856\n"
        "s_nop 0\n"
        "buffer_load_dwordx4 v102, s[32:35], s86 offen  lds\n"
        "s_waitcnt vmcnt(7)\n"
        "s_waitcnt vmcnt(4)\n"
        "s_barrier\n"
        "buffer_load_dwordx2 v[108:109], v104, s[28:31], 0 offen offset:592 sc1\n"
        "buffer_load_dwordx2 v[110:111], v104, s[28:31], 0 offen offset:656 sc1\n"
        "buffer_load_dwordx2 v[112:113], v104, s[28:31], 0 offen offset:720 sc1\n"
        "v_cmp_ne_u32 vcc, s64, v106\n"
        "v_cmp_ne_u32 s[78:79], s65, v107\n"
        "s_or_b64 vcc, vcc, s[78:79]\n"
        "s_cbranch_scc1 Ls65_%=\n"
        "Lk66_%=:\n"
        "s_mov_b32 m0, 133120\n"
        "s_add_u32 s75, s56, 135168\n"
        "buffer_load_dwordx4 v103, s[24:27], s75 offen sc1 lds\n"
        "s_mov_b32 m0, 134144\n"
        "s_add_u32 s75, s75, 2048\n"
        "buffer_load_dwordx4 v103, s[24:27], s75 offen sc1 lds\n"
        "Le67_%=:\n"
        "s_mov_b32 m0, 69632\n"
        "s_add_u32 s86, s57, 1024\n"
        "buffer_load_dwordx4 v101, s[32:35], s86 offen  lds\n"
        "s_mov_b32 m0, 70656\n"
        "s_nop 0\n"
        "buffer_load_dwordx4 v102, s[32:35], s86 offen  lds\n"
        "s_mov_b32 m0, 71680\n"
        "s_add_u32 s86, s57, 33792\n"
        "buffer_load_dwordx4 v101, s[32:35], s86 offen  lds\n"
        "s_mov_b32 m0, 72704\n"
        "s_nop 0\n"
        "buffer_load_dwordx4 v102, s[32:35], s86 offen  lds\n"
        "s_waitcnt vmcnt(9)\n"
        "s_waitcnt vmcnt(4)\n"
        "s_barrier\n"
        "buffer_load_dwordx2 v[106:107], v104, s[28:31], 0 offen offset:784 sc1\n"
        "v_cmp_ne_u32 vcc, s64, v108\n"
        "v_cmp_ne_u32 s[78:79], s65, v109\n"
        "s_or_b64 vcc, vcc, s[78:79]\n"
        "s_cbranch_scc1 Ls68_%=\n"
        "Lk69_%=:\n"
        "s_mov_b32 m0, 141312\n"
        "s_add_u32 s76, s56, 151552\n"
        "buffer_load_dwordx4 v103, s[24:27], s76 offen sc1 lds\n"
        "s_mov_b32 m0, 142336\n"
        "s_add_u32 s76, s76, 2048\n"
        "buffer_load_dwordx4 v103, s[24:27], s76 offen sc1 lds\n"
        "Le70_%=:\n"
        "v_cmp_ne_u32 vcc, s64, v110\n"
        "v_cmp_ne_u32 s[78:79], s65, v111\n"
        "s_or_b64 vcc, vcc, s[78:79]\n"
        "s_cbranch_scc1 Ls71_%=\n"
        "Lk72_%=:\n"
        "s_mov_b32 m0, 149504\n"
        "s_add_u32 s77, s56, 167936\n"
        "buffer_load_dwordx4 v103, s[24:27], s77 offen sc1 lds\n"
        "s_mov_b32 m0, 150528\n"
        "s_add_u32 s77, s77, 2048\n"
        "buffer_load_dwordx4 v103, s[24:27], s77 offen sc1 lds\n"
        "Le73_%=:\n"
        "v_cmp_ne_u32 vcc, s64, v112\n"
        "v_cmp_ne_u32 s[78:79], s65, v113\n"
        "s_or_b64 vcc, vcc, s[78:79]\n"
        "s_cbranch_scc1 Ls74_%=\n"
        "Lk75_%=:\n"
        "s_mov_b32 m0, 157696\n"
        "s_add_u32 s74, s56, 184320\n"
        "buffer_load_dwordx4 v103, s[24:27], s74 offen sc1 lds\n"
        "s_mov_b32 m0, 158720\n"
        "s_add_u32 s74, s74, 2048\n"
        "buffer_load_dwordx4 v103, s[24:27], s74 offen sc1 lds\n"
        "Le76_%=:\n"
        "s_mov_b32 m0, 86016\n"
        "s_add_u32 s86, s57, 1152\n"
        "buffer_load_dwordx4 v101, s[32:35], s86 offen  lds\n"
        "s_mov_b32 m0, 87040\n"
        "s_nop 0\n"
        "buffer_load_dwordx4 v102, s[32:35], s86 offen  lds\n"
        "s_mov_b32 m0, 88064\n"
        "s_add_u32 s86, s57, 33920\n"
        "buffer_load_dwordx4 v101, s[32:35], s86 offen  lds\n"
        "s_mov_b32 m0, 89088\n"
        "s_nop 0\n"
        "buffer_load_dwordx4 v102, s[32:35], s86 offen  lds\n"
        "s_waitcnt vmcnt(11)\n"
        "s_waitcnt vmcnt(8)\n"
        "s_barrier\n"
        "buffer_load_dwordx2 v[108:109], v104, s[28:31], 0 offen offset:848 sc1\n"
        "v_cmp_ne_u32 vcc, s64, v106\n"
        "v_cmp_ne_u32 s[78:79], s65, v107\n"
        "s_or_b64 vcc, vcc, s[78:79]\n"
        "s_cbranch_scc1 Ls77_%=\n"
        "Lk78_%=:\n"
        "s_mov_b32 m0, 133120\n"
        "s_add_u32 s75, s56, 200704\n"
        "buffer_load_dwordx4 v103, s[24:27], s75 offen sc1 lds\n"
        "s_mov_b32 m0, 134144\n"
        "s_add_u32 s75, s75, 2048\n"
        "buffer_load_dwordx4 v103, s[24:27], s75 offen sc1 lds\n"
        "Le79_%=:\n"
        "s_mov_b32 m0, 102400\n"
        "s_add_u32 s86, s57, 1280\n"
        "buffer_load_dwordx4 v101, s[32:35], s86 offen  lds\n"
        "s_mov_b32 m0, 103424\n"
        "s_nop 0\n"
        "buffer_load_dwordx4 v102, s[32:35], s86 offen  lds\n"
        "s_mov_b32 m0, 104448\n"
        "s_add_u32 s86, s57, 34048\n"
        "buffer_load_dwordx4 v101, s[32:35], s86 offen  lds\n"
        "s_mov_b32 m0, 105472\n"
        "s_nop 0\n"
        "buffer_load_dwordx4 v102, s[32:35], s86 offen  lds\n"
        "s_waitcnt vmcnt(7)\n"
        "s_barrier\n"
        "buffer_load_dwordx2 v[110:111], v104, s[28:31], 0 offen offset:912 sc1\n"
        "s_waitcnt vmcnt(7)\n"
        "v_cmp_ne_u32 vcc, s64, v108\n"
        "v_cmp_ne_u32 s[78:79], s65, v109\n"
        "s_or_b64 vcc, vcc, s[78:79]\n"
        "s_cbranch_scc1 Ls80_%=\n"
        "Lk81_%=:\n"
        "s_mov_b32 m0, 141312\n"
        "s_add_u32 s76, s56, 217088\n"
        "buffer_load_dwordx4 v103, s[24:27], s76 offen sc1 lds\n"
        "s_mov_b32 m0, 142336\n"
        "s_add_u32 s76, s76, 2048\n"
        "buffer_load_dwordx4 v103, s[24:27], s76 offen sc1 lds\n"
        "Le82_%=:\n"
        "s_mov_b32 m0, 118784\n"
        "s_add_u32 s86, s57, 1408\n"
        "buffer_load_dwordx4 v101, s[32:35], s86 offen  lds\n"
        "s_mov_b32 m0, 119808\n"
        "s_nop 0\n"
        "buffer_load_dwordx4 v102, s[32:35], s86 offen  lds\n"
        "s_mov_b32 m0, 120832\n"
        "s_add_u32 s86, s57, 34176\n"
        "buffer_load_dwordx4 v101, s[32:35], s86 offen  lds\n"
        "s_mov_b32 m0, 121856\n"
        "s_nop 0\n"
        "buffer_load_dwordx4 v102, s[32:35], s86 offen  lds\n"
        "s_mov_b32 s36, s16\n"
        "s_and_b32 s37, s17, 0xffff\n"
        "s_mov_b32 s38, 14680064\n"
        "s_mov_b32 s39, 0x20000\n"
        "s_mul_i32 s59, s46, 917504\n"
        "s_mul_i32 s74, s45, 448\n"
        "s_add_u32 s59, s59, s74\n"
        "v_add_u32 v34, 0, v32\n"
        "v_mul_u32_u24 v35, 2341, v34\n"
        "v_lshrrev_b32 v35, 16, v35\n"
        "v_mul_u32_u24 v36, 28, v35\n"
        "v_sub_u32 v36, v34, v36\n"
        "v_lshlrev_b32 v36, 4, v36\n"
        "v_min_u32 v37, 7, v35\n"
        "v_cmp_le_u32 vcc, 224, v36\n"
        "v_subrev_u32 v38, 224, v36\n"
        "v_mov_b32 v39, 0\n"
        "v_mov_b32 v40, 2\n"
        "v_cndmask_b32 v38, v36, v38, vcc\n"
        "v_cndmask_b32 v39, v39, v40, vcc\n"
        "v_mul_u32_u24 v64, 3584, v39\n"
        "v_mul_u32_u24 v40, 112, v37\n"
        "v_add_u32 v64, v40, v64\n"
        "v_add_u32 v64, v38, v64\n"
        "v_add_u32 v64, 4992, v64\n"
        "v_add_u32 v40, 5280, v64\n"
        "v_cmp_le_u32 vcc, 96, v38\n"
        "s_nop 1\n"
        "v_cndmask_b32 v64, v64, v40, vcc\n"
        "v_lshlrev_b32 v39, 9, v39\n"
        "v_lshl_add_u32 v39, v37, 5, v39\n"
        "v_xor_b32 v40, 16, v38\n"
        "v_add_u32 v39, v39, v40\n"
        "v_add_u32 v39, 18496, v39\n"
        "v_cmp_le_u32 vcc, 192, v38\n"
        "s_nop 1\n"
        "v_cndmask_b32 v64, v64, v39, vcc\n"
        "v_mul_u32_u24 v68, 7168, v35\n"
        "v_add_u32 v68, v68, v36\n"
        "v_add_u32 v68, 57344, v68\n"
        "s_waitcnt vmcnt(7)\n"
        "s_barrier\n"
        "buffer_load_dwordx2 v[112:113], v104, s[28:31], 0 offen offset:976 sc1\n"
        "s_waitcnt vmcnt(7)\n"
        "v_cmp_ne_u32 vcc, s64, v110\n"
        "v_cmp_ne_u32 s[78:79], s65, v111\n"
        "s_or_b64 vcc, vcc, s[78:79]\n"
        "s_cbranch_scc1 Ls83_%=\n"
        "Lk84_%=:\n"
        "s_mov_b32 m0, 149504\n"
        "s_add_u32 s77, s56, 233472\n"
        "buffer_load_dwordx4 v103, s[24:27], s77 offen sc1 lds\n"
        "s_mov_b32 m0, 150528\n"
        "s_add_u32 s77, s77, 2048\n"
        "buffer_load_dwordx4 v103, s[24:27], s77 offen sc1 lds\n"
        "Le85_%=:\n"
        "s_mov_b32 m0, 69632\n"
        "s_add_u32 s86, s57, 1536\n"
        "buffer_load_dwordx4 v101, s[32:35], s86 offen  lds\n"
        "s_mov_b32 m0, 70656\n"
        "s_nop 0\n"
        "buffer_load_dwordx4 v102, s[32:35], s86 offen  lds\n"
        "s_mov_b32 m0, 71680\n"
        "s_add_u32 s86, s57, 34304\n"
        "buffer_load_dwordx4 v101, s[32:35], s86 offen  lds\n"
        "s_mov_b32 m0, 72704\n"
        "s_nop 0\n"
        "buffer_load_dwordx4 v102, s[32:35], s86 offen  lds\n"
        "v_add_u32 v34, 64, v32\n"
        "v_mul_u32_u24 v35, 2341, v34\n"
        "v_lshrrev_b32 v35, 16, v35\n"
        "v_mul_u32_u24 v36, 28, v35\n"
        "v_sub_u32 v36, v34, v36\n"
        "v_lshlrev_b32 v36, 4, v36\n"
        "v_min_u32 v37, 7, v35\n"
        "v_cmp_le_u32 vcc, 224, v36\n"
        "v_subrev_u32 v38, 224, v36\n"
        "v_mov_b32 v39, 0\n"
        "v_mov_b32 v40, 2\n"
        "v_cndmask_b32 v38, v36, v38, vcc\n"
        "v_cndmask_b32 v39, v39, v40, vcc\n"
        "v_mul_u32_u24 v65, 3584, v39\n"
        "v_mul_u32_u24 v40, 112, v37\n"
        "v_add_u32 v65, v40, v65\n"
        "v_add_u32 v65, v38, v65\n"
        "v_add_u32 v65, 4992, v65\n"
        "v_add_u32 v40, 5280, v65\n"
        "v_cmp_le_u32 vcc, 96, v38\n"
        "s_nop 1\n"
        "v_cndmask_b32 v65, v65, v40, vcc\n"
        "v_lshlrev_b32 v39, 9, v39\n"
        "v_lshl_add_u32 v39, v37, 5, v39\n"
        "v_xor_b32 v40, 16, v38\n"
        "v_add_u32 v39, v39, v40\n"
        "v_add_u32 v39, 18496, v39\n"
        "v_cmp_le_u32 vcc, 192, v38\n"
        "s_nop 1\n"
        "v_cndmask_b32 v65, v65, v39, vcc\n"
        "v_mul_u32_u24 v69, 7168, v35\n"
        "v_add_u32 v69, v69, v36\n"
        "v_add_u32 v69, 57344, v69\n"
        "s_waitcnt vmcnt(7)\n"
        "s_barrier\n"
        "s_waitcnt vmcnt(6)\n"
        "v_cmp_ne_u32 vcc, s64, v112\n"
        "v_cmp_ne_u32 s[78:79], s65, v113\n"
        "s_or_b64 vcc, vcc, s[78:79]\n"
        "s_cbranch_scc1 Ls86_%=\n"
        "Lk87_%=:\n"
        "s_mov_b32 m0, 157696\n"
        "s_add_u32 s74, s56, 249856\n"
        "buffer_load_dwordx4 v103, s[24:27], s74 offen sc1 lds\n"
        "s_mov_b32 m0, 158720\n"
        "s_add_u32 s74, s74, 2048\n"
        "buffer_load_dwordx4 v103, s[24:27], s74 offen sc1 lds\n"
        "Le88_%=:\n"
        "s_mov_b32 m0, 86016\n"
        "s_add_u32 s86, s57, 1664\n"
        "buffer_load_dwordx4 v101, s[32:35], s86 offen  lds\n"
        "s_mov_b32 m0, 87040\n"
        "s_nop 0\n"
        "buffer_load_dwordx4 v102, s[32:35], s86 offen  lds\n"
        "s_mov_b32 m0, 88064\n"
        "s_add_u32 s86, s57, 34432\n"
        "buffer_load_dwordx4 v101, s[32:35], s86 offen  lds\n"
        "s_mov_b32 m0, 89088\n"
        "s_nop 0\n"
        "buffer_load_dwordx4 v102, s[32:35], s86 offen  lds\n"
        "v_add_u32 v34, 128, v32\n"
        "v_mul_u32_u24 v35, 2341, v34\n"
        "v_lshrrev_b32 v35, 16, v35\n"
        "v_mul_u32_u24 v36, 28, v35\n"
        "v_sub_u32 v36, v34, v36\n"
        "v_lshlrev_b32 v36, 4, v36\n"
        "v_min_u32 v37, 7, v35\n"
        "v_cmp_le_u32 vcc, 224, v36\n"
        "v_subrev_u32 v38, 224, v36\n"
        "v_mov_b32 v39, 0\n"
        "v_mov_b32 v40, 2\n"
        "v_cndmask_b32 v38, v36, v38, vcc\n"
        "v_cndmask_b32 v39, v39, v40, vcc\n"
        "v_mul_u32_u24 v66, 3584, v39\n"
        "v_mul_u32_u24 v40, 112, v37\n"
        "v_add_u32 v66, v40, v66\n"
        "v_add_u32 v66, v38, v66\n"
        "v_add_u32 v66, 4992, v66\n"
        "v_add_u32 v40, 5280, v66\n"
        "v_cmp_le_u32 vcc, 96, v38\n"
        "s_nop 1\n"
        "v_cndmask_b32 v66, v66, v40, vcc\n"
        "v_lshlrev_b32 v39, 9, v39\n"
        "v_lshl_add_u32 v39, v37, 5, v39\n"
        "v_xor_b32 v40, 16, v38\n"
        "v_add_u32 v39, v39, v40\n"
        "v_add_u32 v39, 18496, v39\n"
        "v_cmp_le_u32 vcc, 192, v38\n"
        "s_nop 1\n"
        "v_cndmask_b32 v66, v66, v39, vcc\n"
        "v_mul_u32_u24 v70, 7168, v35\n"
        "v_add_u32 v70, v70, v36\n"
        "v_add_u32 v70, 57344, v70\n"
        "s_waitcnt vmcnt(6)\n"
        "s_barrier\n"
        "s_mov_b32 m0, 102400\n"
        "s_add_u32 s86, s57, 1792\n"
        "buffer_load_dwordx4 v101, s[32:35], s86 offen  lds\n"
        "s_mov_b32 m0, 103424\n"
        "s_nop 0\n"
        "buffer_load_dwordx4 v102, s[32:35], s86 offen  lds\n"
        "s_mov_b32 m0, 104448\n"
        "s_add_u32 s86, s57, 34560\n"
        "buffer_load_dwordx4 v101, s[32:35], s86 offen  lds\n"
        "s_mov_b32 m0, 105472\n"
        "s_nop 0\n"
        "buffer_load_dwordx4 v102, s[32:35], s86 offen  lds\n"
        "s_mov_b32 m0, 118784\n"
        "s_add_u32 s86, s57, 1920\n"
        "buffer_load_dwordx4 v101, s[32:35], s86 offen  lds\n"
        "s_mov_b32 m0, 119808\n"
        "s_nop 0\n"
        "buffer_load_dwordx4 v102, s[32:35], s86 offen  lds\n"
        "s_mov_b32 m0, 120832\n"
        "s_add_u32 s86, s57, 34688\n"
        "buffer_load_dwordx4 v101, s[32:35], s86 offen  lds\n"
        "s_mov_b32 m0, 121856\n"
        "s_nop 0\n"
        "buffer_load_dwordx4 v102, s[32:35], s86 offen  lds\n"
        "v_add_u32 v34, 192, v32\n"
        "v_mul_u32_u24 v35, 2341, v34\n"
        "v_lshrrev_b32 v35, 16, v35\n"
        "v_mul_u32_u24 v36, 28, v35\n"
        "v_sub_u32 v36, v34, v36\n"
        "v_lshlrev_b32 v36, 4, v36\n"
        "v_min_u32 v37, 7, v35\n"
        "v_cmp_le_u32 vcc, 224, v36\n"
        "v_subrev_u32 v38, 224, v36\n"
        "v_mov_b32 v39, 0\n"
        "v_mov_b32 v40, 2\n"
        "v_cndmask_b32 v38, v36, v38, vcc\n"
        "v_cndmask_b32 v39, v39, v40, vcc\n"
        "v_mul_u32_u24 v67, 3584, v39\n"
        "v_mul_u32_u24 v40, 112, v37\n"
        "v_add_u32 v67, v40, v67\n"
        "v_add_u32 v67, v38, v67\n"
        "v_add_u32 v67, 4992, v67\n"
        "v_add_u32 v40, 5280, v67\n"
        "v_cmp_le_u32 vcc, 96, v38\n"
        "s_nop 1\n"
        "v_cndmask_b32 v67, v67, v40, vcc\n"
        "v_lshlrev_b32 v39, 9, v39\n"
        "v_lshl_add_u32 v39, v37, 5, v39\n"
        "v_xor_b32 v40, 16, v38\n"
        "v_add_u32 v39, v39, v40\n"
        "v_add_u32 v39, 18496, v39\n"
        "v_cmp_le_u32 vcc, 192, v38\n"
        "s_nop 1\n"
        "v_cndmask_b32 v67, v67, v39, vcc\n"
        "v_mul_u32_u24 v71, 7168, v35\n"
        "v_add_u32 v71, v71, v36\n"
        "v_add_u32 v71, 57344, v71\n"
        "v_mov_b32 v37, 0x7ffffff0\n"
        "v_cmp_gt_u32 vcc, 32, v32\n"
        "s_nop 1\n"
        "v_cndmask_b32 v71, v37, v71, vcc\n"
        "s_waitcnt vmcnt(8)\n"
        "s_barrier\n"
        "s_waitcnt vmcnt(4)\n"
        "s_waitcnt vmcnt(0)\n"
        "s_barrier\n"
        "s_barrier\n"
        "ds_read_b128 v[0:3], v64 offset:61440\n"
        "ds_read_b128 v[4:7], v65 offset:61440\n"
        "ds_read_b128 v[8:11], v66 offset:61440\n"
        "ds_read_b128 v[12:15], v67 offset:61440\n"
        "s_add_u32 s75, s59, 0\n"
        "s_waitcnt lgkmcnt(3)\n"
        "buffer_store_dwordx4 v[0:3], v68, s[36:39], s75 offen sc1 nt\n"
        "s_waitcnt lgkmcnt(2)\n"
        "buffer_store_dwordx4 v[4:7], v69, s[36:39], s75 offen sc1 nt\n"
        "s_waitcnt lgkmcnt(1)\n"
        "buffer_store_dwordx4 v[8:11], v70, s[36:39], s75 offen sc1 nt\n"
        "s_waitcnt lgkmcnt(0)\n"
        "buffer_store_dwordx4 v[12:15], v71, s[36:39], s75 offen sc1 nt\n"
        "s_nop 1\n"
        "s_barrier\n"
        "ds_read_b128 v[16:19], v64 offset:0\n"
        "ds_read_b128 v[20:23], v65 offset:0\n"
        "ds_read_b128 v[24:27], v66 offset:0\n"
        "ds_read_b128 v[28:31], v67 offset:0\n"
        "s_add_u32 s76, s59, 114688\n"
        "s_waitcnt lgkmcnt(3)\n"
        "buffer_store_dwordx4 v[16:19], v68, s[36:39], s76 offen sc1 nt\n"
        "s_waitcnt lgkmcnt(2)\n"
        "buffer_store_dwordx4 v[20:23], v69, s[36:39], s76 offen sc1 nt\n"
        "s_waitcnt lgkmcnt(1)\n"
        "buffer_store_dwordx4 v[24:27], v70, s[36:39], s76 offen sc1 nt\n"
        "s_waitcnt lgkmcnt(0)\n"
        "buffer_store_dwordx4 v[28:31], v71, s[36:39], s76 offen sc1 nt\n"
        "s_nop 1\n"
        "s_barrier\n"
        "ds_read_b128 v[0:3], v64 offset:16384\n"
        "ds_read_b128 v[4:7], v65 offset:16384\n"
        "ds_read_b128 v[8:11], v66 offset:16384\n"
        "ds_read_b128 v[12:15], v67 offset:16384\n"
        "s_add_u32 s77, s59, 229376\n"
        "s_waitcnt lgkmcnt(3)\n"
        "buffer_store_dwordx4 v[0:3], v68, s[36:39], s77 offen sc1 nt\n"
        "s_waitcnt lgkmcnt(2)\n"
        "buffer_store_dwordx4 v[4:7], v69, s[36:39], s77 offen sc1 nt\n"
        "s_waitcnt lgkmcnt(1)\n"
        "buffer_store_dwordx4 v[8:11], v70, s[36:39], s77 offen sc1 nt\n"
        "s_waitcnt lgkmcnt(0)\n"
        "buffer_store_dwordx4 v[12:15], v71, s[36:39], s77 offen sc1 nt\n"
        "s_nop 1\n"
        "s_barrier\n"
        "ds_read_b128 v[16:19], v64 offset:32768\n"
        "ds_read_b128 v[20:23], v65 offset:32768\n"
        "ds_read_b128 v[24:27], v66 offset:32768\n"
        "ds_read_b128 v[28:31], v67 offset:32768\n"
        "s_add_u32 s74, s59, 344064\n"
        "s_waitcnt lgkmcnt(3)\n"
        "buffer_store_dwordx4 v[16:19], v68, s[36:39], s74 offen sc1 nt\n"
        "s_waitcnt lgkmcnt(2)\n"
        "buffer_store_dwordx4 v[20:23], v69, s[36:39], s74 offen sc1 nt\n"
        "s_waitcnt lgkmcnt(1)\n"
        "buffer_store_dwordx4 v[24:27], v70, s[36:39], s74 offen sc1 nt\n"
        "s_waitcnt lgkmcnt(0)\n"
        "buffer_store_dwordx4 v[28:31], v71, s[36:39], s74 offen sc1 nt\n"
        "s_nop 1\n"
        "s_endpgm\n"
        "Lhm2_%=:\n"
        "s_waitcnt vmcnt(8)\n"
        "v_lshl_or_b32 v0, v4, 4, v0\n"
        "v_lshl_or_b32 v1, v5, 4, v1\n"
        "v_lshl_or_b32 v2, v6, 4, v2\n"
        "v_lshl_or_b32 v3, v7, 4, v3\n"
        "v_lshl_or_b32 v8, v12, 4, v8\n"
        "v_lshl_or_b32 v9, v13, 4, v9\n"
        "v_lshl_or_b32 v10, v14, 4, v10\n"
        "v_lshl_or_b32 v11, v15, 4, v11\n"
        "v_lshl_or_b32 v0, v8, 8, v0\n"
        "v_lshl_or_b32 v1, v9, 8, v1\n"
        "v_lshl_or_b32 v2, v10, 8, v2\n"
        "v_lshl_or_b32 v3, v11, 8, v3\n"
        "s_waitcnt vmcnt(4)\n"
        "v_lshl_or_b32 v16, v20, 4, v16\n"
        "v_lshl_or_b32 v17, v21, 4, v17\n"
        "v_lshl_or_b32 v18, v22, 4, v18\n"
        "v_lshl_or_b32 v19, v23, 4, v19\n"
        "v_lshl_or_b32 v24, v28, 4, v24\n"
        "v_lshl_or_b32 v25, v29, 4, v25\n"
        "v_lshl_or_b32 v26, v30, 4, v26\n"
        "v_lshl_or_b32 v27, v31, 4, v27\n"
        "v_lshl_or_b32 v16, v24, 8, v16\n"
        "v_lshl_or_b32 v17, v25, 8, v17\n"
        "v_lshl_or_b32 v18, v26, 8, v18\n"
        "v_lshl_or_b32 v19, v27, 8, v19\n"
        "v_lshl_or_b32 v4, v16, 16, v0\n"
        "v_lshl_or_b32 v5, v17, 16, v1\n"
        "v_lshl_or_b32 v6, v18, 16, v2\n"
        "v_lshl_or_b32 v7, v19, 16, v3\n"
        "s_mov_b64 vcc, s[4:5]\n"
        "s_nop 0\n"
        "v_cndmask_b32_dpp v8, v5, v4, vcc quad_perm:[1,0,3,2] row_mask:0xf bank_mask:0xf\n"
        "v_cndmask_b32_dpp v10, v7, v6, vcc quad_perm:[1,0,3,2] row_mask:0xf bank_mask:0xf\n"
        "s_not_b64 vcc, vcc\n"
        "s_nop 0\n"
        "v_cndmask_b32_dpp v9, v4, v5, vcc quad_perm:[1,0,3,2] row_mask:0xf bank_mask:0xf\n"
        "v_cndmask_b32_dpp v11, v6, v7, vcc quad_perm:[1,0,3,2] row_mask:0xf bank_mask:0xf\n"
        "s_mov_b64 vcc, s[6:7]\n"
        "s_nop 0\n"
        "v_cndmask_b32_dpp v28, v10, v8, vcc quad_perm:[2,3,0,1] row_mask:0xf bank_mask:0xf\n"
        "v_cndmask_b32_dpp v29, v11, v9, vcc quad_perm:[2,3,0,1] row_mask:0xf bank_mask:0xf\n"
        "s_not_b64 vcc, vcc\n"
        "s_nop 0\n"
        "v_cndmask_b32_dpp v30, v8, v10, vcc quad_perm:[2,3,0,1] row_mask:0xf bank_mask:0xf\n"
        "v_cndmask_b32_dpp v31, v9, v11, vcc quad_perm:[2,3,0,1] row_mask:0xf bank_mask:0xf\n"
        "buffer_store_dwordx4 v[28:31], v103, s[24:27], s51 offen sc1\n"
        "s_nop 1\n"
        "s_barrier\n"
        "s_mov_b32 m0, 106496\n"
        "s_add_u32 s86, s57, 256\n"
        "buffer_load_dwordx4 v101, s[32:35], s86 offen  lds\n"
        "s_mov_b32 m0, 107520\n"
        "s_nop 0\n"
        "buffer_load_dwordx4 v102, s[32:35], s86 offen  lds\n"
        "s_mov_b32 m0, 108544\n"
        "s_add_u32 s86, s57, 33024\n"
        "buffer_load_dwordx4 v101, s[32:35], s86 offen  lds\n"
        "s_mov_b32 m0, 109568\n"
        "s_nop 0\n"
        "buffer_load_dwordx4 v102, s[32:35], s86 offen  lds\n"
        "s_waitcnt vmcnt(4)\n"
        "v_mov_b32 v36, s64\n"
        "v_mov_b32 v37, s65\n"
        "s_lshl_b32 s74, s54, 3\n"
        "v_mov_b32 v38, s74\n"
        "s_mov_b64 exec, 1\n"
        "buffer_store_dwordx2 v[36:37], v38, s[28:31], 0 offen sc1\n"
        "s_mov_b64 exec, -1\n"
        "s_barrier\n"
        "s_mov_b32 m0, 122880\n"
        "s_add_u32 s86, s57, 384\n"
        "buffer_load_dwordx4 v101, s[32:35], s86 offen  lds\n"
        "s_mov_b32 m0, 123904\n"
        "s_nop 0\n"
        "buffer_load_dwordx4 v102, s[32:35], s86 offen  lds\n"
        "s_mov_b32 m0, 124928\n"
        "s_add_u32 s86, s57, 33152\n"
        "buffer_load_dwordx4 v101, s[32:35], s86 offen  lds\n"
        "s_mov_b32 m0, 125952\n"
        "s_nop 0\n"
        "buffer_load_dwordx4 v102, s[32:35], s86 offen  lds\n"
        "s_waitcnt vmcnt(5)\n"
        "s_barrier\n"
        "buffer_load_dwordx2 v[106:107], v104, s[28:31], 0 offen offset:288 sc1\n"
        "buffer_load_dwordx2 v[108:109], v104, s[28:31], 0 offen offset:352 sc1\n"
        "s_mov_b32 m0, 73728\n"
        "s_add_u32 s86, s57, 512\n"
        "buffer_load_dwordx4 v101, s[32:35], s86 offen  lds\n"
        "s_mov_b32 m0, 74752\n"
        "s_nop 0\n"
        "buffer_load_dwordx4 v102, s[32:35], s86 offen  lds\n"
        "s_mov_b32 m0, 75776\n"
        "s_add_u32 s86, s57, 33280\n"
        "buffer_load_dwordx4 v101, s[32:35], s86 offen  lds\n"
        "s_mov_b32 m0, 76800\n"
        "s_nop 0\n"
        "buffer_load_dwordx4 v102, s[32:35], s86 offen  lds\n"
        "buffer_load_dwordx4 v[0:3], v100, s[20:23], s52 offen nt\n"
        "s_add_u32 s76, s52, 14336\n"
        "buffer_load_dwordx4 v[4:7], v100, s[20:23], s76 offen nt\n"
        "s_add_u32 s77, s52, 28672\n"
        "buffer_load_dwordx4 v[8:11], v100, s[20:23], s77 offen nt\n"
        "s_add_u32 s74, s52, 43008\n"
        "buffer_load_dwordx4 v[12:15], v100, s[20:23], s74 offen nt\n"
        "s_add_u32 s75, s52, 57344\n"
        "buffer_load_dwordx4 v[16:19], v100, s[20:23], s75 offen nt\n"
        "s_add_u32 s76, s52, 71680\n"
        "buffer_load_dwordx4 v[20:23], v100, s[20:23], s76 offen nt\n"
        "s_add_u32 s77, s52, 86016\n"
        "buffer_load_dwordx4 v[24:27], v100, s[20:23], s77 offen nt\n"
        "s_add_u32 s74, s52, 100352\n"
        "buffer_load_dwordx4 v[28:31], v100, s[20:23], s74 offen nt\n"
        "s_waitcnt vmcnt(14)\n"
        "s_barrier\n"
        "buffer_load_dwordx2 v[110:111], v104, s[28:31], 0 offen offset:416 sc1\n"
        "s_waitcnt vmcnt(14)\n"
        "v_cmp_ne_u32 vcc, s64, v106\n"
        "v_cmp_ne_u32 s[78:79], s65, v107\n"
        "s_or_b64 vcc, vcc, s[78:79]\n"
        "s_cbranch_scc1 Ls89_%=\n"
        "Lk90_%=:\n"
        "s_mov_b32 m0, 135168\n"
        "s_add_u32 s75, s56, 73728\n"
        "buffer_load_dwordx4 v103, s[24:27], s75 offen sc1 lds\n"
        "s_mov_b32 m0, 136192\n"
        "s_add_u32 s75, s75, 2048\n"
        "buffer_load_dwordx4 v103, s[24:27], s75 offen sc1 lds\n"
        "Le91_%=:\n"
        "s_waitcnt vmcnt(15)\n"
        "v_cmp_ne_u32 vcc, s64, v108\n"
        "v_cmp_ne_u32 s[78:79], s65, v109\n"
        "s_or_b64 vcc, vcc, s[78:79]\n"
        "s_cbranch_scc1 Ls92_%=\n"
        "Lk93_%=:\n"
        "s_mov_b32 m0, 143360\n"
        "s_add_u32 s76, s56, 90112\n"
        "buffer_load_dwordx4 v103, s[24:27], s76 offen sc1 lds\n"
        "s_mov_b32 m0, 144384\n"
        "s_add_u32 s76, s76, 2048\n"
        "buffer_load_dwordx4 v103, s[24:27], s76 offen sc1 lds\n"
        "Le94_%=:\n"
        "s_mov_b32 m0, 90112\n"
        "s_add_u32 s86, s57, 640\n"
        "buffer_load_dwordx4 v101, s[32:35], s86 offen  lds\n"
        "s_mov_b32 m0, 91136\n"
        "s_nop 0\n"
        "buffer_load_dwordx4 v102, s[32:35], s86 offen  lds\n"
        "s_mov_b32 m0, 92160\n"
        "s_add_u32 s86, s57, 33408\n"
        "buffer_load_dwordx4 v101, s[32:35], s86 offen  lds\n"
        "s_mov_b32 m0, 93184\n"
        "s_nop 0\n"
        "buffer_load_dwordx4 v102, s[32:35], s86 offen  lds\n"
        "s_waitcnt vmcnt(17)\n"
        "s_waitcnt vmcnt(6)\n"
        "s_waitcnt vmcnt(4)\n"
        "s_barrier\n"
        "buffer_load_dwordx2 v[112:113], v104, s[28:31], 0 offen offset:480 sc1\n"
        "v_cmp_ne_u32 vcc, s64, v110\n"
        "v_cmp_ne_u32 s[78:79], s65, v111\n"
        "s_or_b64 vcc, vcc, s[78:79]\n"
        "s_cbranch_scc1 Ls95_%=\n"
        "Lk96_%=:\n"
        "s_mov_b32 m0, 151552\n"
        "s_add_u32 s77, s56, 106496\n"
        "buffer_load_dwordx4 v103, s[24:27], s77 offen sc1 lds\n"
        "s_mov_b32 m0, 152576\n"
        "s_add_u32 s77, s77, 2048\n"
        "buffer_load_dwordx4 v103, s[24:27], s77 offen sc1 lds\n"
        "Le97_%=:\n"
        "s_mov_b32 m0, 106496\n"
        "s_add_u32 s86, s57, 768\n"
        "buffer_load_dwordx4 v101, s[32:35], s86 offen  lds\n"
        "s_mov_b32 m0, 107520\n"
        "s_nop 0\n"
        "buffer_load_dwordx4 v102, s[32:35], s86 offen  lds\n"
        "s_mov_b32 m0, 108544\n"
        "s_add_u32 s86, s57, 33536\n"
        "buffer_load_dwordx4 v101, s[32:35], s86 offen  lds\n"
        "s_mov_b32 m0, 109568\n"
        "s_nop 0\n"
        "buffer_load_dwordx4 v102, s[32:35], s86 offen  lds\n"
        "v_lshl_or_b32 v0, v4, 4, v0\n"
        "v_lshl_or_b32 v1, v5, 4, v1\n"
        "v_lshl_or_b32 v2, v6, 4, v2\n"
        "v_lshl_or_b32 v3, v7, 4, v3\n"
        "v_lshl_or_b32 v8, v12, 4, v8\n"
        "v_lshl_or_b32 v9, v13, 4, v9\n"
        "v_lshl_or_b32 v10, v14, 4, v10\n"
        "v_lshl_or_b32 v11, v15, 4, v11\n"
        "v_lshl_or_b32 v0, v8, 8, v0\n"
        "v_lshl_or_b32 v1, v9, 8, v1\n"
        "v_lshl_or_b32 v2, v10, 8, v2\n"
        "v_lshl_or_b32 v3, v11, 8, v3\n"
        "v_lshl_or_b32 v16, v20, 4, v16\n"
        "v_lshl_or_b32 v17, v21, 4, v17\n"
        "v_lshl_or_b32 v18, v22, 4, v18\n"
        "v_lshl_or_b32 v19, v23, 4, v19\n"
        "v_lshl_or_b32 v24, v28, 4, v24\n"
        "v_lshl_or_b32 v25, v29, 4, v25\n"
        "v_lshl_or_b32 v26, v30, 4, v26\n"
        "v_lshl_or_b32 v27, v31, 4, v27\n"
        "v_lshl_or_b32 v16, v24, 8, v16\n"
        "v_lshl_or_b32 v17, v25, 8, v17\n"
        "v_lshl_or_b32 v18, v26, 8, v18\n"
        "v_lshl_or_b32 v19, v27, 8, v19\n"
        "v_lshl_or_b32 v4, v16, 16, v0\n"
        "v_lshl_or_b32 v5, v17, 16, v1\n"
        "v_lshl_or_b32 v6, v18, 16, v2\n"
        "v_lshl_or_b32 v7, v19, 16, v3\n"
        "s_mov_b64 vcc, s[4:5]\n"
        "s_nop 0\n"
        "v_cndmask_b32_dpp v8, v5, v4, vcc quad_perm:[1,0,3,2] row_mask:0xf bank_mask:0xf\n"
        "v_cndmask_b32_dpp v10, v7, v6, vcc quad_perm:[1,0,3,2] row_mask:0xf bank_mask:0xf\n"
        "s_not_b64 vcc, vcc\n"
        "s_nop 0\n"
        "v_cndmask_b32_dpp v9, v4, v5, vcc quad_perm:[1,0,3,2] row_mask:0xf bank_mask:0xf\n"
        "v_cndmask_b32_dpp v11, v6, v7, vcc quad_perm:[1,0,3,2] row_mask:0xf bank_mask:0xf\n"
        "s_mov_b64 vcc, s[6:7]\n"
        "s_nop 0\n"
        "v_cndmask_b32_dpp v28, v10, v8, vcc quad_perm:[2,3,0,1] row_mask:0xf bank_mask:0xf\n"
        "v_cndmask_b32_dpp v29, v11, v9, vcc quad_perm:[2,3,0,1] row_mask:0xf bank_mask:0xf\n"
        "s_not_b64 vcc, vcc\n"
        "s_nop 0\n"
        "v_cndmask_b32_dpp v30, v8, v10, vcc quad_perm:[2,3,0,1] row_mask:0xf bank_mask:0xf\n"
        "v_cndmask_b32_dpp v31, v9, v11, vcc quad_perm:[2,3,0,1] row_mask:0xf bank_mask:0xf\n"
        "buffer_store_dwordx4 v[28:31], v103, s[24:27], s53 offen sc1\n"
        "s_nop 1\n"
        "s_waitcnt vmcnt(8)\n"
        "s_waitcnt vmcnt(5)\n"
        "s_barrier\n"
        "buffer_load_dwordx2 v[106:107], v104, s[28:31], 0 offen offset:544 sc1\n"
        "v_cmp_ne_u32 vcc, s64, v112\n"
        "v_cmp_ne_u32 s[78:79], s65, v113\n"
        "s_or_b64 vcc, vcc, s[78:79]\n"
        "s_cbranch_scc1 Ls98_%=\n"
        "Lk99_%=:\n"
        "s_mov_b32 m0, 159744\n"
        "s_add_u32 s74, s56, 122880\n"
        "buffer_load_dwordx4 v103, s[24:27], s74 offen sc1 lds\n"
        "s_mov_b32 m0, 160768\n"
        "s_add_u32 s74, s74, 2048\n"
        "buffer_load_dwordx4 v103, s[24:27], s74 offen sc1 lds\n"
        "Le100_%=:\n"
        "s_mov_b32 m0, 122880\n"
        "s_add_u32 s86, s57, 896\n"
        "buffer_load_dwordx4 v101, s[32:35], s86 offen  lds\n"
        "s_mov_b32 m0, 123904\n"
        "s_nop 0\n"
        "buffer_load_dwordx4 v102, s[32:35], s86 offen  lds\n"
        "s_mov_b32 m0, 124928\n"
        "s_add_u32 s86, s57, 33664\n"
        "buffer_load_dwordx4 v101, s[32:35], s86 offen  lds\n"
        "s_mov_b32 m0, 125952\n"
        "s_nop 0\n"
        "buffer_load_dwordx4 v102, s[32:35], s86 offen  lds\n"
        "s_waitcnt vmcnt(7)\n"
        "v_mov_b32 v36, s64\n"
        "v_mov_b32 v37, s65\n"
        "s_lshl_b32 s74, s55, 3\n"
        "v_mov_b32 v38, s74\n"
        "s_mov_b64 exec, 1\n"
        "buffer_store_dwordx2 v[36:37], v38, s[28:31], 0 offen sc1\n"
        "s_mov_b64 exec, -1\n"
        "s_waitcnt vmcnt(5)\n"
        "s_barrier\n"
        "buffer_load_dwordx2 v[108:109], v104, s[28:31], 0 offen offset:608 sc1\n"
        "buffer_load_dwordx2 v[110:111], v104, s[28:31], 0 offen offset:672 sc1\n"
        "buffer_load_dwordx2 v[112:113], v104, s[28:31], 0 offen offset:736 sc1\n"
        "v_cmp_ne_u32 vcc, s64, v106\n"
        "v_cmp_ne_u32 s[78:79], s65, v107\n"
        "s_or_b64 vcc, vcc, s[78:79]\n"
        "s_cbranch_scc1 Ls101_%=\n"
        "Lk102_%=:\n"
        "s_mov_b32 m0, 135168\n"
        "s_add_u32 s75, s56, 139264\n"
        "buffer_load_dwordx4 v103, s[24:27], s75 offen sc1 lds\n"
        "s_mov_b32 m0, 136192\n"
        "s_add_u32 s75, s75, 2048\n"
        "buffer_load_dwordx4 v103, s[24:27], s75 offen sc1 lds\n"
        "Le103_%=:\n"
        "s_mov_b32 m0, 73728\n"
        "s_add_u32 s86, s57, 1024\n"
        "buffer_load_dwordx4 v101, s[32:35], s86 offen  lds\n"
        "s_mov_b32 m0, 74752\n"
        "s_nop 0\n"
        "buffer_load_dwordx4 v102, s[32:35], s86 offen  lds\n"
        "s_mov_b32 m0, 75776\n"
        "s_add_u32 s86, s57, 33792\n"
        "buffer_load_dwordx4 v101, s[32:35], s86 offen  lds\n"
        "s_mov_b32 m0, 76800\n"
        "s_nop 0\n"
        "buffer_load_dwordx4 v102, s[32:35], s86 offen  lds\n"
        "s_waitcnt vmcnt(10)\n"
        "s_waitcnt vmcnt(4)\n"
        "s_barrier\n"
        "buffer_load_dwordx2 v[106:107], v104, s[28:31], 0 offen offset:800 sc1\n"
        "v_cmp_ne_u32 vcc, s64, v108\n"
        "v_cmp_ne_u32 s[78:79], s65, v109\n"
        "s_or_b64 vcc, vcc, s[78:79]\n"
        "s_cbranch_scc1 Ls104_%=\n"
        "Lk105_%=:\n"
        "s_mov_b32 m0, 143360\n"
        "s_add_u32 s76, s56, 155648\n"
        "buffer_load_dwordx4 v103, s[24:27], s76 offen sc1 lds\n"
        "s_mov_b32 m0, 144384\n"
        "s_add_u32 s76, s76, 2048\n"
        "buffer_load_dwordx4 v103, s[24:27], s76 offen sc1 lds\n"
        "Le106_%=:\n"
        "v_cmp_ne_u32 vcc, s64, v110\n"
        "v_cmp_ne_u32 s[78:79], s65, v111\n"
        "s_or_b64 vcc, vcc, s[78:79]\n"
        "s_cbranch_scc1 Ls107_%=\n"
        "Lk108_%=:\n"
        "s_mov_b32 m0, 151552\n"
        "s_add_u32 s77, s56, 172032\n"
        "buffer_load_dwordx4 v103, s[24:27], s77 offen sc1 lds\n"
        "s_mov_b32 m0, 152576\n"
        "s_add_u32 s77, s77, 2048\n"
        "buffer_load_dwordx4 v103, s[24:27], s77 offen sc1 lds\n"
        "Le109_%=:\n"
        "v_cmp_ne_u32 vcc, s64, v112\n"
        "v_cmp_ne_u32 s[78:79], s65, v113\n"
        "s_or_b64 vcc, vcc, s[78:79]\n"
        "s_cbranch_scc1 Ls110_%=\n"
        "Lk111_%=:\n"
        "s_mov_b32 m0, 159744\n"
        "s_add_u32 s74, s56, 188416\n"
        "buffer_load_dwordx4 v103, s[24:27], s74 offen sc1 lds\n"
        "s_mov_b32 m0, 160768\n"
        "s_add_u32 s74, s74, 2048\n"
        "buffer_load_dwordx4 v103, s[24:27], s74 offen sc1 lds\n"
        "Le112_%=:\n"
        "s_mov_b32 m0, 90112\n"
        "s_add_u32 s86, s57, 1152\n"
        "buffer_load_dwordx4 v101, s[32:35], s86 offen  lds\n"
        "s_mov_b32 m0, 91136\n"
        "s_nop 0\n"
        "buffer_load_dwordx4 v102, s[32:35], s86 offen  lds\n"
        "s_mov_b32 m0, 92160\n"
        "s_add_u32 s86, s57, 33920\n"
        "buffer_load_dwordx4 v101, s[32:35], s86 offen  lds\n"
        "s_mov_b32 m0, 93184\n"
        "s_nop 0\n"
        "buffer_load_dwordx4 v102, s[32:35], s86 offen  lds\n"
        "s_waitcnt vmcnt(11)\n"
        "s_waitcnt vmcnt(8)\n"
        "s_barrier\n"
        "buffer_load_dwordx2 v[108:109], v104, s[28:31], 0 offen offset:864 sc1\n"
        "v_cmp_ne_u32 vcc, s64, v106\n"
        "v_cmp_ne_u32 s[78:79], s65, v107\n"
        "s_or_b64 vcc, vcc, s[78:79]\n"
        "s_cbranch_scc1 Ls113_%=\n"
        "Lk114_%=:\n"
        "s_mov_b32 m0, 135168\n"
        "s_add_u32 s75, s56, 204800\n"
        "buffer_load_dwordx4 v103, s[24:27], s75 offen sc1 lds\n"
        "s_mov_b32 m0, 136192\n"
        "s_add_u32 s75, s75, 2048\n"
        "buffer_load_dwordx4 v103, s[24:27], s75 offen sc1 lds\n"
        "Le115_%=:\n"
        "s_mov_b32 m0, 106496\n"
        "s_add_u32 s86, s57, 1280\n"
        "buffer_load_dwordx4 v101, s[32:35], s86 offen  lds\n"
        "s_mov_b32 m0, 107520\n"
        "s_nop 0\n"
        "buffer_load_dwordx4 v102, s[32:35], s86 offen  lds\n"
        "s_mov_b32 m0, 108544\n"
        "s_add_u32 s86, s57, 34048\n"
        "buffer_load_dwordx4 v101, s[32:35], s86 offen  lds\n"
        "s_mov_b32 m0, 109568\n"
        "s_nop 0\n"
        "buffer_load_dwordx4 v102, s[32:35], s86 offen  lds\n"
        "s_waitcnt vmcnt(7)\n"
        "s_barrier\n"
        "buffer_load_dwordx2 v[110:111], v104, s[28:31], 0 offen offset:928 sc1\n"
        "s_waitcnt vmcnt(7)\n"
        "v_cmp_ne_u32 vcc, s64, v108\n"
        "v_cmp_ne_u32 s[78:79], s65, v109\n"
        "s_or_b64 vcc, vcc, s[78:79]\n"
        "s_cbranch_scc1 Ls116_%=\n"
        "Lk117_%=:\n"
        "s_mov_b32 m0, 143360\n"
        "s_add_u32 s76, s56, 221184\n"
        "buffer_load_dwordx4 v103, s[24:27], s76 offen sc1 lds\n"
        "s_mov_b32 m0, 144384\n"
        "s_add_u32 s76, s76, 2048\n"
        "buffer_load_dwordx4 v103, s[24:27], s76 offen sc1 lds\n"
        "Le118_%=:\n"
        "s_mov_b32 m0, 122880\n"
        "s_add_u32 s86, s57, 1408\n"
        "buffer_load_dwordx4 v101, s[32:35], s86 offen  lds\n"
        "s_mov_b32 m0, 123904\n"
        "s_nop 0\n"
        "buffer_load_dwordx4 v102, s[32:35], s86 offen  lds\n"
        "s_mov_b32 m0, 124928\n"
        "s_add_u32 s86, s57, 34176\n"
        "buffer_load_dwordx4 v101, s[32:35], s86 offen  lds\n"
        "s_mov_b32 m0, 125952\n"
        "s_nop 0\n"
        "buffer_load_dwordx4 v102, s[32:35], s86 offen  lds\n"
        "s_mov_b32 s36, s16\n"
        "s_and_b32 s37, s17, 0xffff\n"
        "s_mov_b32 s38, 14680064\n"
        "s_mov_b32 s39, 0x20000\n"
        "s_mul_i32 s59, s46, 917504\n"
        "s_mul_i32 s74, s45, 448\n"
        "s_add_u32 s59, s59, s74\n"
        "v_add_u32 v34, 0, v32\n"
        "v_mul_u32_u24 v35, 2341, v34\n"
        "v_lshrrev_b32 v35, 16, v35\n"
        "v_mul_u32_u24 v36, 28, v35\n"
        "v_sub_u32 v36, v34, v36\n"
        "v_lshlrev_b32 v36, 4, v36\n"
        "v_min_u32 v37, 7, v35\n"
        "v_cmp_le_u32 vcc, 224, v36\n"
        "v_subrev_u32 v38, 224, v36\n"
        "v_mov_b32 v39, 0\n"
        "v_mov_b32 v40, 2\n"
        "v_cndmask_b32 v38, v36, v38, vcc\n"
        "v_cndmask_b32 v39, v39, v40, vcc\n"
        "v_mul_u32_u24 v64, 3584, v39\n"
        "v_mul_u32_u24 v40, 112, v37\n"
        "v_add_u32 v64, v40, v64\n"
        "v_add_u32 v64, v38, v64\n"
        "v_add_u32 v64, 5888, v64\n"
        "v_add_u32 v40, 1696, v64\n"
        "v_cmp_le_u32 vcc, 96, v38\n"
        "s_nop 1\n"
        "v_cndmask_b32 v64, v64, v40, vcc\n"
        "v_lshlrev_b32 v39, 9, v39\n"
        "v_lshl_add_u32 v39, v37, 5, v39\n"
        "v_xor_b32 v40, 0, v38\n"
        "v_add_u32 v39, v39, v40\n"
        "v_add_u32 v39, 18752, v39\n"
        "v_cmp_le_u32 vcc, 192, v38\n"
        "s_nop 1\n"
        "v_cndmask_b32 v64, v64, v39, vcc\n"
        "v_mul_u32_u24 v68, 7168, v35\n"
        "v_add_u32 v68, v68, v36\n"
        "v_add_u32 v68, 458752, v68\n"
        "s_waitcnt vmcnt(7)\n"
        "s_barrier\n"
        "buffer_load_dwordx2 v[112:113], v104, s[28:31], 0 offen offset:992 sc1\n"
        "s_waitcnt vmcnt(7)\n"
        "v_cmp_ne_u32 vcc, s64, v110\n"
        "v_cmp_ne_u32 s[78:79], s65, v111\n"
        "s_or_b64 vcc, vcc, s[78:79]\n"
        "s_cbranch_scc1 Ls119_%=\n"
        "Lk120_%=:\n"
        "s_mov_b32 m0, 151552\n"
        "s_add_u32 s77, s56, 237568\n"
        "buffer_load_dwordx4 v103, s[24:27], s77 offen sc1 lds\n"
        "s_mov_b32 m0, 152576\n"
        "s_add_u32 s77, s77, 2048\n"
        "buffer_load_dwordx4 v103, s[24:27], s77 offen sc1 lds\n"
        "Le121_%=:\n"
        "s_mov_b32 m0, 73728\n"
        "s_add_u32 s86, s57, 1536\n"
        "buffer_load_dwordx4 v101, s[32:35], s86 offen  lds\n"
        "s_mov_b32 m0, 74752\n"
        "s_nop 0\n"
        "buffer_load_dwordx4 v102, s[32:35], s86 offen  lds\n"
        "s_mov_b32 m0, 75776\n"
        "s_add_u32 s86, s57, 34304\n"
        "buffer_load_dwordx4 v101, s[32:35], s86 offen  lds\n"
        "s_mov_b32 m0, 76800\n"
        "s_nop 0\n"
        "buffer_load_dwordx4 v102, s[32:35], s86 offen  lds\n"
        "v_add_u32 v34, 64, v32\n"
        "v_mul_u32_u24 v35, 2341, v34\n"
        "v_lshrrev_b32 v35, 16, v35\n"
        "v_mul_u32_u24 v36, 28, v35\n"
        "v_sub_u32 v36, v34, v36\n"
        "v_lshlrev_b32 v36, 4, v36\n"
        "v_min_u32 v37, 7, v35\n"
        "v_cmp_le_u32 vcc, 224, v36\n"
        "v_subrev_u32 v38, 224, v36\n"
        "v_mov_b32 v39, 0\n"
        "v_mov_b32 v40, 2\n"
        "v_cndmask_b32 v38, v36, v38, vcc\n"
        "v_cndmask_b32 v39, v39, v40, vcc\n"
        "v_mul_u32_u24 v65, 3584, v39\n"
        "v_mul_u32_u24 v40, 112, v37\n"
        "v_add_u32 v65, v40, v65\n"
        "v_add_u32 v65, v38, v65\n"
        "v_add_u32 v65, 5888, v65\n"
        "v_add_u32 v40, 1696, v65\n"
        "v_cmp_le_u32 vcc, 96, v38\n"
        "s_nop 1\n"
        "v_cndmask_b32 v65, v65, v40, vcc\n"
        "v_lshlrev_b32 v39, 9, v39\n"
        "v_lshl_add_u32 v39, v37, 5, v39\n"
        "v_xor_b32 v40, 0, v38\n"
        "v_add_u32 v39, v39, v40\n"
        "v_add_u32 v39, 18752, v39\n"
        "v_cmp_le_u32 vcc, 192, v38\n"
        "s_nop 1\n"
        "v_cndmask_b32 v65, v65, v39, vcc\n"
        "v_mul_u32_u24 v69, 7168, v35\n"
        "v_add_u32 v69, v69, v36\n"
        "v_add_u32 v69, 458752, v69\n"
        "s_waitcnt vmcnt(7)\n"
        "s_barrier\n"
        "s_waitcnt vmcnt(6)\n"
        "v_cmp_ne_u32 vcc, s64, v112\n"
        "v_cmp_ne_u32 s[78:79], s65, v113\n"
        "s_or_b64 vcc, vcc, s[78:79]\n"
        "s_cbranch_scc1 Ls122_%=\n"
        "Lk123_%=:\n"
        "s_mov_b32 m0, 159744\n"
        "s_add_u32 s74, s56, 253952\n"
        "buffer_load_dwordx4 v103, s[24:27], s74 offen sc1 lds\n"
        "s_mov_b32 m0, 160768\n"
        "s_add_u32 s74, s74, 2048\n"
        "buffer_load_dwordx4 v103, s[24:27], s74 offen sc1 lds\n"
        "Le124_%=:\n"
        "s_mov_b32 m0, 90112\n"
        "s_add_u32 s86, s57, 1664\n"
        "buffer_load_dwordx4 v101, s[32:35], s86 offen  lds\n"
        "s_mov_b32 m0, 91136\n"
        "s_nop 0\n"
        "buffer_load_dwordx4 v102, s[32:35], s86 offen  lds\n"
        "s_mov_b32 m0, 92160\n"
        "s_add_u32 s86, s57, 34432\n"
        "buffer_load_dwordx4 v101, s[32:35], s86 offen  lds\n"
        "s_mov_b32 m0, 93184\n"
        "s_nop 0\n"
        "buffer_load_dwordx4 v102, s[32:35], s86 offen  lds\n"
        "v_add_u32 v34, 128, v32\n"
        "v_mul_u32_u24 v35, 2341, v34\n"
        "v_lshrrev_b32 v35, 16, v35\n"
        "v_mul_u32_u24 v36, 28, v35\n"
        "v_sub_u32 v36, v34, v36\n"
        "v_lshlrev_b32 v36, 4, v36\n"
        "v_min_u32 v37, 7, v35\n"
        "v_cmp_le_u32 vcc, 224, v36\n"
        "v_subrev_u32 v38, 224, v36\n"
        "v_mov_b32 v39, 0\n"
        "v_mov_b32 v40, 2\n"
        "v_cndmask_b32 v38, v36, v38, vcc\n"
        "v_cndmask_b32 v39, v39, v40, vcc\n"
        "v_mul_u32_u24 v66, 3584, v39\n"
        "v_mul_u32_u24 v40, 112, v37\n"
        "v_add_u32 v66, v40, v66\n"
        "v_add_u32 v66, v38, v66\n"
        "v_add_u32 v66, 5888, v66\n"
        "v_add_u32 v40, 1696, v66\n"
        "v_cmp_le_u32 vcc, 96, v38\n"
        "s_nop 1\n"
        "v_cndmask_b32 v66, v66, v40, vcc\n"
        "v_lshlrev_b32 v39, 9, v39\n"
        "v_lshl_add_u32 v39, v37, 5, v39\n"
        "v_xor_b32 v40, 0, v38\n"
        "v_add_u32 v39, v39, v40\n"
        "v_add_u32 v39, 18752, v39\n"
        "v_cmp_le_u32 vcc, 192, v38\n"
        "s_nop 1\n"
        "v_cndmask_b32 v66, v66, v39, vcc\n"
        "v_mul_u32_u24 v70, 7168, v35\n"
        "v_add_u32 v70, v70, v36\n"
        "v_add_u32 v70, 458752, v70\n"
        "s_waitcnt vmcnt(6)\n"
        "s_barrier\n"
        "s_mov_b32 m0, 106496\n"
        "s_add_u32 s86, s57, 1792\n"
        "buffer_load_dwordx4 v101, s[32:35], s86 offen  lds\n"
        "s_mov_b32 m0, 107520\n"
        "s_nop 0\n"
        "buffer_load_dwordx4 v102, s[32:35], s86 offen  lds\n"
        "s_mov_b32 m0, 108544\n"
        "s_add_u32 s86, s57, 34560\n"
        "buffer_load_dwordx4 v101, s[32:35], s86 offen  lds\n"
        "s_mov_b32 m0, 109568\n"
        "s_nop 0\n"
        "buffer_load_dwordx4 v102, s[32:35], s86 offen  lds\n"
        "s_mov_b32 m0, 122880\n"
        "s_add_u32 s86, s57, 1920\n"
        "buffer_load_dwordx4 v101, s[32:35], s86 offen  lds\n"
        "s_mov_b32 m0, 123904\n"
        "s_nop 0\n"
        "buffer_load_dwordx4 v102, s[32:35], s86 offen  lds\n"
        "s_mov_b32 m0, 124928\n"
        "s_add_u32 s86, s57, 34688\n"
        "buffer_load_dwordx4 v101, s[32:35], s86 offen  lds\n"
        "s_mov_b32 m0, 125952\n"
        "s_nop 0\n"
        "buffer_load_dwordx4 v102, s[32:35], s86 offen  lds\n"
        "v_add_u32 v34, 192, v32\n"
        "v_mul_u32_u24 v35, 2341, v34\n"
        "v_lshrrev_b32 v35, 16, v35\n"
        "v_mul_u32_u24 v36, 28, v35\n"
        "v_sub_u32 v36, v34, v36\n"
        "v_lshlrev_b32 v36, 4, v36\n"
        "v_min_u32 v37, 7, v35\n"
        "v_cmp_le_u32 vcc, 224, v36\n"
        "v_subrev_u32 v38, 224, v36\n"
        "v_mov_b32 v39, 0\n"
        "v_mov_b32 v40, 2\n"
        "v_cndmask_b32 v38, v36, v38, vcc\n"
        "v_cndmask_b32 v39, v39, v40, vcc\n"
        "v_mul_u32_u24 v67, 3584, v39\n"
        "v_mul_u32_u24 v40, 112, v37\n"
        "v_add_u32 v67, v40, v67\n"
        "v_add_u32 v67, v38, v67\n"
        "v_add_u32 v67, 5888, v67\n"
        "v_add_u32 v40, 1696, v67\n"
        "v_cmp_le_u32 vcc, 96, v38\n"
        "s_nop 1\n"
        "v_cndmask_b32 v67, v67, v40, vcc\n"
        "v_lshlrev_b32 v39, 9, v39\n"
        "v_lshl_add_u32 v39, v37, 5, v39\n"
        "v_xor_b32 v40, 0, v38\n"
        "v_add_u32 v39, v39, v40\n"
        "v_add_u32 v39, 18752, v39\n"
        "v_cmp_le_u32 vcc, 192, v38\n"
        "s_nop 1\n"
        "v_cndmask_b32 v67, v67, v39, vcc\n"
        "v_mul_u32_u24 v71, 7168, v35\n"
        "v_add_u32 v71, v71, v36\n"
        "v_add_u32 v71, 458752, v71\n"
        "v_mov_b32 v37, 0x7ffffff0\n"
        "v_cmp_gt_u32 vcc, 32, v32\n"
        "s_nop 1\n"
        "v_cndmask_b32 v71, v37, v71, vcc\n"
        "s_waitcnt vmcnt(8)\n"
        "s_barrier\n"
        "s_waitcnt vmcnt(4)\n"
        "s_waitcnt vmcnt(0)\n"
        "s_barrier\n"
        "s_barrier\n"
        "ds_read_b128 v[0:3], v64 offset:61440\n"
        "ds_read_b128 v[4:7], v65 offset:61440\n"
        "ds_read_b128 v[8:11], v66 offset:61440\n"
        "ds_read_b128 v[12:15], v67 offset:61440\n"
        "s_add_u32 s75, s59, 0\n"
        "s_waitcnt lgkmcnt(3)\n"
        "buffer_store_dwordx4 v[0:3], v68, s[36:39], s75 offen sc1 nt\n"
        "s_waitcnt lgkmcnt(2)\n"
        "buffer_store_dwordx4 v[4:7], v69, s[36:39], s75 offen sc1 nt\n"
        "s_waitcnt lgkmcnt(1)\n"
        "buffer_store_dwordx4 v[8:11], v70, s[36:39], s75 offen sc1 nt\n"
        "s_waitcnt lgkmcnt(0)\n"
        "buffer_store_dwordx4 v[12:15], v71, s[36:39], s75 offen sc1 nt\n"
        "s_nop 1\n"
        "s_barrier\n"
        "ds_read_b128 v[16:19], v64 offset:0\n"
        "ds_read_b128 v[20:23], v65 offset:0\n"
        "ds_read_b128 v[24:27], v66 offset:0\n"
        "ds_read_b128 v[28:31], v67 offset:0\n"
        "s_add_u32 s76, s59, 114688\n"
        "s_waitcnt lgkmcnt(3)\n"
        "buffer_store_dwordx4 v[16:19], v68, s[36:39], s76 offen sc1 nt\n"
        "s_waitcnt lgkmcnt(2)\n"
        "buffer_store_dwordx4 v[20:23], v69, s[36:39], s76 offen sc1 nt\n"
        "s_waitcnt lgkmcnt(1)\n"
        "buffer_store_dwordx4 v[24:27], v70, s[36:39], s76 offen sc1 nt\n"
        "s_waitcnt lgkmcnt(0)\n"
        "buffer_store_dwordx4 v[28:31], v71, s[36:39], s76 offen sc1 nt\n"
        "s_nop 1\n"
        "s_barrier\n"
        "ds_read_b128 v[0:3], v64 offset:16384\n"
        "ds_read_b128 v[4:7], v65 offset:16384\n"
        "ds_read_b128 v[8:11], v66 offset:16384\n"
        "ds_read_b128 v[12:15], v67 offset:16384\n"
        "s_add_u32 s77, s59, 229376\n"
        "s_waitcnt lgkmcnt(3)\n"
        "buffer_store_dwordx4 v[0:3], v68, s[36:39], s77 offen sc1 nt\n"
        "s_waitcnt lgkmcnt(2)\n"
        "buffer_store_dwordx4 v[4:7], v69, s[36:39], s77 offen sc1 nt\n"
        "s_waitcnt lgkmcnt(1)\n"
        "buffer_store_dwordx4 v[8:11], v70, s[36:39], s77 offen sc1 nt\n"
        "s_waitcnt lgkmcnt(0)\n"
        "buffer_store_dwordx4 v[12:15], v71, s[36:39], s77 offen sc1 nt\n"
        "s_nop 1\n"
        "s_barrier\n"
        "ds_read_b128 v[16:19], v64 offset:32768\n"
        "ds_read_b128 v[20:23], v65 offset:32768\n"
        "ds_read_b128 v[24:27], v66 offset:32768\n"
        "ds_read_b128 v[28:31], v67 offset:32768\n"
        "s_add_u32 s74, s59, 344064\n"
        "s_waitcnt lgkmcnt(3)\n"
        "buffer_store_dwordx4 v[16:19], v68, s[36:39], s74 offen sc1 nt\n"
        "s_waitcnt lgkmcnt(2)\n"
        "buffer_store_dwordx4 v[20:23], v69, s[36:39], s74 offen sc1 nt\n"
        "s_waitcnt lgkmcnt(1)\n"
        "buffer_store_dwordx4 v[24:27], v70, s[36:39], s74 offen sc1 nt\n"
        "s_waitcnt lgkmcnt(0)\n"
        "buffer_store_dwordx4 v[28:31], v71, s[36:39], s74 offen sc1 nt\n"
        "s_nop 1\n"
        "s_endpgm\n"
        "Lhm3_%=:\n"
        "s_waitcnt vmcnt(8)\n"
        "v_lshl_or_b32 v0, v4, 4, v0\n"
        "v_lshl_or_b32 v1, v5, 4, v1\n"
        "v_lshl_or_b32 v2, v6, 4, v2\n"
        "v_lshl_or_b32 v3, v7, 4, v3\n"
        "v_lshl_or_b32 v8, v12, 4, v8\n"
        "v_lshl_or_b32 v9, v13, 4, v9\n"
        "v_lshl_or_b32 v10, v14, 4, v10\n"
        "v_lshl_or_b32 v11, v15, 4, v11\n"
        "v_lshl_or_b32 v0, v8, 8, v0\n"
        "v_lshl_or_b32 v1, v9, 8, v1\n"
        "v_lshl_or_b32 v2, v10, 8, v2\n"
        "v_lshl_or_b32 v3, v11, 8, v3\n"
        "s_waitcnt vmcnt(4)\n"
        "v_lshl_or_b32 v16, v20, 4, v16\n"
        "v_lshl_or_b32 v17, v21, 4, v17\n"
        "v_lshl_or_b32 v18, v22, 4, v18\n"
        "v_lshl_or_b32 v19, v23, 4, v19\n"
        "v_lshl_or_b32 v24, v28, 4, v24\n"
        "v_lshl_or_b32 v25, v29, 4, v25\n"
        "v_lshl_or_b32 v26, v30, 4, v26\n"
        "v_lshl_or_b32 v27, v31, 4, v27\n"
        "v_lshl_or_b32 v16, v24, 8, v16\n"
        "v_lshl_or_b32 v17, v25, 8, v17\n"
        "v_lshl_or_b32 v18, v26, 8, v18\n"
        "v_lshl_or_b32 v19, v27, 8, v19\n"
        "v_lshl_or_b32 v4, v16, 16, v0\n"
        "v_lshl_or_b32 v5, v17, 16, v1\n"
        "v_lshl_or_b32 v6, v18, 16, v2\n"
        "v_lshl_or_b32 v7, v19, 16, v3\n"
        "s_mov_b64 vcc, s[4:5]\n"
        "s_nop 0\n"
        "v_cndmask_b32_dpp v8, v5, v4, vcc quad_perm:[1,0,3,2] row_mask:0xf bank_mask:0xf\n"
        "v_cndmask_b32_dpp v10, v7, v6, vcc quad_perm:[1,0,3,2] row_mask:0xf bank_mask:0xf\n"
        "s_not_b64 vcc, vcc\n"
        "s_nop 0\n"
        "v_cndmask_b32_dpp v9, v4, v5, vcc quad_perm:[1,0,3,2] row_mask:0xf bank_mask:0xf\n"
        "v_cndmask_b32_dpp v11, v6, v7, vcc quad_perm:[1,0,3,2] row_mask:0xf bank_mask:0xf\n"
        "s_mov_b64 vcc, s[6:7]\n"
        "s_nop 0\n"
        "v_cndmask_b32_dpp v28, v10, v8, vcc quad_perm:[2,3,0,1] row_mask:0xf bank_mask:0xf\n"
        "v_cndmask_b32_dpp v29, v11, v9, vcc quad_perm:[2,3,0,1] row_mask:0xf bank_mask:0xf\n"
        "s_not_b64 vcc, vcc\n"
        "s_nop 0\n"
        "v_cndmask_b32_dpp v30, v8, v10, vcc quad_perm:[2,3,0,1] row_mask:0xf bank_mask:0xf\n"
        "v_cndmask_b32_dpp v31, v9, v11, vcc quad_perm:[2,3,0,1] row_mask:0xf bank_mask:0xf\n"
        "buffer_store_dwordx4 v[28:31], v103, s[24:27], s51 offen sc1\n"
        "s_nop 1\n"
        "s_barrier\n"
        "s_mov_b32 m0, 110592\n"
        "s_add_u32 s86, s57, 256\n"
        "buffer_load_dwordx4 v101, s[32:35], s86 offen  lds\n"
        "s_mov_b32 m0, 111616\n"
        "s_nop 0\n"
        "buffer_load_dwordx4 v102, s[32:35], s86 offen  lds\n"
        "s_mov_b32 m0, 112640\n"
        "s_add_u32 s86, s57, 33024\n"
        "buffer_load_dwordx4 v101, s[32:35], s86 offen  lds\n"
        "s_mov_b32 m0, 113664\n"
        "s_nop 0\n"
        "buffer_load_dwordx4 v102, s[32:35], s86 offen  lds\n"
        "s_waitcnt vmcnt(4)\n"
        "v_mov_b32 v36, s64\n"
        "v_mov_b32 v37, s65\n"
        "s_lshl_b32 s74, s54, 3\n"
        "v_mov_b32 v38, s74\n"
        "s_mov_b64 exec, 1\n"
        "buffer_store_dwordx2 v[36:37], v38, s[28:31], 0 offen sc1\n"
        "s_mov_b64 exec, -1\n"
        "s_barrier\n"
        "s_mov_b32 m0, 126976\n"
        "s_add_u32 s86, s57, 384\n"
        "buffer_load_dwordx4 v101, s[32:35], s86 offen  lds\n"
        "s_mov_b32 m0, 128000\n"
        "s_nop 0\n"
        "buffer_load_dwordx4 v102, s[32:35], s86 offen  lds\n"
        "s_mov_b32 m0, 129024\n"
        "s_add_u32 s86, s57, 33152\n"
        "buffer_load_dwordx4 v101, s[32:35], s86 offen  lds\n"
        "s_mov_b32 m0, 130048\n"
        "s_nop 0\n"
        "buffer_load_dwordx4 v102, s[32:35], s86 offen  lds\n"
        "s_waitcnt vmcnt(5)\n"
        "s_barrier\n"
        "buffer_load_dwordx2 v[106:107], v104, s[28:31], 0 offen offset:304 sc1\n"
        "buffer_load_dwordx2 v[108:109], v104, s[28:31], 0 offen offset:368 sc1\n"
        "s_mov_b32 m0, 77824\n"
        "s_add_u32 s86, s57, 512\n"
        "buffer_load_dwordx4 v101, s[32:35], s86 offen  lds\n"
        "s_mov_b32 m0, 78848\n"
        "s_nop 0\n"
        "buffer_load_dwordx4 v102, s[32:35], s86 offen  lds\n"
        "s_mov_b32 m0, 79872\n"
        "s_add_u32 s86, s57, 33280\n"
        "buffer_load_dwordx4 v101, s[32:35], s86 offen  lds\n"
        "s_mov_b32 m0, 80896\n"
        "s_nop 0\n"
        "buffer_load_dwordx4 v102, s[32:35], s86 offen  lds\n"
        "s_waitcnt vmcnt(6)\n"
        "s_barrier\n"
        "buffer_load_dwordx2 v[110:111], v104, s[28:31], 0 offen offset:432 sc1\n"
        "s_waitcnt vmcnt(6)\n"
        "v_cmp_ne_u32 vcc, s64, v106\n"
        "v_cmp_ne_u32 s[78:79], s65, v107\n"
        "s_or_b64 vcc, vcc, s[78:79]\n"
        "s_cbranch_scc1 Ls125_%=\n"
        "Lk126_%=:\n"
        "s_mov_b32 m0, 137216\n"
        "s_add_u32 s76, s56, 77824\n"
        "buffer_load_dwordx4 v103, s[24:27], s76 offen sc1 lds\n"
        "s_mov_b32 m0, 138240\n"
        "s_add_u32 s76, s76, 2048\n"
        "buffer_load_dwordx4 v103, s[24:27], s76 offen sc1 lds\n"
        "Le127_%=:\n"
        "s_waitcnt vmcnt(7)\n"
        "v_cmp_ne_u32 vcc, s64, v108\n"
        "v_cmp_ne_u32 s[78:79], s65, v109\n"
        "s_or_b64 vcc, vcc, s[78:79]\n"
        "s_cbranch_scc1 Ls128_%=\n"
        "Lk129_%=:\n"
        "s_mov_b32 m0, 145408\n"
        "s_add_u32 s77, s56, 94208\n"
        "buffer_load_dwordx4 v103, s[24:27], s77 offen sc1 lds\n"
        "s_mov_b32 m0, 146432\n"
        "s_add_u32 s77, s77, 2048\n"
        "buffer_load_dwordx4 v103, s[24:27], s77 offen sc1 lds\n"
        "Le130_%=:\n"
        "s_mov_b32 m0, 94208\n"
        "s_add_u32 s86, s57, 640\n"
        "buffer_load_dwordx4 v101, s[32:35], s86 offen  lds\n"
        "s_mov_b32 m0, 95232\n"
        "s_nop 0\n"
        "buffer_load_dwordx4 v102, s[32:35], s86 offen  lds\n"
        "s_mov_b32 m0, 96256\n"
        "s_add_u32 s86, s57, 33408\n"
        "buffer_load_dwordx4 v101, s[32:35], s86 offen  lds\n"
        "s_mov_b32 m0, 97280\n"
        "s_nop 0\n"
        "buffer_load_dwordx4 v102, s[32:35], s86 offen  lds\n"
        "s_waitcnt vmcnt(9)\n"
        "s_waitcnt vmcnt(6)\n"
        "s_waitcnt vmcnt(4)\n"
        "s_barrier\n"
        "buffer_load_dwordx2 v[112:113], v104, s[28:31], 0 offen offset:496 sc1\n"
        "v_cmp_ne_u32 vcc, s64, v110\n"
        "v_cmp_ne_u32 s[78:79], s65, v111\n"
        "s_or_b64 vcc, vcc, s[78:79]\n"
        "s_cbranch_scc1 Ls131_%=\n"
        "Lk132_%=:\n"
        "s_mov_b32 m0, 153600\n"
        "s_add_u32 s74, s56, 110592\n"
        "buffer_load_dwordx4 v103, s[24:27], s74 offen sc1 lds\n"
        "s_mov_b32 m0, 154624\n"
        "s_add_u32 s74, s74, 2048\n"
        "buffer_load_dwordx4 v103, s[24:27], s74 offen sc1 lds\n"
        "Le133_%=:\n"
        "s_mov_b32 m0, 110592\n"
        "s_add_u32 s86, s57, 768\n"
        "buffer_load_dwordx4 v101, s[32:35], s86 offen  lds\n"
        "s_mov_b32 m0, 111616\n"
        "s_nop 0\n"
        "buffer_load_dwordx4 v102, s[32:35], s86 offen  lds\n"
        "s_mov_b32 m0, 112640\n"
        "s_add_u32 s86, s57, 33536\n"
        "buffer_load_dwordx4 v101, s[32:35], s86 offen  lds\n"
        "s_mov_b32 m0, 113664\n"
        "s_nop 0\n"
        "buffer_load_dwordx4 v102, s[32:35], s86 offen  lds\n"
        "buffer_load_dwordx4 v[0:3], v100, s[20:23], s52 offen nt\n"
        "s_add_u32 s75, s52, 14336\n"
        "buffer_load_dwordx4 v[4:7], v100, s[20:23], s75 offen nt\n"
        "s_add_u32 s76, s52, 28672\n"
        "buffer_load_dwordx4 v[8:11], v100, s[20:23], s76 offen nt\n"
        "s_add_u32 s77, s52, 43008\n"
        "buffer_load_dwordx4 v[12:15], v100, s[20:23], s77 offen nt\n"
        "s_add_u32 s74, s52, 57344\n"
        "buffer_load_dwordx4 v[16:19], v100, s[20:23], s74 offen nt\n"
        "s_add_u32 s75, s52, 71680\n"
        "buffer_load_dwordx4 v[20:23], v100, s[20:23], s75 offen nt\n"
        "s_add_u32 s76, s52, 86016\n"
        "buffer_load_dwordx4 v[24:27], v100, s[20:23], s76 offen nt\n"
        "s_add_u32 s77, s52, 100352\n"
        "buffer_load_dwordx4 v[28:31], v100, s[20:23], s77 offen nt\n"
        "s_waitcnt vmcnt(15)\n"
        "s_waitcnt vmcnt(12)\n"
        "s_barrier\n"
        "buffer_load_dwordx2 v[106:107], v104, s[28:31], 0 offen offset:560 sc1\n"
        "v_cmp_ne_u32 vcc, s64, v112\n"
        "v_cmp_ne_u32 s[78:79], s65, v113\n"
        "s_or_b64 vcc, vcc, s[78:79]\n"
        "s_cbranch_scc1 Ls134_%=\n"
        "Lk135_%=:\n"
        "s_mov_b32 m0, 161792\n"
        "s_add_u32 s74, s56, 126976\n"
        "buffer_load_dwordx4 v103, s[24:27], s74 offen sc1 lds\n"
        "s_mov_b32 m0, 162816\n"
        "s_add_u32 s74, s74, 2048\n"
        "buffer_load_dwordx4 v103, s[24:27], s74 offen sc1 lds\n"
        "Le136_%=:\n"
        "s_mov_b32 m0, 126976\n"
        "s_add_u32 s86, s57, 896\n"
        "buffer_load_dwordx4 v101, s[32:35], s86 offen  lds\n"
        "s_mov_b32 m0, 128000\n"
        "s_nop 0\n"
        "buffer_load_dwordx4 v102, s[32:35], s86 offen  lds\n"
        "s_mov_b32 m0, 129024\n"
        "s_add_u32 s86, s57, 33664\n"
        "buffer_load_dwordx4 v101, s[32:35], s86 offen  lds\n"
        "s_mov_b32 m0, 130048\n"
        "s_nop 0\n"
        "buffer_load_dwordx4 v102, s[32:35], s86 offen  lds\n"
        "s_waitcnt vmcnt(15)\n"
        "s_waitcnt vmcnt(4)\n"
        "s_barrier\n"
        "buffer_load_dwordx2 v[108:109], v104, s[28:31], 0 offen offset:624 sc1\n"
        "buffer_load_dwordx2 v[110:111], v104, s[28:31], 0 offen offset:688 sc1\n"
        "buffer_load_dwordx2 v[112:113], v104, s[28:31], 0 offen offset:752 sc1\n"
        "v_cmp_ne_u32 vcc, s64, v106\n"
        "v_cmp_ne_u32 s[78:79], s65, v107\n"
        "s_or_b64 vcc, vcc, s[78:79]\n"
        "s_cbranch_scc1 Ls137_%=\n"
        "Lk138_%=:\n"
        "s_mov_b32 m0, 137216\n"
        "s_add_u32 s75, s56, 143360\n"
        "buffer_load_dwordx4 v103, s[24:27], s75 offen sc1 lds\n"
        "s_mov_b32 m0, 138240\n"
        "s_add_u32 s75, s75, 2048\n"
        "buffer_load_dwordx4 v103, s[24:27], s75 offen sc1 lds\n"
        "Le139_%=:\n"
        "s_mov_b32 m0, 77824\n"
        "s_add_u32 s86, s57, 1024\n"
        "buffer_load_dwordx4 v101, s[32:35], s86 offen  lds\n"
        "s_mov_b32 m0, 78848\n"
        "s_nop 0\n"
        "buffer_load_dwordx4 v102, s[32:35], s86 offen  lds\n"
        "s_mov_b32 m0, 79872\n"
        "s_add_u32 s86, s57, 33792\n"
        "buffer_load_dwordx4 v101, s[32:35], s86 offen  lds\n"
        "s_mov_b32 m0, 80896\n"
        "s_nop 0\n"
        "buffer_load_dwordx4 v102, s[32:35], s86 offen  lds\n"
        "v_lshl_or_b32 v0, v4, 4, v0\n"
        "v_lshl_or_b32 v1, v5, 4, v1\n"
        "v_lshl_or_b32 v2, v6, 4, v2\n"
        "v_lshl_or_b32 v3, v7, 4, v3\n"
        "v_lshl_or_b32 v8, v12, 4, v8\n"
        "v_lshl_or_b32 v9, v13, 4, v9\n"
        "v_lshl_or_b32 v10, v14, 4, v10\n"
        "v_lshl_or_b32 v11, v15, 4, v11\n"
        "v_lshl_or_b32 v0, v8, 8, v0\n"
        "v_lshl_or_b32 v1, v9, 8, v1\n"
        "v_lshl_or_b32 v2, v10, 8, v2\n"
        "v_lshl_or_b32 v3, v11, 8, v3\n"
        "v_lshl_or_b32 v16, v20, 4, v16\n"
        "v_lshl_or_b32 v17, v21, 4, v17\n"
        "v_lshl_or_b32 v18, v22, 4, v18\n"
        "v_lshl_or_b32 v19, v23, 4, v19\n"
        "v_lshl_or_b32 v24, v28, 4, v24\n"
        "v_lshl_or_b32 v25, v29, 4, v25\n"
        "v_lshl_or_b32 v26, v30, 4, v26\n"
        "v_lshl_or_b32 v27, v31, 4, v27\n"
        "v_lshl_or_b32 v16, v24, 8, v16\n"
        "v_lshl_or_b32 v17, v25, 8, v17\n"
        "v_lshl_or_b32 v18, v26, 8, v18\n"
        "v_lshl_or_b32 v19, v27, 8, v19\n"
        "v_lshl_or_b32 v4, v16, 16, v0\n"
        "v_lshl_or_b32 v5, v17, 16, v1\n"
        "v_lshl_or_b32 v6, v18, 16, v2\n"
        "v_lshl_or_b32 v7, v19, 16, v3\n"
        "s_mov_b64 vcc, s[4:5]\n"
        "s_nop 0\n"
        "v_cndmask_b32_dpp v8, v5, v4, vcc quad_perm:[1,0,3,2] row_mask:0xf bank_mask:0xf\n"
        "v_cndmask_b32_dpp v10, v7, v6, vcc quad_perm:[1,0,3,2] row_mask:0xf bank_mask:0xf\n"
        "s_not_b64 vcc, vcc\n"
        "s_nop 0\n"
        "v_cndmask_b32_dpp v9, v4, v5, vcc quad_perm:[1,0,3,2] row_mask:0xf bank_mask:0xf\n"
        "v_cndmask_b32_dpp v11, v6, v7, vcc quad_perm:[1,0,3,2] row_mask:0xf bank_mask:0xf\n"
        "s_mov_b64 vcc, s[6:7]\n"
        "s_nop 0\n"
        "v_cndmask_b32_dpp v28, v10, v8, vcc quad_perm:[2,3,0,1] row_mask:0xf bank_mask:0xf\n"
        "v_cndmask_b32_dpp v29, v11, v9, vcc quad_perm:[2,3,0,1] row_mask:0xf bank_mask:0xf\n"
        "s_not_b64 vcc, vcc\n"
        "s_nop 0\n"
        "v_cndmask_b32_dpp v30, v8, v10, vcc quad_perm:[2,3,0,1] row_mask:0xf bank_mask:0xf\n"
        "v_cndmask_b32_dpp v31, v9, v11, vcc quad_perm:[2,3,0,1] row_mask:0xf bank_mask:0xf\n"
        "buffer_store_dwordx4 v[28:31], v103, s[24:27], s53 offen sc1\n"
        "s_nop 1\n"
        "s_waitcnt vmcnt(10)\n"
        "s_waitcnt vmcnt(5)\n"
        "s_barrier\n"
        "buffer_load_dwordx2 v[106:107], v104, s[28:31], 0 offen offset:816 sc1\n"
        "v_cmp_ne_u32 vcc, s64, v108\n"
        "v_cmp_ne_u32 s[78:79], s65, v109\n"
        "s_or_b64 vcc, vcc, s[78:79]\n"
        "s_cbranch_scc1 Ls140_%=\n"
        "Lk141_%=:\n"
        "s_mov_b32 m0, 145408\n"
        "s_add_u32 s76, s56, 159744\n"
        "buffer_load_dwordx4 v103, s[24:27], s76 offen sc1 lds\n"
        "s_mov_b32 m0, 146432\n"
        "s_add_u32 s76, s76, 2048\n"
        "buffer_load_dwordx4 v103, s[24:27], s76 offen sc1 lds\n"
        "Le142_%=:\n"
        "v_cmp_ne_u32 vcc, s64, v110\n"
        "v_cmp_ne_u32 s[78:79], s65, v111\n"
        "s_or_b64 vcc, vcc, s[78:79]\n"
        "s_cbranch_scc1 Ls143_%=\n"
        "Lk144_%=:\n"
        "s_mov_b32 m0, 153600\n"
        "s_add_u32 s77, s56, 176128\n"
        "buffer_load_dwordx4 v103, s[24:27], s77 offen sc1 lds\n"
        "s_mov_b32 m0, 154624\n"
        "s_add_u32 s77, s77, 2048\n"
        "buffer_load_dwordx4 v103, s[24:27], s77 offen sc1 lds\n"
        "Le145_%=:\n"
        "v_cmp_ne_u32 vcc, s64, v112\n"
        "v_cmp_ne_u32 s[78:79], s65, v113\n"
        "s_or_b64 vcc, vcc, s[78:79]\n"
        "s_cbranch_scc1 Ls146_%=\n"
        "Lk147_%=:\n"
        "s_mov_b32 m0, 161792\n"
        "s_add_u32 s74, s56, 192512\n"
        "buffer_load_dwordx4 v103, s[24:27], s74 offen sc1 lds\n"
        "s_mov_b32 m0, 162816\n"
        "s_add_u32 s74, s74, 2048\n"
        "buffer_load_dwordx4 v103, s[24:27], s74 offen sc1 lds\n"
        "Le148_%=:\n"
        "s_mov_b32 m0, 94208\n"
        "s_add_u32 s86, s57, 1152\n"
        "buffer_load_dwordx4 v101, s[32:35], s86 offen  lds\n"
        "s_mov_b32 m0, 95232\n"
        "s_nop 0\n"
        "buffer_load_dwordx4 v102, s[32:35], s86 offen  lds\n"
        "s_mov_b32 m0, 96256\n"
        "s_add_u32 s86, s57, 33920\n"
        "buffer_load_dwordx4 v101, s[32:35], s86 offen  lds\n"
        "s_mov_b32 m0, 97280\n"
        "s_nop 0\n"
        "buffer_load_dwordx4 v102, s[32:35], s86 offen  lds\n"
        "s_waitcnt vmcnt(11)\n"
        "v_mov_b32 v36, s64\n"
        "v_mov_b32 v37, s65\n"
        "s_lshl_b32 s74, s55, 3\n"
        "v_mov_b32 v38, s74\n"
        "s_mov_b64 exec, 1\n"
        "buffer_store_dwordx2 v[36:37], v38, s[28:31], 0 offen sc1\n"
        "s_mov_b64 exec, -1\n"
        "s_waitcnt vmcnt(9)\n"
        "s_barrier\n"
        "buffer_load_dwordx2 v[108:109], v104, s[28:31], 0 offen offset:880 sc1\n"
        "v_cmp_ne_u32 vcc, s64, v106\n"
        "v_cmp_ne_u32 s[78:79], s65, v107\n"
        "s_or_b64 vcc, vcc, s[78:79]\n"
        "s_cbranch_scc1 Ls149_%=\n"
        "Lk150_%=:\n"
        "s_mov_b32 m0, 137216\n"
        "s_add_u32 s75, s56, 208896\n"
        "buffer_load_dwordx4 v103, s[24:27], s75 offen sc1 lds\n"
        "s_mov_b32 m0, 138240\n"
        "s_add_u32 s75, s75, 2048\n"
        "buffer_load_dwordx4 v103, s[24:27], s75 offen sc1 lds\n"
        "Le151_%=:\n"
        "s_mov_b32 m0, 110592\n"
        "s_add_u32 s86, s57, 1280\n"
        "buffer_load_dwordx4 v101, s[32:35], s86 offen  lds\n"
        "s_mov_b32 m0, 111616\n"
        "s_nop 0\n"
        "buffer_load_dwordx4 v102, s[32:35], s86 offen  lds\n"
        "s_mov_b32 m0, 112640\n"
        "s_add_u32 s86, s57, 34048\n"
        "buffer_load_dwordx4 v101, s[32:35], s86 offen  lds\n"
        "s_mov_b32 m0, 113664\n"
        "s_nop 0\n"
        "buffer_load_dwordx4 v102, s[32:35], s86 offen  lds\n"
        "v_mov_b32 v40, s64\n"
        "v_mov_b32 v41, s65\n"
        "v_mov_b32 v42, s66\n"
        "v_mov_b32 v43, s67\n"
        "v_mov_b32 v44, 0\n"
        "s_mov_b64 exec, 1\n"
        "global_atomic_cmpswap_x2 v44, v[40:43], s[68:69] sc1\n"
        "s_mov_b64 exec, -1\n"
        "s_waitcnt vmcnt(9)\n"
        "s_barrier\n"
        "buffer_load_dwordx2 v[110:111], v104, s[28:31], 0 offen offset:944 sc1\n"
        "s_waitcnt vmcnt(8)\n"
        "v_cmp_ne_u32 vcc, s64, v108\n"
        "v_cmp_ne_u32 s[78:79], s65, v109\n"
        "s_or_b64 vcc, vcc, s[78:79]\n"
        "s_cbranch_scc1 Ls152_%=\n"
        "Lk153_%=:\n"
        "s_mov_b32 m0, 145408\n"
        "s_add_u32 s76, s56, 225280\n"
        "buffer_load_dwordx4 v103, s[24:27], s76 offen sc1 lds\n"
        "s_mov_b32 m0, 146432\n"
        "s_add_u32 s76, s76, 2048\n"
        "buffer_load_dwordx4 v103, s[24:27], s76 offen sc1 lds\n"
        "Le154_%=:\n"
        "s_mov_b32 m0, 126976\n"
        "s_add_u32 s86, s57, 1408\n"
        "buffer_load_dwordx4 v101, s[32:35], s86 offen  lds\n"
        "s_mov_b32 m0, 128000\n"
        "s_nop 0\n"
        "buffer_load_dwordx4 v102, s[32:35], s86 offen  lds\n"
        "s_mov_b32 m0, 129024\n"
        "s_add_u32 s86, s57, 34176\n"
        "buffer_load_dwordx4 v101, s[32:35], s86 offen  lds\n"
        "s_mov_b32 m0, 130048\n"
        "s_nop 0\n"
        "buffer_load_dwordx4 v102, s[32:35], s86 offen  lds\n"
        "s_mov_b32 s36, s16\n"
        "s_and_b32 s37, s17, 0xffff\n"
        "s_mov_b32 s38, 14680064\n"
        "s_mov_b32 s39, 0x20000\n"
        "s_mul_i32 s59, s46, 917504\n"
        "s_mul_i32 s74, s45, 448\n"
        "s_add_u32 s59, s59, s74\n"
        "v_add_u32 v34, 0, v32\n"
        "v_mul_u32_u24 v35, 2341, v34\n"
        "v_lshrrev_b32 v35, 16, v35\n"
        "v_mul_u32_u24 v36, 28, v35\n"
        "v_sub_u32 v36, v34, v36\n"
        "v_lshlrev_b32 v36, 4, v36\n"
        "v_min_u32 v37, 7, v35\n"
        "v_cmp_le_u32 vcc, 224, v36\n"
        "v_subrev_u32 v38, 224, v36\n"
        "v_mov_b32 v39, 0\n"
        "v_mov_b32 v40, 2\n"
        "v_cndmask_b32 v38, v36, v38, vcc\n"
        "v_cndmask_b32 v39, v39, v40, vcc\n"
        "v_mul_u32_u24 v64, 3584, v39\n"
        "v_mul_u32_u24 v40, 112, v37\n"
        "v_add_u32 v64, v40, v64\n"
        "v_add_u32 v64, v38, v64\n"
        "v_add_u32 v64, 6784, v64\n"
        "v_add_u32 v40, 1696, v64\n"
        "v_cmp_le_u32 vcc, 96, v38\n"
        "s_nop 1\n"
        "v_cndmask_b32 v64, v64, v40, vcc\n"
        "v_lshlrev_b32 v39, 9, v39\n"
        "v_lshl_add_u32 v39, v37, 5, v39\n"
        "v_xor_b32 v40, 16, v38\n"
        "v_add_u32 v39, v39, v40\n"
        "v_add_u32 v39, 19008, v39\n"
        "v_cmp_le_u32 vcc, 192, v38\n"
        "s_nop 1\n"
        "v_cndmask_b32 v64, v64, v39, vcc\n"
        "v_mul_u32_u24 v68, 7168, v35\n"
        "v_add_u32 v68, v68, v36\n"
        "v_add_u32 v68, 516096, v68\n"
        "s_waitcnt vmcnt(8)\n"
        "s_barrier\n"
        "buffer_load_dwordx2 v[112:113], v104, s[28:31], 0 offen offset:1008 sc1\n"
        "s_waitcnt vmcnt(7)\n"
        "v_cmp_ne_u32 vcc, s64, v110\n"
        "v_cmp_ne_u32 s[78:79], s65, v111\n"
        "s_or_b64 vcc, vcc, s[78:79]\n"
        "s_cbranch_scc1 Ls155_%=\n"
        "Lk156_%=:\n"
        "s_mov_b32 m0, 153600\n"
        "s_add_u32 s77, s56, 241664\n"
        "buffer_load_dwordx4 v103, s[24:27], s77 offen sc1 lds\n"
        "s_mov_b32 m0, 154624\n"
        "s_add_u32 s77, s77, 2048\n"
        "buffer_load_dwordx4 v103, s[24:27], s77 offen sc1 lds\n"
        "Le157_%=:\n"
        "s_mov_b32 m0, 77824\n"
        "s_add_u32 s86, s57, 1536\n"
        "buffer_load_dwordx4 v101, s[32:35], s86 offen  lds\n"
        "s_mov_b32 m0, 78848\n"
        "s_nop 0\n"
        "buffer_load_dwordx4 v102, s[32:35], s86 offen  lds\n"
        "s_mov_b32 m0, 79872\n"
        "s_add_u32 s86, s57, 34304\n"
        "buffer_load_dwordx4 v101, s[32:35], s86 offen  lds\n"
        "s_mov_b32 m0, 80896\n"
        "s_nop 0\n"
        "buffer_load_dwordx4 v102, s[32:35], s86 offen  lds\n"
        "v_add_u32 v34, 64, v32\n"
        "v_mul_u32_u24 v35, 2341, v34\n"
        "v_lshrrev_b32 v35, 16, v35\n"
        "v_mul_u32_u24 v36, 28, v35\n"
        "v_sub_u32 v36, v34, v36\n"
        "v_lshlrev_b32 v36, 4, v36\n"
        "v_min_u32 v37, 7, v35\n"
        "v_cmp_le_u32 vcc, 224, v36\n"
        "v_subrev_u32 v38, 224, v36\n"
        "v_mov_b32 v39, 0\n"
        "v_mov_b32 v40, 2\n"
        "v_cndmask_b32 v38, v36, v38, vcc\n"
        "v_cndmask_b32 v39, v39, v40, vcc\n"
        "v_mul_u32_u24 v65, 3584, v39\n"
        "v_mul_u32_u24 v40, 112, v37\n"
        "v_add_u32 v65, v40, v65\n"
        "v_add_u32 v65, v38, v65\n"
        "v_add_u32 v65, 6784, v65\n"
        "v_add_u32 v40, 1696, v65\n"
        "v_cmp_le_u32 vcc, 96, v38\n"
        "s_nop 1\n"
        "v_cndmask_b32 v65, v65, v40, vcc\n"
        "v_lshlrev_b32 v39, 9, v39\n"
        "v_lshl_add_u32 v39, v37, 5, v39\n"
        "v_xor_b32 v40, 16, v38\n"
        "v_add_u32 v39, v39, v40\n"
        "v_add_u32 v39, 19008, v39\n"
        "v_cmp_le_u32 vcc, 192, v38\n"
        "s_nop 1\n"
        "v_cndmask_b32 v65, v65, v39, vcc\n"
        "v_mul_u32_u24 v69, 7168, v35\n"
        "v_add_u32 v69, v69, v36\n"
        "v_add_u32 v69, 516096, v69\n"
        "s_waitcnt vmcnt(7)\n"
        "s_barrier\n"
        "s_waitcnt vmcnt(6)\n"
        "v_cmp_ne_u32 vcc, s64, v112\n"
        "v_cmp_ne_u32 s[78:79], s65, v113\n"
        "s_or_b64 vcc, vcc, s[78:79]\n"
        "s_cbranch_scc1 Ls158_%=\n"
        "Lk159_%=:\n"
        "s_mov_b32 m0, 161792\n"
        "s_add_u32 s74, s56, 258048\n"
        "buffer_load_dwordx4 v103, s[24:27], s74 offen sc1 lds\n"
        "s_mov_b32 m0, 162816\n"
        "s_add_u32 s74, s74, 2048\n"
        "buffer_load_dwordx4 v103, s[24:27], s74 offen sc1 lds\n"
        "Le160_%=:\n"
        "s_mov_b32 m0, 94208\n"
        "s_add_u32 s86, s57, 1664\n"
        "buffer_load_dwordx4 v101, s[32:35], s86 offen  lds\n"
        "s_mov_b32 m0, 95232\n"
        "s_nop 0\n"
        "buffer_load_dwordx4 v102, s[32:35], s86 offen  lds\n"
        "s_mov_b32 m0, 96256\n"
        "s_add_u32 s86, s57, 34432\n"
        "buffer_load_dwordx4 v101, s[32:35], s86 offen  lds\n"
        "s_mov_b32 m0, 97280\n"
        "s_nop 0\n"
        "buffer_load_dwordx4 v102, s[32:35], s86 offen  lds\n"
        "v_add_u32 v34, 128, v32\n"
        "v_mul_u32_u24 v35, 2341, v34\n"
        "v_lshrrev_b32 v35, 16, v35\n"
        "v_mul_u32_u24 v36, 28, v35\n"
        "v_sub_u32 v36, v34, v36\n"
        "v_lshlrev_b32 v36, 4, v36\n"
        "v_min_u32 v37, 7, v35\n"
        "v_cmp_le_u32 vcc, 224, v36\n"
        "v_subrev_u32 v38, 224, v36\n"
        "v_mov_b32 v39, 0\n"
        "v_mov_b32 v40, 2\n"
        "v_cndmask_b32 v38, v36, v38, vcc\n"
        "v_cndmask_b32 v39, v39, v40, vcc\n"
        "v_mul_u32_u24 v66, 3584, v39\n"
        "v_mul_u32_u24 v40, 112, v37\n"
        "v_add_u32 v66, v40, v66\n"
        "v_add_u32 v66, v38, v66\n"
        "v_add_u32 v66, 6784, v66\n"
        "v_add_u32 v40, 1696, v66\n"
        "v_cmp_le_u32 vcc, 96, v38\n"
        "s_nop 1\n"
        "v_cndmask_b32 v66, v66, v40, vcc\n"
        "v_lshlrev_b32 v39, 9, v39\n"
        "v_lshl_add_u32 v39, v37, 5, v39\n"
        "v_xor_b32 v40, 16, v38\n"
        "v_add_u32 v39, v39, v40\n"
        "v_add_u32 v39, 19008, v39\n"
        "v_cmp_le_u32 vcc, 192, v38\n"
        "s_nop 1\n"
        "v_cndmask_b32 v66, v66, v39, vcc\n"
        "v_mul_u32_u24 v70, 7168, v35\n"
        "v_add_u32 v70, v70, v36\n"
        "v_add_u32 v70, 516096, v70\n"
        "s_waitcnt vmcnt(6)\n"
        "s_barrier\n"
        "s_mov_b32 m0, 110592\n"
        "s_add_u32 s86, s57, 1792\n"
        "buffer_load_dwordx4 v101, s[32:35], s86 offen  lds\n"
        "s_mov_b32 m0, 111616\n"
        "s_nop 0\n"
        "buffer_load_dwordx4 v102, s[32:35], s86 offen  lds\n"
        "s_mov_b32 m0, 112640\n"
        "s_add_u32 s86, s57, 34560\n"
        "buffer_load_dwordx4 v101, s[32:35], s86 offen  lds\n"
        "s_mov_b32 m0, 113664\n"
        "s_nop 0\n"
        "buffer_load_dwordx4 v102, s[32:35], s86 offen  lds\n"
        "s_mov_b32 m0, 126976\n"
        "s_add_u32 s86, s57, 1920\n"
        "buffer_load_dwordx4 v101, s[32:35], s86 offen  lds\n"
        "s_mov_b32 m0, 128000\n"
        "s_nop 0\n"
        "buffer_load_dwordx4 v102, s[32:35], s86 offen  lds\n"
        "s_mov_b32 m0, 129024\n"
        "s_add_u32 s86, s57, 34688\n"
        "buffer_load_dwordx4 v101, s[32:35], s86 offen  lds\n"
        "s_mov_b32 m0, 130048\n"
        "s_nop 0\n"
        "buffer_load_dwordx4 v102, s[32:35], s86 offen  lds\n"
        "v_add_u32 v34, 192, v32\n"
        "v_mul_u32_u24 v35, 2341, v34\n"
        "v_lshrrev_b32 v35, 16, v35\n"
        "v_mul_u32_u24 v36, 28, v35\n"
        "v_sub_u32 v36, v34, v36\n"
        "v_lshlrev_b32 v36, 4, v36\n"
        "v_min_u32 v37, 7, v35\n"
        "v_cmp_le_u32 vcc, 224, v36\n"
        "v_subrev_u32 v38, 224, v36\n"
        "v_mov_b32 v39, 0\n"
        "v_mov_b32 v40, 2\n"
        "v_cndmask_b32 v38, v36, v38, vcc\n"
        "v_cndmask_b32 v39, v39, v40, vcc\n"
        "v_mul_u32_u24 v67, 3584, v39\n"
        "v_mul_u32_u24 v40, 112, v37\n"
        "v_add_u32 v67, v40, v67\n"
        "v_add_u32 v67, v38, v67\n"
        "v_add_u32 v67, 6784, v67\n"
        "v_add_u32 v40, 1696, v67\n"
        "v_cmp_le_u32 vcc, 96, v38\n"
        "s_nop 1\n"
        "v_cndmask_b32 v67, v67, v40, vcc\n"
        "v_lshlrev_b32 v39, 9, v39\n"
        "v_lshl_add_u32 v39, v37, 5, v39\n"
        "v_xor_b32 v40, 16, v38\n"
        "v_add_u32 v39, v39, v40\n"
        "v_add_u32 v39, 19008, v39\n"
        "v_cmp_le_u32 vcc, 192, v38\n"
        "s_nop 1\n"
        "v_cndmask_b32 v67, v67, v39, vcc\n"
        "v_mul_u32_u24 v71, 7168, v35\n"
        "v_add_u32 v71, v71, v36\n"
        "v_add_u32 v71, 516096, v71\n"
        "v_mov_b32 v37, 0x7ffffff0\n"
        "v_cmp_gt_u32 vcc, 32, v32\n"
        "s_nop 1\n"
        "v_cndmask_b32 v71, v37, v71, vcc\n"
        "s_waitcnt vmcnt(8)\n"
        "s_barrier\n"
        "s_waitcnt vmcnt(4)\n"
        "s_waitcnt vmcnt(0)\n"
        "s_barrier\n"
        "s_barrier\n"
        "ds_read_b128 v[0:3], v64 offset:61440\n"
        "ds_read_b128 v[4:7], v65 offset:61440\n"
        "ds_read_b128 v[8:11], v66 offset:61440\n"
        "ds_read_b128 v[12:15], v67 offset:61440\n"
        "s_add_u32 s75, s59, 0\n"
        "s_waitcnt lgkmcnt(3)\n"
        "buffer_store_dwordx4 v[0:3], v68, s[36:39], s75 offen sc1 nt\n"
        "s_waitcnt lgkmcnt(2)\n"
        "buffer_store_dwordx4 v[4:7], v69, s[36:39], s75 offen sc1 nt\n"
        "s_waitcnt lgkmcnt(1)\n"
        "buffer_store_dwordx4 v[8:11], v70, s[36:39], s75 offen sc1 nt\n"
        "s_waitcnt lgkmcnt(0)\n"
        "buffer_store_dwordx4 v[12:15], v71, s[36:39], s75 offen sc1 nt\n"
        "s_nop 1\n"
        "s_barrier\n"
        "ds_read_b128 v[16:19], v64 offset:0\n"
        "ds_read_b128 v[20:23], v65 offset:0\n"
        "ds_read_b128 v[24:27], v66 offset:0\n"
        "ds_read_b128 v[28:31], v67 offset:0\n"
        "s_add_u32 s76, s59, 114688\n"
        "s_waitcnt lgkmcnt(3)\n"
        "buffer_store_dwordx4 v[16:19], v68, s[36:39], s76 offen sc1 nt\n"
        "s_waitcnt lgkmcnt(2)\n"
        "buffer_store_dwordx4 v[20:23], v69, s[36:39], s76 offen sc1 nt\n"
        "s_waitcnt lgkmcnt(1)\n"
        "buffer_store_dwordx4 v[24:27], v70, s[36:39], s76 offen sc1 nt\n"
        "s_waitcnt lgkmcnt(0)\n"
        "buffer_store_dwordx4 v[28:31], v71, s[36:39], s76 offen sc1 nt\n"
        "s_nop 1\n"
        "s_barrier\n"
        "ds_read_b128 v[0:3], v64 offset:16384\n"
        "ds_read_b128 v[4:7], v65 offset:16384\n"
        "ds_read_b128 v[8:11], v66 offset:16384\n"
        "ds_read_b128 v[12:15], v67 offset:16384\n"
        "s_add_u32 s77, s59, 229376\n"
        "s_waitcnt lgkmcnt(3)\n"
        "buffer_store_dwordx4 v[0:3], v68, s[36:39], s77 offen sc1 nt\n"
        "s_waitcnt lgkmcnt(2)\n"
        "buffer_store_dwordx4 v[4:7], v69, s[36:39], s77 offen sc1 nt\n"
        "s_waitcnt lgkmcnt(1)\n"
        "buffer_store_dwordx4 v[8:11], v70, s[36:39], s77 offen sc1 nt\n"
        "s_waitcnt lgkmcnt(0)\n"
        "buffer_store_dwordx4 v[12:15], v71, s[36:39], s77 offen sc1 nt\n"
        "s_nop 1\n"
        "s_barrier\n"
        "ds_read_b128 v[16:19], v64 offset:32768\n"
        "ds_read_b128 v[20:23], v65 offset:32768\n"
        "ds_read_b128 v[24:27], v66 offset:32768\n"
        "ds_read_b128 v[28:31], v67 offset:32768\n"
        "s_add_u32 s74, s59, 344064\n"
        "s_waitcnt lgkmcnt(3)\n"
        "buffer_store_dwordx4 v[16:19], v68, s[36:39], s74 offen sc1 nt\n"
        "s_waitcnt lgkmcnt(2)\n"
        "buffer_store_dwordx4 v[20:23], v69, s[36:39], s74 offen sc1 nt\n"
        "s_waitcnt lgkmcnt(1)\n"
        "buffer_store_dwordx4 v[24:27], v70, s[36:39], s74 offen sc1 nt\n"
        "s_waitcnt lgkmcnt(0)\n"
        "buffer_store_dwordx4 v[28:31], v71, s[36:39], s74 offen sc1 nt\n"
        "s_nop 1\n"
        "s_endpgm\n"
        "Lend_%=:\n"
        "Ls15_%=:\n"
        "s_mov_b32 s84, 64\n"
        "s_add_u32 s82, s56, 0\n"
        "Lst0l_%=:\n"
        "buffer_load_dwordx4 v[120:123], v250, s[24:27], s82 offen sc1\n"
        "buffer_load_dwordx4 v[124:127], v250, s[24:27], s82 offen offset:1024 sc1\n"
        "s_add_u32 s83, s56, 8192\n"
        "buffer_load_dwordx4 v[136:139], v250, s[24:27], s83 offen sc1\n"
        "buffer_load_dwordx4 v[140:143], v250, s[24:27], s83 offen offset:1024 sc1\n"
        "s_add_u32 s83, s56, 16384\n"
        "buffer_load_dwordx4 v[152:155], v250, s[24:27], s83 offen sc1\n"
        "buffer_load_dwordx4 v[156:159], v250, s[24:27], s83 offen offset:1024 sc1\n"
        "s_add_u32 s83, s56, 24576\n"
        "buffer_load_dwordx4 v[168:171], v250, s[24:27], s83 offen sc1\n"
        "buffer_load_dwordx4 v[172:175], v250, s[24:27], s83 offen offset:1024 sc1\n"
        "s_waitcnt vmcnt(0)\n"
        "v_cmp_ne_u32 vcc, s64, v121\n"
        "v_cmp_ne_u32 s[78:79], s65, v123\n"
        "s_or_b64 vcc, vcc, s[78:79]\n"
        "v_cmp_ne_u32 s[78:79], s64, v125\n"
        "s_or_b64 vcc, vcc, s[78:79]\n"
        "v_cmp_ne_u32 s[78:79], s65, v127\n"
        "s_or_b64 vcc, vcc, s[78:79]\n"
        "s_cbranch_scc0 Lst0r_%=\n"
        "s_sleep 1\n"
        "s_sub_u32 s84, s84, 1\n"
        "s_cmp_lg_u32 s84, 0\n"
        "s_cbranch_scc1 Lst0l_%=\n"
        "v_mbcnt_lo_u32_b32 v112, -1, 0\n"
        "v_mbcnt_hi_u32_b32 v112, -1, v112\n"
        "v_lshrrev_b32 v113, 4, v112\n"
        "v_mul_u32_u24 v113, 114688, v113\n"
        "v_and_b32 v112, 15, v112\n"
        "s_mul_i32 s85, s49, 112\n"
        "s_mul_i32 s82, s48, 48\n"
        "s_add_u32 s82, s82, s85\n"
        "v_lshrrev_b32 v114, 2, v112\n"
        "v_lshlrev_b32 v114, 3, v114\n"
        "v_and_b32 v115, 3, v112\n"
        "v_add3_u32 v114, v114, v115, s82\n"
        "v_lshl_add_u32 v114, v114, 2, v113\n"
        "s_add_u32 s83, s62, 0\n"
        "s_mov_b32 s84, 0\n"
        "v_mov_b32 v120, 0\n"
        "v_mov_b32 v122, 0\n"
        "Lst0q0_%=:\n"
        "buffer_load_dword v115, v114, s[20:23], s83 offen nt\n"
        "buffer_load_dword v116, v114, s[20:23], s83 offen offset:16 nt\n"
        "s_add_u32 s83, s83, 14336\n"
        "s_waitcnt vmcnt(0)\n"
        "v_lshl_or_b32 v120, v115, s84, v120\n"
        "v_lshl_or_b32 v122, v116, s84, v122\n"
        "s_add_u32 s84, s84, 4\n"
        "s_cmp_lg_u32 s84, 32\n"
        "s_cbranch_scc1 Lst0q0_%=\n"
        "v_add_u32 v114, s82, v112\n"
        "v_add_u32 v114, 32, v114\n"
        "v_lshl_add_u32 v114, v114, 2, v113\n"
        "s_add_u32 s83, s62, 0\n"
        "s_mov_b32 s84, 0\n"
        "v_mov_b32 v124, 0\n"
        "Lst0q1_%=:\n"
        "buffer_load_dword v115, v114, s[20:23], s83 offen nt\n"
        "s_add_u32 s83, s83, 14336\n"
        "s_waitcnt vmcnt(0)\n"
        "v_lshl_or_b32 v124, v115, s84, v124\n"
        "s_add_u32 s84, s84, 4\n"
        "s_cmp_lg_u32 s84, 32\n"
        "s_cbranch_scc1 Lst0q1_%=\n"
        "v_add_u32 v114, s85, v112\n"
        "v_add_u32 v114, 96, v114\n"
        "v_lshl_add_u32 v114, v114, 2, v113\n"
        "s_add_u32 s83, s62, 0\n"
        "s_mov_b32 s84, 0\n"
        "v_mov_b32 v126, 0\n"
        "Lst0q2_%=:\n"
        "buffer_load_dword v115, v114, s[20:23], s83 offen nt\n"
        "s_add_u32 s83, s83, 14336\n"
        "s_waitcnt vmcnt(0)\n"
        "v_lshl_or_b32 v126, v115, s84, v126\n"
        "s_add_u32 s84, s84, 4\n"
        "s_cmp_lg_u32 s84, 32\n"
        "s_cbranch_scc1 Lst0q2_%=\n"
        "Lst0r_%=:\n"
        "s_branch Lk16_%=\n"
        "Ls1_%=:\n"
        "s_mov_b32 s84, 64\n"
        "s_add_u32 s82, s56, 8192\n"
        "Lst1l_%=:\n"
        "buffer_load_dwordx4 v[136:139], v250, s[24:27], s82 offen sc1\n"
        "buffer_load_dwordx4 v[140:143], v250, s[24:27], s82 offen offset:1024 sc1\n"
        "s_add_u32 s83, s56, 16384\n"
        "buffer_load_dwordx4 v[152:155], v250, s[24:27], s83 offen sc1\n"
        "buffer_load_dwordx4 v[156:159], v250, s[24:27], s83 offen offset:1024 sc1\n"
        "s_add_u32 s83, s56, 24576\n"
        "buffer_load_dwordx4 v[168:171], v250, s[24:27], s83 offen sc1\n"
        "buffer_load_dwordx4 v[172:175], v250, s[24:27], s83 offen offset:1024 sc1\n"
        "s_waitcnt vmcnt(0)\n"
        "v_cmp_ne_u32 vcc, s64, v137\n"
        "v_cmp_ne_u32 s[78:79], s65, v139\n"
        "s_or_b64 vcc, vcc, s[78:79]\n"
        "v_cmp_ne_u32 s[78:79], s64, v141\n"
        "s_or_b64 vcc, vcc, s[78:79]\n"
        "v_cmp_ne_u32 s[78:79], s65, v143\n"
        "s_or_b64 vcc, vcc, s[78:79]\n"
        "s_cbranch_scc0 Lst1r_%=\n"
        "s_sleep 1\n"
        "s_sub_u32 s84, s84, 1\n"
        "s_cmp_lg_u32 s84, 0\n"
        "s_cbranch_scc1 Lst1l_%=\n"
        "v_mbcnt_lo_u32_b32 v128, -1, 0\n"
        "v_mbcnt_hi_u32_b32 v128, -1, v128\n"
        "v_lshrrev_b32 v129, 4, v128\n"
        "v_mul_u32_u24 v129, 114688, v129\n"
        "v_and_b32 v128, 15, v128\n"
        "s_mul_i32 s85, s49, 112\n"
        "s_mul_i32 s82, s48, 48\n"
        "s_add_u32 s82, s82, s85\n"
        "v_lshrrev_b32 v130, 2, v128\n"
        "v_lshlrev_b32 v130, 3, v130\n"
        "v_and_b32 v131, 3, v128\n"
        "v_add3_u32 v130, v130, v131, s82\n"
        "v_lshl_add_u32 v130, v130, 2, v129\n"
        "s_add_u32 s83, s62, 458752\n"
        "s_mov_b32 s84, 0\n"
        "v_mov_b32 v136, 0\n"
        "v_mov_b32 v138, 0\n"
        "Lst1q0_%=:\n"
        "buffer_load_dword v131, v130, s[20:23], s83 offen nt\n"
        "buffer_load_dword v132, v130, s[20:23], s83 offen offset:16 nt\n"
        "s_add_u32 s83, s83, 14336\n"
        "s_waitcnt vmcnt(0)\n"
        "v_lshl_or_b32 v136, v131, s84, v136\n"
        "v_lshl_or_b32 v138, v132, s84, v138\n"
        "s_add_u32 s84, s84, 4\n"
        "s_cmp_lg_u32 s84, 32\n"
        "s_cbranch_scc1 Lst1q0_%=\n"
        "v_add_u32 v130, s82, v128\n"
        "v_add_u32 v130, 32, v130\n"
        "v_lshl_add_u32 v130, v130, 2, v129\n"
        "s_add_u32 s83, s62, 458752\n"
        "s_mov_b32 s84, 0\n"
        "v_mov_b32 v140, 0\n"
        "Lst1q1_%=:\n"
        "buffer_load_dword v131, v130, s[20:23], s83 offen nt\n"
        "s_add_u32 s83, s83, 14336\n"
        "s_waitcnt vmcnt(0)\n"
        "v_lshl_or_b32 v140, v131, s84, v140\n"
        "s_add_u32 s84, s84, 4\n"
        "s_cmp_lg_u32 s84, 32\n"
        "s_cbranch_scc1 Lst1q1_%=\n"
        "v_add_u32 v130, s85, v128\n"
        "v_add_u32 v130, 96, v130\n"
        "v_lshl_add_u32 v130, v130, 2, v129\n"
        "s_add_u32 s83, s62, 458752\n"
        "s_mov_b32 s84, 0\n"
        "v_mov_b32 v142, 0\n"
        "Lst1q2_%=:\n"
        "buffer_load_dword v131, v130, s[20:23], s83 offen nt\n"
        "s_add_u32 s83, s83, 14336\n"
        "s_waitcnt vmcnt(0)\n"
        "v_lshl_or_b32 v142, v131, s84, v142\n"
        "s_add_u32 s84, s84, 4\n"
        "s_cmp_lg_u32 s84, 32\n"
        "s_cbranch_scc1 Lst1q2_%=\n"
        "Lst1r_%=:\n"
        "s_branch Lk2_%=\n"
        "Ls3_%=:\n"
        "s_mov_b32 s84, 64\n"
        "s_add_u32 s82, s56, 16384\n"
        "Lst2l_%=:\n"
        "buffer_load_dwordx4 v[152:155], v250, s[24:27], s82 offen sc1\n"
        "buffer_load_dwordx4 v[156:159], v250, s[24:27], s82 offen offset:1024 sc1\n"
        "s_add_u32 s83, s56, 24576\n"
        "buffer_load_dwordx4 v[168:171], v250, s[24:27], s83 offen sc1\n"
        "buffer_load_dwordx4 v[172:175], v250, s[24:27], s83 offen offset:1024 sc1\n"
        "s_add_u32 s83, s56, 32768\n"
        "buffer_load_dwordx4 v[184:187], v250, s[24:27], s83 offen sc1\n"
        "buffer_load_dwordx4 v[188:191], v250, s[24:27], s83 offen offset:1024 sc1\n"
        "s_waitcnt vmcnt(0)\n"
        "v_cmp_ne_u32 vcc, s64, v153\n"
        "v_cmp_ne_u32 s[78:79], s65, v155\n"
        "s_or_b64 vcc, vcc, s[78:79]\n"
        "v_cmp_ne_u32 s[78:79], s64, v157\n"
        "s_or_b64 vcc, vcc, s[78:79]\n"
        "v_cmp_ne_u32 s[78:79], s65, v159\n"
        "s_or_b64 vcc, vcc, s[78:79]\n"
        "s_cbranch_scc0 Lst2r_%=\n"
        "s_sleep 1\n"
        "s_sub_u32 s84, s84, 1\n"
        "s_cmp_lg_u32 s84, 0\n"
        "s_cbranch_scc1 Lst2l_%=\n"
        "v_mbcnt_lo_u32_b32 v144, -1, 0\n"
        "v_mbcnt_hi_u32_b32 v144, -1, v144\n"
        "v_lshrrev_b32 v145, 4, v144\n"
        "v_mul_u32_u24 v145, 114688, v145\n"
        "v_and_b32 v144, 15, v144\n"
        "s_mul_i32 s85, s49, 112\n"
        "s_mul_i32 s82, s48, 48\n"
        "s_add_u32 s82, s82, s85\n"
        "v_lshrrev_b32 v146, 2, v144\n"
        "v_lshlrev_b32 v146, 3, v146\n"
        "v_and_b32 v147, 3, v144\n"
        "v_add3_u32 v146, v146, v147, s82\n"
        "v_lshl_add_u32 v146, v146, 2, v145\n"
        "s_add_u32 s83, s62, 917504\n"
        "s_mov_b32 s84, 0\n"
        "v_mov_b32 v152, 0\n"
        "v_mov_b32 v154, 0\n"
        "Lst2q0_%=:\n"
        "buffer_load_dword v147, v146, s[20:23], s83 offen nt\n"
        "buffer_load_dword v148, v146, s[20:23], s83 offen offset:16 nt\n"
        "s_add_u32 s83, s83, 14336\n"
        "s_waitcnt vmcnt(0)\n"
        "v_lshl_or_b32 v152, v147, s84, v152\n"
        "v_lshl_or_b32 v154, v148, s84, v154\n"
        "s_add_u32 s84, s84, 4\n"
        "s_cmp_lg_u32 s84, 32\n"
        "s_cbranch_scc1 Lst2q0_%=\n"
        "v_add_u32 v146, s82, v144\n"
        "v_add_u32 v146, 32, v146\n"
        "v_lshl_add_u32 v146, v146, 2, v145\n"
        "s_add_u32 s83, s62, 917504\n"
        "s_mov_b32 s84, 0\n"
        "v_mov_b32 v156, 0\n"
        "Lst2q1_%=:\n"
        "buffer_load_dword v147, v146, s[20:23], s83 offen nt\n"
        "s_add_u32 s83, s83, 14336\n"
        "s_waitcnt vmcnt(0)\n"
        "v_lshl_or_b32 v156, v147, s84, v156\n"
        "s_add_u32 s84, s84, 4\n"
        "s_cmp_lg_u32 s84, 32\n"
        "s_cbranch_scc1 Lst2q1_%=\n"
        "v_add_u32 v146, s85, v144\n"
        "v_add_u32 v146, 96, v146\n"
        "v_lshl_add_u32 v146, v146, 2, v145\n"
        "s_add_u32 s83, s62, 917504\n"
        "s_mov_b32 s84, 0\n"
        "v_mov_b32 v158, 0\n"
        "Lst2q2_%=:\n"
        "buffer_load_dword v147, v146, s[20:23], s83 offen nt\n"
        "s_add_u32 s83, s83, 14336\n"
        "s_waitcnt vmcnt(0)\n"
        "v_lshl_or_b32 v158, v147, s84, v158\n"
        "s_add_u32 s84, s84, 4\n"
        "s_cmp_lg_u32 s84, 32\n"
        "s_cbranch_scc1 Lst2q2_%=\n"
        "Lst2r_%=:\n"
        "s_branch Lk4_%=\n"
        "Ls5_%=:\n"
        "s_mov_b32 s84, 64\n"
        "s_add_u32 s82, s56, 24576\n"
        "Lst3l_%=:\n"
        "buffer_load_dwordx4 v[168:171], v250, s[24:27], s82 offen sc1\n"
        "buffer_load_dwordx4 v[172:175], v250, s[24:27], s82 offen offset:1024 sc1\n"
        "s_add_u32 s83, s56, 32768\n"
        "buffer_load_dwordx4 v[184:187], v250, s[24:27], s83 offen sc1\n"
        "buffer_load_dwordx4 v[188:191], v250, s[24:27], s83 offen offset:1024 sc1\n"
        "s_add_u32 s83, s56, 40960\n"
        "buffer_load_dwordx4 v[200:203], v250, s[24:27], s83 offen sc1\n"
        "buffer_load_dwordx4 v[204:207], v250, s[24:27], s83 offen offset:1024 sc1\n"
        "s_waitcnt vmcnt(0)\n"
        "v_cmp_ne_u32 vcc, s64, v169\n"
        "v_cmp_ne_u32 s[78:79], s65, v171\n"
        "s_or_b64 vcc, vcc, s[78:79]\n"
        "v_cmp_ne_u32 s[78:79], s64, v173\n"
        "s_or_b64 vcc, vcc, s[78:79]\n"
        "v_cmp_ne_u32 s[78:79], s65, v175\n"
        "s_or_b64 vcc, vcc, s[78:79]\n"
        "s_cbranch_scc0 Lst3r_%=\n"
        "s_sleep 1\n"
        "s_sub_u32 s84, s84, 1\n"
        "s_cmp_lg_u32 s84, 0\n"
        "s_cbranch_scc1 Lst3l_%=\n"
        "v_mbcnt_lo_u32_b32 v160, -1, 0\n"
        "v_mbcnt_hi_u32_b32 v160, -1, v160\n"
        "v_lshrrev_b32 v161, 4, v160\n"
        "v_mul_u32_u24 v161, 114688, v161\n"
        "v_and_b32 v160, 15, v160\n"
        "s_mul_i32 s85, s49, 112\n"
        "s_mul_i32 s82, s48, 48\n"
        "s_add_u32 s82, s82, s85\n"
        "v_lshrrev_b32 v162, 2, v160\n"
        "v_lshlrev_b32 v162, 3, v162\n"
        "v_and_b32 v163, 3, v160\n"
        "v_add3_u32 v162, v162, v163, s82\n"
        "v_lshl_add_u32 v162, v162, 2, v161\n"
        "s_add_u32 s83, s62, 1376256\n"
        "s_mov_b32 s84, 0\n"
        "v_mov_b32 v168, 0\n"
        "v_mov_b32 v170, 0\n"
        "Lst3q0_%=:\n"
        "buffer_load_dword v163, v162, s[20:23], s83 offen nt\n"
        "buffer_load_dword v164, v162, s[20:23], s83 offen offset:16 nt\n"
        "s_add_u32 s83, s83, 14336\n"
        "s_waitcnt vmcnt(0)\n"
        "v_lshl_or_b32 v168, v163, s84, v168\n"
        "v_lshl_or_b32 v170, v164, s84, v170\n"
        "s_add_u32 s84, s84, 4\n"
        "s_cmp_lg_u32 s84, 32\n"
        "s_cbranch_scc1 Lst3q0_%=\n"
        "v_add_u32 v162, s82, v160\n"
        "v_add_u32 v162, 32, v162\n"
        "v_lshl_add_u32 v162, v162, 2, v161\n"
        "s_add_u32 s83, s62, 1376256\n"
        "s_mov_b32 s84, 0\n"
        "v_mov_b32 v172, 0\n"
        "Lst3q1_%=:\n"
        "buffer_load_dword v163, v162, s[20:23], s83 offen nt\n"
        "s_add_u32 s83, s83, 14336\n"
        "s_waitcnt vmcnt(0)\n"
        "v_lshl_or_b32 v172, v163, s84, v172\n"
        "s_add_u32 s84, s84, 4\n"
        "s_cmp_lg_u32 s84, 32\n"
        "s_cbranch_scc1 Lst3q1_%=\n"
        "v_add_u32 v162, s85, v160\n"
        "v_add_u32 v162, 96, v162\n"
        "v_lshl_add_u32 v162, v162, 2, v161\n"
        "s_add_u32 s83, s62, 1376256\n"
        "s_mov_b32 s84, 0\n"
        "v_mov_b32 v174, 0\n"
        "Lst3q2_%=:\n"
        "buffer_load_dword v163, v162, s[20:23], s83 offen nt\n"
        "s_add_u32 s83, s83, 14336\n"
        "s_waitcnt vmcnt(0)\n"
        "v_lshl_or_b32 v174, v163, s84, v174\n"
        "s_add_u32 s84, s84, 4\n"
        "s_cmp_lg_u32 s84, 32\n"
        "s_cbranch_scc1 Lst3q2_%=\n"
        "Lst3r_%=:\n"
        "s_branch Lk6_%=\n"
        "Ls7_%=:\n"
        "s_mov_b32 s84, 64\n"
        "s_add_u32 s82, s56, 32768\n"
        "Lst4l_%=:\n"
        "buffer_load_dwordx4 v[184:187], v250, s[24:27], s82 offen sc1\n"
        "buffer_load_dwordx4 v[188:191], v250, s[24:27], s82 offen offset:1024 sc1\n"
        "s_add_u32 s83, s56, 40960\n"
        "buffer_load_dwordx4 v[200:203], v250, s[24:27], s83 offen sc1\n"
        "buffer_load_dwordx4 v[204:207], v250, s[24:27], s83 offen offset:1024 sc1\n"
        "s_add_u32 s83, s56, 49152\n"
        "buffer_load_dwordx4 v[120:123], v250, s[24:27], s83 offen sc1\n"
        "buffer_load_dwordx4 v[124:127], v250, s[24:27], s83 offen offset:1024 sc1\n"
        "s_waitcnt vmcnt(0)\n"
        "v_cmp_ne_u32 vcc, s64, v185\n"
        "v_cmp_ne_u32 s[78:79], s65, v187\n"
        "s_or_b64 vcc, vcc, s[78:79]\n"
        "v_cmp_ne_u32 s[78:79], s64, v189\n"
        "s_or_b64 vcc, vcc, s[78:79]\n"
        "v_cmp_ne_u32 s[78:79], s65, v191\n"
        "s_or_b64 vcc, vcc, s[78:79]\n"
        "s_cbranch_scc0 Lst4r_%=\n"
        "s_sleep 1\n"
        "s_sub_u32 s84, s84, 1\n"
        "s_cmp_lg_u32 s84, 0\n"
        "s_cbranch_scc1 Lst4l_%=\n"
        "v_mbcnt_lo_u32_b32 v176, -1, 0\n"
        "v_mbcnt_hi_u32_b32 v176, -1, v176\n"
        "v_lshrrev_b32 v177, 4, v176\n"
        "v_mul_u32_u24 v177, 114688, v177\n"
        "v_and_b32 v176, 15, v176\n"
        "s_mul_i32 s85, s49, 112\n"
        "s_mul_i32 s82, s48, 48\n"
        "s_add_u32 s82, s82, s85\n"
        "v_lshrrev_b32 v178, 2, v176\n"
        "v_lshlrev_b32 v178, 3, v178\n"
        "v_and_b32 v179, 3, v176\n"
        "v_add3_u32 v178, v178, v179, s82\n"
        "v_lshl_add_u32 v178, v178, 2, v177\n"
        "s_add_u32 s83, s62, 1835008\n"
        "s_mov_b32 s84, 0\n"
        "v_mov_b32 v184, 0\n"
        "v_mov_b32 v186, 0\n"
        "Lst4q0_%=:\n"
        "buffer_load_dword v179, v178, s[20:23], s83 offen nt\n"
        "buffer_load_dword v180, v178, s[20:23], s83 offen offset:16 nt\n"
        "s_add_u32 s83, s83, 14336\n"
        "s_waitcnt vmcnt(0)\n"
        "v_lshl_or_b32 v184, v179, s84, v184\n"
        "v_lshl_or_b32 v186, v180, s84, v186\n"
        "s_add_u32 s84, s84, 4\n"
        "s_cmp_lg_u32 s84, 32\n"
        "s_cbranch_scc1 Lst4q0_%=\n"
        "v_add_u32 v178, s82, v176\n"
        "v_add_u32 v178, 32, v178\n"
        "v_lshl_add_u32 v178, v178, 2, v177\n"
        "s_add_u32 s83, s62, 1835008\n"
        "s_mov_b32 s84, 0\n"
        "v_mov_b32 v188, 0\n"
        "Lst4q1_%=:\n"
        "buffer_load_dword v179, v178, s[20:23], s83 offen nt\n"
        "s_add_u32 s83, s83, 14336\n"
        "s_waitcnt vmcnt(0)\n"
        "v_lshl_or_b32 v188, v179, s84, v188\n"
        "s_add_u32 s84, s84, 4\n"
        "s_cmp_lg_u32 s84, 32\n"
        "s_cbranch_scc1 Lst4q1_%=\n"
        "v_add_u32 v178, s85, v176\n"
        "v_add_u32 v178, 96, v178\n"
        "v_lshl_add_u32 v178, v178, 2, v177\n"
        "s_add_u32 s83, s62, 1835008\n"
        "s_mov_b32 s84, 0\n"
        "v_mov_b32 v190, 0\n"
        "Lst4q2_%=:\n"
        "buffer_load_dword v179, v178, s[20:23], s83 offen nt\n"
        "s_add_u32 s83, s83, 14336\n"
        "s_waitcnt vmcnt(0)\n"
        "v_lshl_or_b32 v190, v179, s84, v190\n"
        "s_add_u32 s84, s84, 4\n"
        "s_cmp_lg_u32 s84, 32\n"
        "s_cbranch_scc1 Lst4q2_%=\n"
        "Lst4r_%=:\n"
        "s_branch Lk8_%=\n"
        "Ls9_%=:\n"
        "s_mov_b32 s84, 64\n"
        "s_add_u32 s82, s56, 40960\n"
        "Lst5l_%=:\n"
        "buffer_load_dwordx4 v[200:203], v250, s[24:27], s82 offen sc1\n"
        "buffer_load_dwordx4 v[204:207], v250, s[24:27], s82 offen offset:1024 sc1\n"
        "s_add_u32 s83, s56, 49152\n"
        "buffer_load_dwordx4 v[120:123], v250, s[24:27], s83 offen sc1\n"
        "buffer_load_dwordx4 v[124:127], v250, s[24:27], s83 offen offset:1024 sc1\n"
        "s_add_u32 s83, s56, 57344\n"
        "buffer_load_dwordx4 v[136:139], v250, s[24:27], s83 offen sc1\n"
        "buffer_load_dwordx4 v[140:143], v250, s[24:27], s83 offen offset:1024 sc1\n"
        "s_waitcnt vmcnt(0)\n"
        "v_cmp_ne_u32 vcc, s64, v201\n"
        "v_cmp_ne_u32 s[78:79], s65, v203\n"
        "s_or_b64 vcc, vcc, s[78:79]\n"
        "v_cmp_ne_u32 s[78:79], s64, v205\n"
        "s_or_b64 vcc, vcc, s[78:79]\n"
        "v_cmp_ne_u32 s[78:79], s65, v207\n"
        "s_or_b64 vcc, vcc, s[78:79]\n"
        "s_cbranch_scc0 Lst5r_%=\n"
        "s_sleep 1\n"
        "s_sub_u32 s84, s84, 1\n"
        "s_cmp_lg_u32 s84, 0\n"
        "s_cbranch_scc1 Lst5l_%=\n"
        "v_mbcnt_lo_u32_b32 v192, -1, 0\n"
        "v_mbcnt_hi_u32_b32 v192, -1, v192\n"
        "v_lshrrev_b32 v193, 4, v192\n"
        "v_mul_u32_u24 v193, 114688, v193\n"
        "v_and_b32 v192, 15, v192\n"
        "s_mul_i32 s85, s49, 112\n"
        "s_mul_i32 s82, s48, 48\n"
        "s_add_u32 s82, s82, s85\n"
        "v_lshrrev_b32 v194, 2, v192\n"
        "v_lshlrev_b32 v194, 3, v194\n"
        "v_and_b32 v195, 3, v192\n"
        "v_add3_u32 v194, v194, v195, s82\n"
        "v_lshl_add_u32 v194, v194, 2, v193\n"
        "s_add_u32 s83, s62, 2293760\n"
        "s_mov_b32 s84, 0\n"
        "v_mov_b32 v200, 0\n"
        "v_mov_b32 v202, 0\n"
        "Lst5q0_%=:\n"
        "buffer_load_dword v195, v194, s[20:23], s83 offen nt\n"
        "buffer_load_dword v196, v194, s[20:23], s83 offen offset:16 nt\n"
        "s_add_u32 s83, s83, 14336\n"
        "s_waitcnt vmcnt(0)\n"
        "v_lshl_or_b32 v200, v195, s84, v200\n"
        "v_lshl_or_b32 v202, v196, s84, v202\n"
        "s_add_u32 s84, s84, 4\n"
        "s_cmp_lg_u32 s84, 32\n"
        "s_cbranch_scc1 Lst5q0_%=\n"
        "v_add_u32 v194, s82, v192\n"
        "v_add_u32 v194, 32, v194\n"
        "v_lshl_add_u32 v194, v194, 2, v193\n"
        "s_add_u32 s83, s62, 2293760\n"
        "s_mov_b32 s84, 0\n"
        "v_mov_b32 v204, 0\n"
        "Lst5q1_%=:\n"
        "buffer_load_dword v195, v194, s[20:23], s83 offen nt\n"
        "s_add_u32 s83, s83, 14336\n"
        "s_waitcnt vmcnt(0)\n"
        "v_lshl_or_b32 v204, v195, s84, v204\n"
        "s_add_u32 s84, s84, 4\n"
        "s_cmp_lg_u32 s84, 32\n"
        "s_cbranch_scc1 Lst5q1_%=\n"
        "v_add_u32 v194, s85, v192\n"
        "v_add_u32 v194, 96, v194\n"
        "v_lshl_add_u32 v194, v194, 2, v193\n"
        "s_add_u32 s83, s62, 2293760\n"
        "s_mov_b32 s84, 0\n"
        "v_mov_b32 v206, 0\n"
        "Lst5q2_%=:\n"
        "buffer_load_dword v195, v194, s[20:23], s83 offen nt\n"
        "s_add_u32 s83, s83, 14336\n"
        "s_waitcnt vmcnt(0)\n"
        "v_lshl_or_b32 v206, v195, s84, v206\n"
        "s_add_u32 s84, s84, 4\n"
        "s_cmp_lg_u32 s84, 32\n"
        "s_cbranch_scc1 Lst5q2_%=\n"
        "Lst5r_%=:\n"
        "s_branch Lk10_%=\n"
        "Ls11_%=:\n"
        "s_mov_b32 s84, 64\n"
        "s_add_u32 s82, s56, 49152\n"
        "Lst6l_%=:\n"
        "buffer_load_dwordx4 v[120:123], v250, s[24:27], s82 offen sc1\n"
        "buffer_load_dwordx4 v[124:127], v250, s[24:27], s82 offen offset:1024 sc1\n"
        "s_add_u32 s83, s56, 57344\n"
        "buffer_load_dwordx4 v[136:139], v250, s[24:27], s83 offen sc1\n"
        "buffer_load_dwordx4 v[140:143], v250, s[24:27], s83 offen offset:1024 sc1\n"
        "s_waitcnt vmcnt(0)\n"
        "v_cmp_ne_u32 vcc, s64, v121\n"
        "v_cmp_ne_u32 s[78:79], s65, v123\n"
        "s_or_b64 vcc, vcc, s[78:79]\n"
        "v_cmp_ne_u32 s[78:79], s64, v125\n"
        "s_or_b64 vcc, vcc, s[78:79]\n"
        "v_cmp_ne_u32 s[78:79], s65, v127\n"
        "s_or_b64 vcc, vcc, s[78:79]\n"
        "s_cbranch_scc0 Lst6r_%=\n"
        "s_sleep 1\n"
        "s_sub_u32 s84, s84, 1\n"
        "s_cmp_lg_u32 s84, 0\n"
        "s_cbranch_scc1 Lst6l_%=\n"
        "v_mbcnt_lo_u32_b32 v112, -1, 0\n"
        "v_mbcnt_hi_u32_b32 v112, -1, v112\n"
        "v_lshrrev_b32 v113, 4, v112\n"
        "v_mul_u32_u24 v113, 114688, v113\n"
        "v_and_b32 v112, 15, v112\n"
        "s_mul_i32 s85, s49, 112\n"
        "s_mul_i32 s82, s48, 48\n"
        "s_add_u32 s82, s82, s85\n"
        "v_lshrrev_b32 v114, 2, v112\n"
        "v_lshlrev_b32 v114, 3, v114\n"
        "v_and_b32 v115, 3, v112\n"
        "v_add3_u32 v114, v114, v115, s82\n"
        "v_lshl_add_u32 v114, v114, 2, v113\n"
        "s_add_u32 s83, s62, 2752512\n"
        "s_mov_b32 s84, 0\n"
        "v_mov_b32 v120, 0\n"
        "v_mov_b32 v122, 0\n"
        "Lst6q0_%=:\n"
        "buffer_load_dword v115, v114, s[20:23], s83 offen nt\n"
        "buffer_load_dword v116, v114, s[20:23], s83 offen offset:16 nt\n"
        "s_add_u32 s83, s83, 14336\n"
        "s_waitcnt vmcnt(0)\n"
        "v_lshl_or_b32 v120, v115, s84, v120\n"
        "v_lshl_or_b32 v122, v116, s84, v122\n"
        "s_add_u32 s84, s84, 4\n"
        "s_cmp_lg_u32 s84, 32\n"
        "s_cbranch_scc1 Lst6q0_%=\n"
        "v_add_u32 v114, s82, v112\n"
        "v_add_u32 v114, 32, v114\n"
        "v_lshl_add_u32 v114, v114, 2, v113\n"
        "s_add_u32 s83, s62, 2752512\n"
        "s_mov_b32 s84, 0\n"
        "v_mov_b32 v124, 0\n"
        "Lst6q1_%=:\n"
        "buffer_load_dword v115, v114, s[20:23], s83 offen nt\n"
        "s_add_u32 s83, s83, 14336\n"
        "s_waitcnt vmcnt(0)\n"
        "v_lshl_or_b32 v124, v115, s84, v124\n"
        "s_add_u32 s84, s84, 4\n"
        "s_cmp_lg_u32 s84, 32\n"
        "s_cbranch_scc1 Lst6q1_%=\n"
        "v_add_u32 v114, s85, v112\n"
        "v_add_u32 v114, 96, v114\n"
        "v_lshl_add_u32 v114, v114, 2, v113\n"
        "s_add_u32 s83, s62, 2752512\n"
        "s_mov_b32 s84, 0\n"
        "v_mov_b32 v126, 0\n"
        "Lst6q2_%=:\n"
        "buffer_load_dword v115, v114, s[20:23], s83 offen nt\n"
        "s_add_u32 s83, s83, 14336\n"
        "s_waitcnt vmcnt(0)\n"
        "v_lshl_or_b32 v126, v115, s84, v126\n"
        "s_add_u32 s84, s84, 4\n"
        "s_cmp_lg_u32 s84, 32\n"
        "s_cbranch_scc1 Lst6q2_%=\n"
        "Lst6r_%=:\n"
        "s_branch Lk12_%=\n"
        "Ls13_%=:\n"
        "s_mov_b32 s84, 64\n"
        "s_add_u32 s82, s56, 57344\n"
        "Lst7l_%=:\n"
        "buffer_load_dwordx4 v[136:139], v250, s[24:27], s82 offen sc1\n"
        "buffer_load_dwordx4 v[140:143], v250, s[24:27], s82 offen offset:1024 sc1\n"
        "s_waitcnt vmcnt(0)\n"
        "v_cmp_ne_u32 vcc, s64, v137\n"
        "v_cmp_ne_u32 s[78:79], s65, v139\n"
        "s_or_b64 vcc, vcc, s[78:79]\n"
        "v_cmp_ne_u32 s[78:79], s64, v141\n"
        "s_or_b64 vcc, vcc, s[78:79]\n"
        "v_cmp_ne_u32 s[78:79], s65, v143\n"
        "s_or_b64 vcc, vcc, s[78:79]\n"
        "s_cbranch_scc0 Lst7r_%=\n"
        "s_sleep 1\n"
        "s_sub_u32 s84, s84, 1\n"
        "s_cmp_lg_u32 s84, 0\n"
        "s_cbranch_scc1 Lst7l_%=\n"
        "v_mbcnt_lo_u32_b32 v128, -1, 0\n"
        "v_mbcnt_hi_u32_b32 v128, -1, v128\n"
        "v_lshrrev_b32 v129, 4, v128\n"
        "v_mul_u32_u24 v129, 114688, v129\n"
        "v_and_b32 v128, 15, v128\n"
        "s_mul_i32 s85, s49, 112\n"
        "s_mul_i32 s82, s48, 48\n"
        "s_add_u32 s82, s82, s85\n"
        "v_lshrrev_b32 v130, 2, v128\n"
        "v_lshlrev_b32 v130, 3, v130\n"
        "v_and_b32 v131, 3, v128\n"
        "v_add3_u32 v130, v130, v131, s82\n"
        "v_lshl_add_u32 v130, v130, 2, v129\n"
        "s_add_u32 s83, s62, 3211264\n"
        "s_mov_b32 s84, 0\n"
        "v_mov_b32 v136, 0\n"
        "v_mov_b32 v138, 0\n"
        "Lst7q0_%=:\n"
        "buffer_load_dword v131, v130, s[20:23], s83 offen nt\n"
        "buffer_load_dword v132, v130, s[20:23], s83 offen offset:16 nt\n"
        "s_add_u32 s83, s83, 14336\n"
        "s_waitcnt vmcnt(0)\n"
        "v_lshl_or_b32 v136, v131, s84, v136\n"
        "v_lshl_or_b32 v138, v132, s84, v138\n"
        "s_add_u32 s84, s84, 4\n"
        "s_cmp_lg_u32 s84, 32\n"
        "s_cbranch_scc1 Lst7q0_%=\n"
        "v_add_u32 v130, s82, v128\n"
        "v_add_u32 v130, 32, v130\n"
        "v_lshl_add_u32 v130, v130, 2, v129\n"
        "s_add_u32 s83, s62, 3211264\n"
        "s_mov_b32 s84, 0\n"
        "v_mov_b32 v140, 0\n"
        "Lst7q1_%=:\n"
        "buffer_load_dword v131, v130, s[20:23], s83 offen nt\n"
        "s_add_u32 s83, s83, 14336\n"
        "s_waitcnt vmcnt(0)\n"
        "v_lshl_or_b32 v140, v131, s84, v140\n"
        "s_add_u32 s84, s84, 4\n"
        "s_cmp_lg_u32 s84, 32\n"
        "s_cbranch_scc1 Lst7q1_%=\n"
        "v_add_u32 v130, s85, v128\n"
        "v_add_u32 v130, 96, v130\n"
        "v_lshl_add_u32 v130, v130, 2, v129\n"
        "s_add_u32 s83, s62, 3211264\n"
        "s_mov_b32 s84, 0\n"
        "v_mov_b32 v142, 0\n"
        "Lst7q2_%=:\n"
        "buffer_load_dword v131, v130, s[20:23], s83 offen nt\n"
        "s_add_u32 s83, s83, 14336\n"
        "s_waitcnt vmcnt(0)\n"
        "v_lshl_or_b32 v142, v131, s84, v142\n"
        "s_add_u32 s84, s84, 4\n"
        "s_cmp_lg_u32 s84, 32\n"
        "s_cbranch_scc1 Lst7q2_%=\n"
        "Lst7r_%=:\n"
        "s_branch Lk14_%=\n"
        "Ls17_%=:\n"
        "s_mov_b32 s82, 256\n"
        "s_add_u32 s83, s62, 3670016\n"
        "s_mov_b32 s85, 131072\n"
        "s_getpc_b64 s[80:81]\n"
        "s_branch Lslowd_%=\n"
        "s_cmp_eq_u32 s84, 0\n"
        "s_cbranch_scc1 Lk18_%=\n"
        "s_branch Le19_%=\n"
        "Ls20_%=:\n"
        "s_mov_b32 s82, 320\n"
        "s_add_u32 s83, s62, 4587520\n"
        "s_mov_b32 s85, 139264\n"
        "s_getpc_b64 s[80:81]\n"
        "s_branch Lslowd_%=\n"
        "s_cmp_eq_u32 s84, 0\n"
        "s_cbranch_scc1 Lk21_%=\n"
        "s_branch Le22_%=\n"
        "Ls23_%=:\n"
        "s_mov_b32 s82, 384\n"
        "s_add_u32 s83, s62, 5505024\n"
        "s_mov_b32 s85, 147456\n"
        "s_getpc_b64 s[80:81]\n"
        "s_branch Lslowd_%=\n"
        "s_cmp_eq_u32 s84, 0\n"
        "s_cbranch_scc1 Lk24_%=\n"
        "s_branch Le25_%=\n"
        "Ls26_%=:\n"
        "s_mov_b32 s82, 448\n"
        "s_add_u32 s83, s62, 6422528\n"
        "s_mov_b32 s85, 155648\n"
        "s_getpc_b64 s[80:81]\n"
        "s_branch Lslowd_%=\n"
        "s_cmp_eq_u32 s84, 0\n"
        "s_cbranch_scc1 Lk27_%=\n"
        "s_branch Le28_%=\n"
        "Ls29_%=:\n"
        "s_mov_b32 s82, 512\n"
        "s_add_u32 s83, s62, 7340032\n"
        "s_mov_b32 s85, 131072\n"
        "s_getpc_b64 s[80:81]\n"
        "s_branch Lslowd_%=\n"
        "s_cmp_eq_u32 s84, 0\n"
        "s_cbranch_scc1 Lk30_%=\n"
        "s_branch Le31_%=\n"
        "Ls32_%=:\n"
        "s_mov_b32 s82, 576\n"
        "s_add_u32 s83, s62, 8257536\n"
        "s_mov_b32 s85, 139264\n"
        "s_getpc_b64 s[80:81]\n"
        "s_branch Lslowd_%=\n"
        "s_cmp_eq_u32 s84, 0\n"
        "s_cbranch_scc1 Lk33_%=\n"
        "s_branch Le34_%=\n"
        "Ls35_%=:\n"
        "s_mov_b32 s82, 640\n"
        "s_add_u32 s83, s62, 9175040\n"
        "s_mov_b32 s85, 147456\n"
        "s_getpc_b64 s[80:81]\n"
        "s_branch Lslowd_%=\n"
        "s_cmp_eq_u32 s84, 0\n"
        "s_cbranch_scc1 Lk36_%=\n"
        "s_branch Le37_%=\n"
        "Ls38_%=:\n"
        "s_mov_b32 s82, 704\n"
        "s_add_u32 s83, s62, 10092544\n"
        "s_mov_b32 s85, 155648\n"
        "s_getpc_b64 s[80:81]\n"
        "s_branch Lslowd_%=\n"
        "s_cmp_eq_u32 s84, 0\n"
        "s_cbranch_scc1 Lk39_%=\n"
        "s_branch Le40_%=\n"
        "Ls41_%=:\n"
        "s_mov_b32 s82, 768\n"
        "s_add_u32 s83, s62, 11010048\n"
        "s_mov_b32 s85, 131072\n"
        "s_getpc_b64 s[80:81]\n"
        "s_branch Lslowd_%=\n"
        "s_cmp_eq_u32 s84, 0\n"
        "s_cbranch_scc1 Lk42_%=\n"
        "s_branch Le43_%=\n"
        "Ls44_%=:\n"
        "s_mov_b32 s82, 832\n"
        "s_add_u32 s83, s62, 11927552\n"
        "s_mov_b32 s85, 139264\n"
        "s_getpc_b64 s[80:81]\n"
        "s_branch Lslowd_%=\n"
        "s_cmp_eq_u32 s84, 0\n"
        "s_cbranch_scc1 Lk45_%=\n"
        "s_branch Le46_%=\n"
        "Ls47_%=:\n"
        "s_mov_b32 s82, 896\n"
        "s_add_u32 s83, s62, 12845056\n"
        "s_mov_b32 s85, 147456\n"
        "s_getpc_b64 s[80:81]\n"
        "s_branch Lslowd_%=\n"
        "s_cmp_eq_u32 s84, 0\n"
        "s_cbranch_scc1 Lk48_%=\n"
        "s_branch Le49_%=\n"
        "Ls50_%=:\n"
        "s_mov_b32 s82, 960\n"
        "s_add_u32 s83, s62, 13762560\n"
        "s_mov_b32 s85, 155648\n"
        "s_getpc_b64 s[80:81]\n"
        "s_branch Lslowd_%=\n"
        "s_cmp_eq_u32 s84, 0\n"
        "s_cbranch_scc1 Lk51_%=\n"
        "s_branch Le52_%=\n"
        "Ls53_%=:\n"
        "s_mov_b32 s82, 272\n"
        "s_add_u32 s83, s62, 3899392\n"
        "s_mov_b32 s85, 133120\n"
        "s_getpc_b64 s[80:81]\n"
        "s_branch Lslowd_%=\n"
        "s_cmp_eq_u32 s84, 0\n"
        "s_cbranch_scc1 Lk54_%=\n"
        "s_branch Le55_%=\n"
        "Ls56_%=:\n"
        "s_mov_b32 s82, 336\n"
        "s_add_u32 s83, s62, 4816896\n"
        "s_mov_b32 s85, 141312\n"
        "s_getpc_b64 s[80:81]\n"
        "s_branch Lslowd_%=\n"
        "s_cmp_eq_u32 s84, 0\n"
        "s_cbranch_scc1 Lk57_%=\n"
        "s_branch Le58_%=\n"
        "Ls59_%=:\n"
        "s_mov_b32 s82, 400\n"
        "s_add_u32 s83, s62, 5734400\n"
        "s_mov_b32 s85, 149504\n"
        "s_getpc_b64 s[80:81]\n"
        "s_branch Lslowd_%=\n"
        "s_cmp_eq_u32 s84, 0\n"
        "s_cbranch_scc1 Lk60_%=\n"
        "s_branch Le61_%=\n"
        "Ls62_%=:\n"
        "s_mov_b32 s82, 464\n"
        "s_add_u32 s83, s62, 6651904\n"
        "s_mov_b32 s85, 157696\n"
        "s_getpc_b64 s[80:81]\n"
        "s_branch Lslowd_%=\n"
        "s_cmp_eq_u32 s84, 0\n"
        "s_cbranch_scc1 Lk63_%=\n"
        "s_branch Le64_%=\n"
        "Ls65_%=:\n"
        "s_mov_b32 s82, 528\n"
        "s_add_u32 s83, s62, 7569408\n"
        "s_mov_b32 s85, 133120\n"
        "s_getpc_b64 s[80:81]\n"
        "s_branch Lslowd_%=\n"
        "s_cmp_eq_u32 s84, 0\n"
        "s_cbranch_scc1 Lk66_%=\n"
        "s_branch Le67_%=\n"
        "Ls68_%=:\n"
        "s_mov_b32 s82, 592\n"
        "s_add_u32 s83, s62, 8486912\n"
        "s_mov_b32 s85, 141312\n"
        "s_getpc_b64 s[80:81]\n"
        "s_branch Lslowd_%=\n"
        "s_cmp_eq_u32 s84, 0\n"
        "s_cbranch_scc1 Lk69_%=\n"
        "s_branch Le70_%=\n"
        "Ls71_%=:\n"
        "s_mov_b32 s82, 656\n"
        "s_add_u32 s83, s62, 9404416\n"
        "s_mov_b32 s85, 149504\n"
        "s_getpc_b64 s[80:81]\n"
        "s_branch Lslowd_%=\n"
        "s_cmp_eq_u32 s84, 0\n"
        "s_cbranch_scc1 Lk72_%=\n"
        "s_branch Le73_%=\n"
        "Ls74_%=:\n"
        "s_mov_b32 s82, 720\n"
        "s_add_u32 s83, s62, 10321920\n"
        "s_mov_b32 s85, 157696\n"
        "s_getpc_b64 s[80:81]\n"
        "s_branch Lslowd_%=\n"
        "s_cmp_eq_u32 s84, 0\n"
        "s_cbranch_scc1 Lk75_%=\n"
        "s_branch Le76_%=\n"
        "Ls77_%=:\n"
        "s_mov_b32 s82, 784\n"
        "s_add_u32 s83, s62, 11239424\n"
        "s_mov_b32 s85, 133120\n"
        "s_getpc_b64 s[80:81]\n"
        "s_branch Lslowd_%=\n"
        "s_cmp_eq_u32 s84, 0\n"
        "s_cbranch_scc1 Lk78_%=\n"
        "s_branch Le79_%=\n"
        "Ls80_%=:\n"
        "s_mov_b32 s82, 848\n"
        "s_add_u32 s83, s62, 12156928\n"
        "s_mov_b32 s85, 141312\n"
        "s_getpc_b64 s[80:81]\n"
        "s_branch Lslowd_%=\n"
        "s_cmp_eq_u32 s84, 0\n"
        "s_cbranch_scc1 Lk81_%=\n"
        "s_branch Le82_%=\n"
        "Ls83_%=:\n"
        "s_mov_b32 s82, 912\n"
        "s_add_u32 s83, s62, 13074432\n"
        "s_mov_b32 s85, 149504\n"
        "s_getpc_b64 s[80:81]\n"
        "s_branch Lslowd_%=\n"
        "s_cmp_eq_u32 s84, 0\n"
        "s_cbranch_scc1 Lk84_%=\n"
        "s_branch Le85_%=\n"
        "Ls86_%=:\n"
        "s_mov_b32 s82, 976\n"
        "s_add_u32 s83, s62, 13991936\n"
        "s_mov_b32 s85, 157696\n"
        "s_getpc_b64 s[80:81]\n"
        "s_branch Lslowd_%=\n"
        "s_cmp_eq_u32 s84, 0\n"
        "s_cbranch_scc1 Lk87_%=\n"
        "s_branch Le88_%=\n"
        "Ls89_%=:\n"
        "s_mov_b32 s82, 288\n"
        "s_add_u32 s83, s62, 4128768\n"
        "s_mov_b32 s85, 135168\n"
        "s_getpc_b64 s[80:81]\n"
        "s_branch Lslowd_%=\n"
        "s_cmp_eq_u32 s84, 0\n"
        "s_cbranch_scc1 Lk90_%=\n"
        "s_branch Le91_%=\n"
        "Ls92_%=:\n"
        "s_mov_b32 s82, 352\n"
        "s_add_u32 s83, s62, 5046272\n"
        "s_mov_b32 s85, 143360\n"
        "s_getpc_b64 s[80:81]\n"
        "s_branch Lslowd_%=\n"
        "s_cmp_eq_u32 s84, 0\n"
        "s_cbranch_scc1 Lk93_%=\n"
        "s_branch Le94_%=\n"
        "Ls95_%=:\n"
        "s_mov_b32 s82, 416\n"
        "s_add_u32 s83, s62, 5963776\n"
        "s_mov_b32 s85, 151552\n"
        "s_getpc_b64 s[80:81]\n"
        "s_branch Lslowd_%=\n"
        "s_cmp_eq_u32 s84, 0\n"
        "s_cbranch_scc1 Lk96_%=\n"
        "s_branch Le97_%=\n"
        "Ls98_%=:\n"
        "s_mov_b32 s82, 480\n"
        "s_add_u32 s83, s62, 6881280\n"
        "s_mov_b32 s85, 159744\n"
        "s_getpc_b64 s[80:81]\n"
        "s_branch Lslowd_%=\n"
        "s_cmp_eq_u32 s84, 0\n"
        "s_cbranch_scc1 Lk99_%=\n"
        "s_branch Le100_%=\n"
        "Ls101_%=:\n"
        "s_mov_b32 s82, 544\n"
        "s_add_u32 s83, s62, 7798784\n"
        "s_mov_b32 s85, 135168\n"
        "s_getpc_b64 s[80:81]\n"
        "s_branch Lslowd_%=\n"
        "s_cmp_eq_u32 s84, 0\n"
        "s_cbranch_scc1 Lk102_%=\n"
        "s_branch Le103_%=\n"
        "Ls104_%=:\n"
        "s_mov_b32 s82, 608\n"
        "s_add_u32 s83, s62, 8716288\n"
        "s_mov_b32 s85, 143360\n"
        "s_getpc_b64 s[80:81]\n"
        "s_branch Lslowd_%=\n"
        "s_cmp_eq_u32 s84, 0\n"
        "s_cbranch_scc1 Lk105_%=\n"
        "s_branch Le106_%=\n"
        "Ls107_%=:\n"
        "s_mov_b32 s82, 672\n"
        "s_add_u32 s83, s62, 9633792\n"
        "s_mov_b32 s85, 151552\n"
        "s_getpc_b64 s[80:81]\n"
        "s_branch Lslowd_%=\n"
        "s_cmp_eq_u32 s84, 0\n"
        "s_cbranch_scc1 Lk108_%=\n"
        "s_branch Le109_%=\n"
        "Ls110_%=:\n"
        "s_mov_b32 s82, 736\n"
        "s_add_u32 s83, s62, 10551296\n"
        "s_mov_b32 s85, 159744\n"
        "s_getpc_b64 s[80:81]\n"
        "s_branch Lslowd_%=\n"
        "s_cmp_eq_u32 s84, 0\n"
        "s_cbranch_scc1 Lk111_%=\n"
        "s_branch Le112_%=\n"
        "Ls113_%=:\n"
        "s_mov_b32 s82, 800\n"
        "s_add_u32 s83, s62, 11468800\n"
        "s_mov_b32 s85, 135168\n"
        "s_getpc_b64 s[80:81]\n"
        "s_branch Lslowd_%=\n"
        "s_cmp_eq_u32 s84, 0\n"
        "s_cbranch_scc1 Lk114_%=\n"
        "s_branch Le115_%=\n"
        "Ls116_%=:\n"
        "s_mov_b32 s82, 864\n"
        "s_add_u32 s83, s62, 12386304\n"
        "s_mov_b32 s85, 143360\n"
        "s_getpc_b64 s[80:81]\n"
        "s_branch Lslowd_%=\n"
        "s_cmp_eq_u32 s84, 0\n"
        "s_cbranch_scc1 Lk117_%=\n"
        "s_branch Le118_%=\n"
        "Ls119_%=:\n"
        "s_mov_b32 s82, 928\n"
        "s_add_u32 s83, s62, 13303808\n"
        "s_mov_b32 s85, 151552\n"
        "s_getpc_b64 s[80:81]\n"
        "s_branch Lslowd_%=\n"
        "s_cmp_eq_u32 s84, 0\n"
        "s_cbranch_scc1 Lk120_%=\n"
        "s_branch Le121_%=\n"
        "Ls122_%=:\n"
        "s_mov_b32 s82, 992\n"
        "s_add_u32 s83, s62, 14221312\n"
        "s_mov_b32 s85, 159744\n"
        "s_getpc_b64 s[80:81]\n"
        "s_branch Lslowd_%=\n"
        "s_cmp_eq_u32 s84, 0\n"
        "s_cbranch_scc1 Lk123_%=\n"
        "s_branch Le124_%=\n"
        "Ls125_%=:\n"
        "s_mov_b32 s82, 304\n"
        "s_add_u32 s83, s62, 4358144\n"
        "s_mov_b32 s85, 137216\n"
        "s_getpc_b64 s[80:81]\n"
        "s_branch Lslowd_%=\n"
        "s_cmp_eq_u32 s84, 0\n"
        "s_cbranch_scc1 Lk126_%=\n"
        "s_branch Le127_%=\n"
        "Ls128_%=:\n"
        "s_mov_b32 s82, 368\n"
        "s_add_u32 s83, s62, 5275648\n"
        "s_mov_b32 s85, 145408\n"
        "s_getpc_b64 s[80:81]\n"
        "s_branch Lslowd_%=\n"
        "s_cmp_eq_u32 s84, 0\n"
        "s_cbranch_scc1 Lk129_%=\n"
        "s_branch Le130_%=\n"
        "Ls131_%=:\n"
        "s_mov_b32 s82, 432\n"
        "s_add_u32 s83, s62, 6193152\n"
        "s_mov_b32 s85, 153600\n"
        "s_getpc_b64 s[80:81]\n"
        "s_branch Lslowd_%=\n"
        "s_cmp_eq_u32 s84, 0\n"
        "s_cbranch_scc1 Lk132_%=\n"
        "s_branch Le133_%=\n"
        "Ls134_%=:\n"
        "s_mov_b32 s82, 496\n"
        "s_add_u32 s83, s62, 7110656\n"
        "s_mov_b32 s85, 161792\n"
        "s_getpc_b64 s[80:81]\n"
        "s_branch Lslowd_%=\n"
        "s_cmp_eq_u32 s84, 0\n"
        "s_cbranch_scc1 Lk135_%=\n"
        "s_branch Le136_%=\n"
        "Ls137_%=:\n"
        "s_mov_b32 s82, 560\n"
        "s_add_u32 s83, s62, 8028160\n"
        "s_mov_b32 s85, 137216\n"
        "s_getpc_b64 s[80:81]\n"
        "s_branch Lslowd_%=\n"
        "s_cmp_eq_u32 s84, 0\n"
        "s_cbranch_scc1 Lk138_%=\n"
        "s_branch Le139_%=\n"
        "Ls140_%=:\n"
        "s_mov_b32 s82, 624\n"
        "s_add_u32 s83, s62, 8945664\n"
        "s_mov_b32 s85, 145408\n"
        "s_getpc_b64 s[80:81]\n"
        "s_branch Lslowd_%=\n"
        "s_cmp_eq_u32 s84, 0\n"
        "s_cbranch_scc1 Lk141_%=\n"
        "s_branch Le142_%=\n"
        "Ls143_%=:\n"
        "s_mov_b32 s82, 688\n"
        "s_add_u32 s83, s62, 9863168\n"
        "s_mov_b32 s85, 153600\n"
        "s_getpc_b64 s[80:81]\n"
        "s_branch Lslowd_%=\n"
        "s_cmp_eq_u32 s84, 0\n"
        "s_cbranch_scc1 Lk144_%=\n"
        "s_branch Le145_%=\n"
        "Ls146_%=:\n"
        "s_mov_b32 s82, 752\n"
        "s_add_u32 s83, s62, 10780672\n"
        "s_mov_b32 s85, 161792\n"
        "s_getpc_b64 s[80:81]\n"
        "s_branch Lslowd_%=\n"
        "s_cmp_eq_u32 s84, 0\n"
        "s_cbranch_scc1 Lk147_%=\n"
        "s_branch Le148_%=\n"
        "Ls149_%=:\n"
        "s_mov_b32 s82, 816\n"
        "s_add_u32 s83, s62, 11698176\n"
        "s_mov_b32 s85, 137216\n"
        "s_getpc_b64 s[80:81]\n"
        "s_branch Lslowd_%=\n"
        "s_cmp_eq_u32 s84, 0\n"
        "s_cbranch_scc1 Lk150_%=\n"
        "s_branch Le151_%=\n"
        "Ls152_%=:\n"
        "s_mov_b32 s82, 880\n"
        "s_add_u32 s83, s62, 12615680\n"
        "s_mov_b32 s85, 145408\n"
        "s_getpc_b64 s[80:81]\n"
        "s_branch Lslowd_%=\n"
        "s_cmp_eq_u32 s84, 0\n"
        "s_cbranch_scc1 Lk153_%=\n"
        "s_branch Le154_%=\n"
        "Ls155_%=:\n"
        "s_mov_b32 s82, 944\n"
        "s_add_u32 s83, s62, 13533184\n"
        "s_mov_b32 s85, 153600\n"
        "s_getpc_b64 s[80:81]\n"
        "s_branch Lslowd_%=\n"
        "s_cmp_eq_u32 s84, 0\n"
        "s_cbranch_scc1 Lk156_%=\n"
        "s_branch Le157_%=\n"
        "Ls158_%=:\n"
        "s_mov_b32 s82, 1008\n"
        "s_add_u32 s83, s62, 14450688\n"
        "s_mov_b32 s85, 161792\n"
        "s_getpc_b64 s[80:81]\n"
        "s_branch Lslowd_%=\n"
        "s_cmp_eq_u32 s84, 0\n"
        "s_cbranch_scc1 Lk159_%=\n"
        "s_branch Le160_%=\n"
        "Lslowd_%=:\n"
        "s_add_u32 s80, s80, 4\n"
        "s_addc_u32 s81, s81, 0\n"
        "s_mov_b32 s84, 64\n"
        "Lsdl_%=:\n"
        "buffer_load_dwordx2 v[238:239], v104, s[28:31], s82 offen sc1\n"
        "s_waitcnt vmcnt(0)\n"
        "v_cmp_ne_u32 vcc, s64, v238\n"
        "v_cmp_ne_u32 s[78:79], s65, v239\n"
        "s_or_b64 vcc, vcc, s[78:79]\n"
        "s_cbranch_scc0 Lsdok_%=\n"
        "s_sleep 1\n"
        "s_sub_u32 s84, s84, 1\n"
        "s_cmp_lg_u32 s84, 0\n"
        "s_cbranch_scc1 Lsdl_%=\n"
        "s_mov_b32 s82, 0\n"
        "Lsdg_%=:\n"
        "v_mov_b32 v238, 0\n"
        "v_mov_b32 v239, 0\n"
        "v_mov_b32 v240, 0\n"
        "v_mov_b32 v241, 0\n"
        "s_mov_b32 s84, 0\n"
        "Lsdq_%=:\n"
        "buffer_load_dword v242, v246, s[20:23], s83 offen nt\n"
        "buffer_load_dword v243, v246, s[20:23], s83 offen offset:16 nt\n"
        "buffer_load_dword v244, v247, s[20:23], s83 offen nt\n"
        "buffer_load_dword v245, v248, s[20:23], s83 offen nt\n"
        "s_add_u32 s83, s83, 14336\n"
        "s_waitcnt vmcnt(0)\n"
        "v_lshl_or_b32 v238, v242, s84, v238\n"
        "v_lshl_or_b32 v239, v243, s84, v239\n"
        "v_lshl_or_b32 v240, v244, s84, v240\n"
        "v_lshl_or_b32 v241, v245, s84, v241\n"
        "s_add_u32 s84, s84, 4\n"
        "s_cmp_lg_u32 s84, 32\n"
        "s_cbranch_scc1 Lsdq_%=\n"
        "v_add_u32 v242, s85, v103\n"
        "ds_write_b128 v242, v[238:241]\n"
        "s_waitcnt lgkmcnt(0)\n"
        "s_add_u32 s85, s85, 1024\n"
        "s_add_u32 s82, s82, 1\n"
        "s_cmp_lt_u32 s82, 2\n"
        "s_cbranch_scc1 Lsdg_%=\n"
        "s_mov_b32 s84, 1\n"
        "s_setpc_b64 s[80:81]\n"
        "Lsdok_%=:\n"
        "s_mov_b32 s84, 0\n"
        "s_setpc_b64 s[80:81]\n"
        : [tid] "+v"(tid)
        : [ka] "s"(ka), [bid] "s"(blockIdx.x), [ldsb] "s"(ldsb)
        : "v0", "v1", "v2", "v3", "v4", "v5", "v6", "v7", "v8", "v9", "v10", "v11", "v12", "v13", "v14", "v15", "v16", "v17", "v18", "v19", "v20", "v21", "v22", "v23", "v24", "v25", "v26", "v27", "v28", "v29", "v30", "v31", "v32", "v33", "v34", "v35", "v36", "v37", "v38", "v39", "v40", "v41", "v42", "v43", "v44", "v45", "v46", "v47", "v48", "v49", "v50", "v51", "v52", "v53", "v54", "v55", "v56", "v57", "v58", "v59", "v60", "v61", "v62", "v63", "v64", "v65", "v66", "v67", "v68", "v69", "v70", "v71", "v72", "v73", "v74", "v75", "v76", "v77", "v78", "v79", "v80", "v81", "v82", "v83", "v84", "v85", "v86", "v87", "v88", "v89", "v90", "v91", "v92", "v93", "v94", "v95", "v96", "v97", "v98", "v99", "v100", "v101", "v102", "v103", "v104", "v105", "v106", "v107", "v108", "v109", "v110", "v111", "v112", "v113", "v114", "v115", "v116", "v117", "v118", "v119", "v120", "v121", "v122", "v123", "v124", "v125", "v126", "v127", "v128", "v129", "v130", "v131", "v132", "v133", "v134", "v135", "v136", "v137", "v138", "v139", "v140", "v141", "v142", "v143", "v144", "v145", "v146", "v147", "v148", "v149", "v150", "v151", "v152", "v153", "v154", "v155", "v156", "v157", "v158", "v159", "v160", "v161", "v162", "v163", "v164", "v165", "v166", "v167", "v168", "v169", "v170", "v171", "v172", "v173", "v174", "v175", "v176", "v177", "v178", "v179", "v180", "v181", "v182", "v183", "v184", "v185", "v186", "v187", "v188", "v189", "v190", "v191", "v192", "v193", "v194", "v195", "v196", "v197", "v198", "v199", "v200", "v201", "v202", "v203", "v204", "v205", "v206", "v207", "v208", "v209", "v210", "v211", "v212", "v213", "v214", "v215", "v216", "v217", "v218", "v219", "v220", "v221", "v222", "v223", "v224", "v225", "v226", "v227", "v228", "v229", "v230", "v231", "v232", "v233", "v234", "v235", "v236", "v237", "v238", "v239", "v240", "v241", "v242", "v243", "v244", "v245", "v246", "v247", "v248", "v249", "v250", "v251", "v252", "v253", "v254", "s4", "s5", "s6", "s7", "s8", "s9", "s10", "s11", "s12", "s13", "s14", "s15", "s16", "s17", "s18", "s19", "s20", "s21", "s22", "s23", "s24", "s25", "s26", "s27", "s28", "s29", "s30", "s31", "s32", "s33", "s34", "s35", "s36", "s37", "s38", "s39", "s40", "s41", "s42", "s43", "s44", "s45", "s46", "s47", "s48", "s49", "s50", "s51", "s52", "s53", "s54", "s55", "s56", "s57", "s58", "s59", "s60", "s61", "s62", "s63", "s64", "s65", "s66", "s67", "s68", "s69", "s70", "s71", "s72", "s73", "s74", "s75", "s76", "s77", "s78", "s79", "s80", "s81", "s82", "s83", "s84", "s85", "s86", "s87", "s88", "s89", "s90", "s91", "s92", "s93", "s94", "s95", "s96", "s97", "s98", "s99", "s100", "s101", "vcc", "scc", "memory"
    );
}

extern "C" void kernel_launch(void* const* d_in, const int* in_sizes, int n_in,
                              void* d_out, int out_size, void* d_ws, size_t ws_size,
                              hipStream_t stream) {
    k_nf4<<<256, 512, 0, stream>>>((const unsigned short*)d_in[0], (const int*)d_in[1], (const unsigned short*)d_in[2],
                                   (const unsigned short*)d_in[3], (unsigned short*)d_out, (char*)d_ws);
}
```
